```python
import torch

def _softmax(x, axis=-1):
    return torch.softmax(x, dim=axis)
def _gelu(x):
    return torch.nn.functional.gelu(x, approximate="tanh")
def _silu(x):
    return torch.nn.functional.silu(x)
def _sigmoid(x):
    return torch.sigmoid(x)
def _layernorm(x, axis=-1, eps=1e-5):
    m = x.mean(dim=axis, keepdim=True)
    v = x.var(dim=axis, keepdim=True, unbiased=False)
    return (x - m) / torch.sqrt(v + eps)
def _scatter_add(tgt, idx, src, axis=0):
    return tgt.clone().index_add_(axis, idx, src)
def _segment_sum(src, seg, n):
    out = torch.zeros((n,) + src.shape[1:], dtype=src.dtype)
    return out.index_add_(0, seg, src)
def _pad(x, pw):
    flat = tuple(v for pair in reversed(pw) for v in pair)
    return torch.nn.functional.pad(x, flat)

kernel_name = 'parallel_sum__linear_kernel_hedgehog__dense_activation_gelu__prompt'

def setup_inputs():

    _w = (lambda a: (lambda t: t.to(torch.bfloat16) if t.is_floating_point() else t)(torch.as_tensor(a)))
    def _A_setup():
        B, S, D, H = 1, 2048, 512, 32
        Dh = D // H
        return {
            "x": _w((lambda *s: torch.randn(*s, dtype=torch.bfloat16) * 0.05)(B, S, D)),
            "wqkv": _w((lambda *s: torch.randn(*s, dtype=torch.bfloat16) * 0.2)(D, 3 * D)),
            "w_phi": _w((lambda *s: torch.randn(*s, dtype=torch.bfloat16) * 0.2)(H, Dh, Dh) * (Dh ** -0.5)),
            "wo": _w((lambda *s: torch.randn(*s, dtype=torch.bfloat16) * 0.05)(D, D)),
        }
    _A_in = _A_setup()

    def _B_setup():
        B, S, D, F = 1, 2048, 512, 1792
        return {
            "x": _w((lambda *s: torch.randn(*s, dtype=torch.bfloat16) * 0.05)(B, S, D)),
            "w1": _w((lambda *s: torch.randn(*s, dtype=torch.bfloat16) * 0.8)(D, F) * (D ** -0.5)),
            "b1": _w((lambda *s: torch.randn(*s, dtype=torch.bfloat16) * 12.8)(F) * 0.01),
            "w2": _w((lambda *s: torch.randn(*s, dtype=torch.bfloat16) * 0.4)(F, D) * (F ** -0.5)),
            "b2": _w((lambda *s: torch.randn(*s, dtype=torch.bfloat16) * 6.4)(D) * 0.01),
        }
    _B_in = _B_setup()

    return {
        "A_x": _A_in["x"],
        "A_wqkv": _A_in["wqkv"],
        "A_w_phi": _A_in["w_phi"],
        "A_wo": _A_in["wo"],
        "B_x": _B_in["x"],
        "B_w1": _B_in["w1"],
        "B_b1": _B_in["b1"],
        "B_w2": _B_in["w2"],
        "B_b2": _B_in["b2"],
    }

def reference(A_x, A_wqkv, A_w_phi, A_wo, B_x, B_w1, B_b1, B_w2, B_b2):
    def _A_ref(x, wqkv, w_phi, wo):
        B, S, D = x.shape
        H = 32; Dh = D // H
        qkv = (lambda x, *d: x.permute(*d))((x @ wqkv).reshape(B, S, 3, H, Dh), 2, 0, 3, 1, 4)
        q, k, v = qkv[0], qkv[1], qkv[2]
        pq = torch.einsum("bhsd,hdf->bhsf", q, w_phi)
        pk = torch.einsum("bhsd,hdf->bhsf", k, w_phi)
        phi_q = torch.concatenate([_softmax(pq, axis=-1), _softmax(-pq, axis=-1)], axis=-1)
        phi_k = torch.concatenate([_softmax(pk, axis=-1), _softmax(-pk, axis=-1)], axis=-1)
        kv = torch.einsum("bhsf,bhsd->bhfd", phi_k, v)
        z  = torch.einsum("bhsf->bhf", phi_k)
        num = torch.einsum("bhqf,bhfd->bhqd", phi_q, kv)
        den = torch.einsum("bhqf,bhf->bhq", phi_q, z)[..., None] + 1e-6
        o = num / den
        return (lambda x, *d: x.permute(*d))(o, 0, 2, 1, 3).reshape(B, S, D) @ wo

    def _B_ref(x, w1, b1, w2, b2):
        h = torch.einsum("bsd,df->bsf", x, w1) + b1
        h = _gelu(h)
        return torch.einsum("bsf,fd->bsd", h, w2) + b2

    x = A_x
    xn = _layernorm(x, axis=-1)
    return x + _A_ref(xn, A_wqkv, A_w_phi, A_wo) + _B_ref(xn, B_w1, B_b1, B_w2, B_b2)
```

```cpp
#include <hip/hip_runtime.h>
#include <hip/hip_bf16.h>
#include <stdint.h>

typedef unsigned short u16;
__device__ __forceinline__ float bf2f(u16 b){ return __uint_as_float(((unsigned)b)<<16); }
__device__ __forceinline__ u16 f2bf(float f){ unsigned u=__float_as_uint(f); u += 0x7fffu + ((u>>16)&1u); return (u16)(u>>16); }

constexpr int S=2048, D=512, H=32, DH=16, F=1792;

__global__ void k_ln(const u16* x, float* xn){
  int r=blockIdx.x; int t=threadIdx.x;
  float v[8]; float s=0;
  for(int j=0;j<8;j++){ v[j]=bf2f(x[r*D+t*8+j]); s+=v[j]; }
  for(int m=32;m>=1;m>>=1) s+=__shfl_xor(s,m);
  float mean=s/D; float q=0;
  for(int j=0;j<8;j++){ float d=v[j]-mean; q+=d*d; }
  for(int m=32;m>=1;m>>=1) q+=__shfl_xor(q,m);
  float rs=1.0f/sqrtf(q/D+1e-5f);
  for(int j=0;j<8;j++) xn[r*D+t*8+j]=(v[j]-mean)*rs;
}
__global__ void k_gemm(const float* A, const u16* B, float* C, int M, int N, int K){
  __shared__ float As[16][17]; __shared__ float Bs[16][17];
  int tx=threadIdx.x, ty=threadIdx.y; int row=blockIdx.y*16+ty, col=blockIdx.x*16+tx;
  float acc=0;
  for(int k0=0;k0<K;k0+=16){
    As[ty][tx]=A[(size_t)row*K+k0+tx]; Bs[ty][tx]=bf2f(B[(size_t)(k0+ty)*N+col]);
    __syncthreads();
    for(int k=0;k<16;k++) acc+=As[ty][k]*Bs[k][tx];
    __syncthreads();
  }
  C[(size_t)row*N+col]=acc;
}
__global__ void k_gelu(float* h, const u16* b1){
  int i=blockIdx.x*256+threadIdx.x; int c=i%F; float x=h[i]+bf2f(b1[c]);
  float u=0.7978845608028654f*(x+0.044715f*x*x*x);
  h[i]=0.5f*x*(1.0f+tanhf(u));
}
__global__ void k_phi(const float* qkv, const u16* wphi, float* phiq, float* phik){
  int i=blockIdx.x*256+threadIdx.x;
  int which=i&1; int h=(i>>1)%H; int s=(i>>1)/H;
  const float* q=qkv+(size_t)s*3*D+which*D+h*DH;
  float p[16]; float mx=-1e30f, mn=1e30f;
  for(int f=0;f<16;f++){ float a=0; for(int d=0;d<16;d++) a+=q[d]*bf2f(wphi[h*256+d*16+f]); p[f]=a; mx=fmaxf(mx,a); mn=fminf(mn,a);}
  float sp=0,sn=0; float ep[16],en[16];
  for(int f=0;f<16;f++){ ep[f]=__expf(p[f]-mx); en[f]=__expf(mn-p[f]); sp+=ep[f]; sn+=en[f]; }
  float* o=(which?phik:phiq)+((size_t)s*H+h)*32;
  for(int f=0;f<16;f++){ o[f]=ep[f]/sp; o[16+f]=en[f]/sn; }
}
__global__ void k_kv(const float* phik, const float* qkv, float* kv){
  int h=blockIdx.x; int t=threadIdx.x; if(t>=32*17) return; int f=t/17, d=t%17;
  float a=0;
  for(int s=0;s<S;s++){ float p=phik[((size_t)s*H+h)*32+f]; float v = d<16? qkv[(size_t)s*3*D+2*D+h*DH+d] : 1.0f; a+=p*v; }
  kv[h*544+t]=a;
}
__global__ void k_o(const float* phiq, const float* kv, float* o){
  int i=blockIdx.x*256+threadIdx.x;
  int s=i/D, c=i%D; int h=c/16, d=c%16;
  const float* p=phiq+((size_t)s*H+h)*32; float num=0, den=0;
  for(int f=0;f<32;f++){ num+=p[f]*kv[h*544+f*17+d]; den+=p[f]*kv[h*544+f*17+16]; }
  o[i]=num/(den+1e-6f);
}
__global__ void k_fin(const u16* x, const float* a, const float* y, const u16* b2, u16* out){
  int i=blockIdx.x*256+threadIdx.x; int c=i%D;
  out[i]=f2bf(bf2f(x[i])+a[i]+y[i]+bf2f(b2[c]));
}

extern "C" void kernel_launch(void* const* d_in, const int* in_sizes, int n_in,
                              void* d_out, int out_size, void* d_ws, size_t ws_size,
                              hipStream_t stream) {
  const u16* x=(const u16*)d_in[0]; const u16* wqkv=(const u16*)d_in[1]; const u16* wphi=(const u16*)d_in[2]; const u16* wo=(const u16*)d_in[3];
  const u16* w1=(const u16*)d_in[5]; const u16* b1=(const u16*)d_in[6]; const u16* w2=(const u16*)d_in[7]; const u16* b2=(const u16*)d_in[8];
  float* ws=(float*)d_ws;
  float* xn=ws; float* qkv=xn+S*D; float* h=qkv+S*3*D; float* phiq=h+S*F; float* phik=phiq+S*H*32; float* kv=phik+S*H*32; float* o=kv+32*544; float* a=o+S*D; float* y=a+S*D;
  k_ln<<<S,64,0,stream>>>(x,xn);
  k_gemm<<<dim3(3*D/16,S/16),dim3(16,16),0,stream>>>(xn,wqkv,qkv,S,3*D,D);
  k_gemm<<<dim3(F/16,S/16),dim3(16,16),0,stream>>>(xn,w1,h,S,F,D);
  k_gelu<<<S*F/256,256,0,stream>>>(h,b1);
  k_phi<<<S*H*2/256,256,0,stream>>>(qkv,wphi,phiq,phik);
  k_kv<<<H,576,0,stream>>>(phik,qkv,kv);
  k_o<<<S*D/256,256,0,stream>>>(phiq,kv,o);
  k_gemm<<<dim3(D/16,S/16),dim3(16,16),0,stream>>>(o,wo,a,S,D,D);
  k_gemm<<<dim3(D/16,S/16),dim3(16,16),0,stream>>>(h,w2,y,S,D,F);
  k_fin<<<S*D/256,256,0,stream>>>(x,a,y,b2,(u16*)d_out);
}
```

```cpp
#include <hip/hip_runtime.h>
#include <stdint.h>
#include <type_traits>

namespace hh {

#define LAS __attribute__((address_space(3)))
#define GAS __attribute__((address_space(1)))
typedef unsigned v4u __attribute__((ext_vector_type(4)));
typedef unsigned v2u __attribute__((ext_vector_type(2)));
typedef float f4 __attribute__((ext_vector_type(4)));
typedef float f2 __attribute__((ext_vector_type(2)));
typedef short s4 __attribute__((ext_vector_type(4)));
typedef __bf16 b8 __attribute__((ext_vector_type(8)));
typedef __bf16 b2 __attribute__((ext_vector_type(2)));

constexpr unsigned WS_EPOCH = 0;
constexpr unsigned WS_FLAG = 256;
constexpr unsigned WS_STATS = 8192;
constexpr unsigned WS_CS = WS_STATS + 2048 * 16;
constexpr unsigned PART_BYTES = 17408;
constexpr unsigned WS_P = WS_CS + 3328 * 32;
constexpr unsigned WS_R = WS_P + 8 * 16 * PART_BYTES;
constexpr unsigned WS_H = (WS_R + 8 * PART_BYTES + 4095u) & ~4095u;
constexpr unsigned WS_DBG = WS_H + 128 * 72 * 1024;
constexpr unsigned WS_END = WS_DBG + 256 * 1024 * 8;

constexpr int NSLOT = 7;
constexpr unsigned L_RING = 0;
constexpr unsigned L_SCR = NSLOT * 16384;
constexpr unsigned SCRW = 5248;
constexpr unsigned L_CS = L_SCR + 8 * SCRW;
constexpr unsigned L_ST = L_CS + 1024;
constexpr unsigned L_B1 = L_ST + 1024;
constexpr unsigned L_WPHI = L_B1 + 512;
constexpr unsigned L_B2 = L_WPHI + 2048;
constexpr unsigned L_END = L_B2 + 256;
static_assert(L_END <= 163840, "LDS");

constexpr int NITEM = 39, NSTEP = 32, DH = 4;
constexpr int ST_E1 = 8, ST_E2 = 13, ST_F0 = 14;
constexpr int ST_G1_ISSUE = 4, ST_G1_CHECK = 6;
constexpr int ST_PUBA = 11;
constexpr int ST_POLLA_ISSUE = 11, ST_POLLA_CHECK = 13;
constexpr int ST_R_ISSUE = 11, ST_R_CHECK = 13;
constexpr int ST_PUBB = 16;
constexpr int ST_POLLB_ISSUE = 16, ST_POLLB_CHECK = 18;
constexpr int ST_RG_ISSUE = 15, ST_RG_CHECK = 17;
constexpr int ST_O = 18;
constexpr int ST_PUBO = 21;
constexpr int ST_POLLO_ISSUE = 22, ST_POLLO_CHECK = 24;

struct Sched {
  int slot[NITEM], issue[NITEM], first[NITEM], last[NITEM];
  int cum[NSTEP + 1];
  int pos_item[2][NITEM];
  int pos_h[2][18];
  int h_issue[18];
  int pos_g1[2];
};
constexpr Sched make_sched() {
  Sched s{};
  for (int i = 0; i < NITEM; ++i) {
    if (i < 16) { s.first[i] = i >> 1; s.last[i] = i >> 1; }
    else if (i == 16) { s.first[i] = 9; s.last[i] = 12; }
    else if (i < 21) { s.first[i] = 9 + (i - 17); s.last[i] = 9 + (i - 17); }
    else { s.first[i] = ST_F0 + (i - 21); s.last[i] = ST_F0 + (i - 21); }
  }
  for (int c = 0; c < 18; ++c) s.h_issue[c] = c < DH ? ST_E2 : ST_F0 + c - DH;
  int n = 4;
  int next = 0;
  int owner[NSLOT] = {};
  for (int k = 0; k < NSLOT; ++k) { s.slot[next] = k; s.issue[next] = -1; owner[k] = next; n += 2; s.pos_item[0][next] = s.pos_item[1][next] = n - 1; ++next; }
  for (int st = 0; st < NSTEP; ++st) {
    s.cum[st] = n;
    const int first_new = next;
    for (int rep = 0; rep < NSLOT; ++rep) {
      int best = -1;
      for (int k = 0; k < NSLOT; ++k)
        if (owner[k] >= 0 && s.last[owner[k]] < st && (best < 0 || owner[k] < owner[best])) best = k;
      if (best < 0) break;
      if (next < NITEM) { s.slot[next] = best; s.issue[next] = st; owner[best] = next; ++next; }
      else owner[best] = -1;
    }
    int nreg = 0;
    for (int c = 0; c < 18; ++c) if (s.h_issue[c] == st) nreg += 2;
    if (st == ST_G1_ISSUE) nreg += 2;
    const int nring = 2 * (next - first_new);
    for (int o = 0; o < 2; ++o) {
      int p = n + (o == 0 ? 0 : nreg);
      for (int i = first_new; i < next; ++i) { p += 2; s.pos_item[o][i] = p - 1; }
      p = n + (o == 0 ? nring : 0);
      for (int c = 0; c < 18; ++c) if (s.h_issue[c] == st) { p += 2; s.pos_h[o][c] = p - 1; }
      if (st == ST_G1_ISSUE) { p += 2; s.pos_g1[o] = p - 1; }
    }
    n += nring + nreg;
  }
  s.cum[NSTEP] = n;
  return s;
}
constexpr Sched SC = make_sched();
constexpr int imin(int a, int b) { return a < b ? a : b; }
constexpr int g1_wait() {
  const int need = SC.pos_g1[0] > SC.pos_g1[1] ? SC.pos_g1[0] : SC.pos_g1[1];
  return SC.cum[ST_G1_CHECK] - 1 - need;
}
constexpr int top_wait(int st) {
  int need = -1;
  for (int o = 0; o < 2; ++o) {
    for (int i = 0; i < NITEM; ++i) if (SC.first[i] == st && SC.pos_item[o][i] > need) need = SC.pos_item[o][i];
    if (st >= ST_F0 && SC.pos_h[o][st - ST_F0] > need) need = SC.pos_h[o][st - ST_F0];
    if (st == ST_G1_CHECK && SC.pos_g1[o] > need) need = SC.pos_g1[o];
  }
  int n = need < 0 ? 63 : SC.cum[st] - 1 - need;
  if (st == ST_PUBA) n = imin(n, SC.cum[st] - SC.cum[ST_E1 + 1]);
  if (st == ST_PUBB) n = imin(n, SC.cum[st] - SC.cum[ST_E2 + 1]);
  if (st == ST_PUBO) n = imin(n, SC.cum[st] - SC.cum[ST_O + 1]);
  return n > 63 ? 63 : n;
}
constexpr int extra_wait(int a, int b) { int n = SC.cum[b] - SC.cum[a + 1]; return n > 63 ? 63 : n; }

__host__ __device__ constexpr int unit_pos(int q, int off) { return off >= 2 && off <= 5 ? 4 * q + (off - 2) : 16 + 3 * q + (off == 6 ? 2 : off); }
__host__ __device__ constexpr int pos_unit(int pos) { return pos < 16 ? 7 * (pos >> 2) + 2 + (pos & 3) : 7 * ((pos - 16) / 3) + (((pos - 16) % 3) == 2 ? 6 : (pos - 16) % 3); }

#define DI __device__ __forceinline__
DI void glds16(const void* sbase, unsigned voff, unsigned lds) {
  asm volatile("s_mov_b32 m0, %2\n\ts_nop 0\n\tglobal_load_lds_dwordx4 %0, %1" ::"v"(voff), "s"(sbase), "s"(lds) : "memory");
}
DI void glds4(const void* sbase, unsigned voff, unsigned lds) {
  asm volatile("s_mov_b32 m0, %2\n\ts_nop 0\n\tglobal_load_lds_dword %0, %1" ::"v"(voff), "s"(sbase), "s"(lds) : "memory");
}
DI void glds4v(const void* vaddr, unsigned lds) {
  asm volatile("s_mov_b32 m0, %1\n\ts_nop 0\n\tglobal_load_lds_dword %0, off" ::"v"(vaddr), "s"(lds) : "memory");
}
DI void gld16(v4u& d, const void* sbase, unsigned voff) {
  asm volatile("global_load_dwordx4 %0, %1, %2" : "=v"(d) : "v"(voff), "s"(sbase) : "memory");
}
DI void gld16_sc1(v4u& d, const void* sbase, unsigned voff) {
  asm volatile("global_load_dwordx4 %0, %1, %2 sc1" : "=v"(d) : "v"(voff), "s"(sbase) : "memory");
}
template <int OFF> DI void gld16_sc1_off(v4u& d, const void* sbase, unsigned voff) {
  asm volatile("global_load_dwordx4 %0, %1, %2 offset:%3 sc1" : "=v"(d) : "v"(voff), "s"(sbase), "n"(OFF) : "memory");
}
DI void gld4_sc1(unsigned& d, const void* sbase, unsigned voff) {
  asm volatile("global_load_dword %0, %1, %2 sc1" : "=v"(d) : "v"(voff), "s"(sbase) : "memory");
}
template <int N> DI void wait_vm() { asm volatile("s_waitcnt vmcnt(%0)" ::"n"(N) : "memory"); }
template <int N> DI void wait_top() { asm volatile("s_waitcnt vmcnt(%0) lgkmcnt(0)" ::"n"(N) : "memory"); }
DI void bar() { asm volatile("" ::: "memory"); __builtin_amdgcn_s_barrier(); asm volatile("" ::: "memory"); }
DI void pin(v4u& a) { asm volatile("" : "+v"(a)); }
DI void pin(unsigned& a) { asm volatile("" : "+v"(a)); }

DI unsigned pk(float a, float b) { b2 r = {(__bf16)a, (__bf16)b}; return __builtin_bit_cast(unsigned, r); }
DI float bflo(unsigned u) { return __builtin_bit_cast(float, u << 16); }
DI float bfhi(unsigned u) { return __builtin_bit_cast(float, u & 0xffff0000u); }
DI unsigned fbits(float f) { return __builtin_bit_cast(unsigned, f); }
DI float bitsf(unsigned u) { return __builtin_bit_cast(float, u); }
DI f4 mfma32(v4u a, v4u b, f4 c) { return __builtin_amdgcn_mfma_f32_16x16x32_bf16(__builtin_bit_cast(b8, a), __builtin_bit_cast(b8, b), c, 0, 0, 0); }
DI f4 mfma16(v2u a, v2u b, f4 c) { return __builtin_amdgcn_mfma_f32_16x16x16bf16_1k(__builtin_bit_cast(s4, a), __builtin_bit_cast(s4, b), c, 0, 0, 0); }
DI v2u trd(LAS char* L, unsigned a) { return __builtin_bit_cast(v2u, __builtin_amdgcn_ds_read_tr16_b64_v4i16((LAS s4*)(L + a))); }
DI v4u trd2(LAS char* L, unsigned a0, unsigned a1) { v2u x = trd(L, a0), y = trd(L, a1); return (v4u){x.x, x.y, y.x, y.y}; }
DI v4u ld128(LAS char* L, unsigned a) { return *(LAS v4u*)(L + a); }
DI f4 ldf4(LAS char* L, unsigned a) { return *(LAS f4*)(L + a); }
DI f2 ldf2(LAS char* L, unsigned a) { return *(LAS f2*)(L + a); }
DI v2u ld64(LAS char* L, unsigned a) { return *(LAS v2u*)(L + a); }

template <int CTRL> DI float dpp(float v) { return bitsf((unsigned)__builtin_amdgcn_update_dpp(0, (int)fbits(v), CTRL, 0xf, 0xf, true)); }
DI void swap16(float v, float& a, float& b) { auto r = __builtin_amdgcn_permlane16_swap(fbits(v), fbits(v), false, false); a = bitsf((unsigned)r[0]); b = bitsf((unsigned)r[1]); }
DI void swap32(float v, float& a, float& b) { auto r = __builtin_amdgcn_permlane32_swap(fbits(v), fbits(v), false, false); a = bitsf((unsigned)r[0]); b = bitsf((unsigned)r[1]); }
DI float xmax(float v) { float a, b; swap16(v, a, b); v = fmaxf(a, b); swap32(v, a, b); return fmaxf(a, b); }
DI float xsum(float v) { float a, b; swap16(v, a, b); v = a + b; swap32(v, a, b); return a + b; }
DI float rowsum16(float v) { v += dpp<0x128>(v); v += dpp<0x124>(v); v += dpp<0x122>(v); v += dpp<0x121>(v); return v; }
DI float wavesum(float v) { return xsum(rowsum16(v)); }

constexpr float LOG2E = 1.4426950408889634f;
DI float gelu(float u) {
  const float c0 = -2.0f * LOG2E * 0.7978845608028654f, c1 = c0 * 0.044715f;
  float t = u * u;
  float e = __builtin_amdgcn_exp2f(u * __builtin_fmaf(t, c1, c0));
  return u * __builtin_amdgcn_rcpf(1.0f + e);
}
DI float vmax(float a, float b) { return __builtin_elementwise_maximum(a, b); }
DI float vmin(float a, float b) { return __builtin_elementwise_minimum(a, b); }
DI void sw32(float& a, float& b) { auto r = __builtin_amdgcn_permlane32_swap(fbits(a), fbits(b), false, false); a = bitsf((unsigned)r[0]); b = bitsf((unsigned)r[1]); }
DI void sw16(float& a, float& b) { auto r = __builtin_amdgcn_permlane16_swap(fbits(a), fbits(b), false, false); a = bitsf((unsigned)r[0]); b = bitsf((unsigned)r[1]); }
template <class OP> DI float fold4(float a0, float a1, float a2, float a3, OP op) {
  sw32(a0, a1); sw32(a2, a3);
  float m01 = op(a0, a1), m23 = op(a2, a3);
  sw16(m01, m23);
  return op(m01, m23);
}
DI void unfold4(float m, float (&o)[4]) {
  float a = m, b = m;
  sw16(a, b);
  float a2 = a, b2 = b;
  sw32(a, a2); sw32(b, b2);
  o[0] = a; o[1] = a2; o[2] = b; o[3] = b2;
}
DI void features4(const f4 (&p)[4], v2u (&pos)[4], v2u (&neg)[4]) {
  float mx[4], mn[4];
#pragma unroll
  for (int t = 0; t < 4; ++t) {
    mx[t] = vmax(vmax(vmax(p[t][0], p[t][1]), p[t][2]), p[t][3]);
    mn[t] = vmin(vmin(vmin(p[t][0], p[t][1]), p[t][2]), p[t][3]);
  }
  const float MX = fold4(mx[0], mx[1], mx[2], mx[3], [](float a, float b) { return vmax(a, b); }) * -LOG2E;
  const float MN = fold4(mn[0], mn[1], mn[2], mn[3], [](float a, float b) { return vmin(a, b); }) * LOG2E;
  float a[4], b[4];
  unfold4(MX, a);
  unfold4(MN, b);
  float ep[4][4], en[4][4], sp[4], sn[4];
#pragma unroll
  for (int r = 0; r < 4; ++r)
#pragma unroll
    for (int t = 0; t < 4; ++t) {
      ep[t][r] = __builtin_amdgcn_exp2f(__builtin_fmaf(p[t][r], LOG2E, a[t]));
      en[t][r] = __builtin_amdgcn_exp2f(__builtin_fmaf(p[t][r], -LOG2E, b[t]));
    }
#pragma unroll
  for (int t = 0; t < 4; ++t) {
    sp[t] = (ep[t][0] + ep[t][1]) + (ep[t][2] + ep[t][3]);
    sn[t] = (en[t][0] + en[t][1]) + (en[t][2] + en[t][3]);
  }
  const float IP = __builtin_amdgcn_rcpf(fold4(sp[0], sp[1], sp[2], sp[3], [](float a_, float b_) { return a_ + b_; }));
  const float IN = __builtin_amdgcn_rcpf(fold4(sn[0], sn[1], sn[2], sn[3], [](float a_, float b_) { return a_ + b_; }));
  float ip[4], in[4];
  unfold4(IP, ip);
  unfold4(IN, in);
#pragma unroll
  for (int t = 0; t < 4; ++t) {
    pos[t] = (v2u){pk(ep[t][0] * ip[t], ep[t][1] * ip[t]), pk(ep[t][2] * ip[t], ep[t][3] * ip[t])};
    neg[t] = (v2u){pk(en[t][0] * in[t], en[t][1] * in[t]), pk(en[t][2] * in[t], en[t][3] * in[t])};
  }
}

template <unsigned OFF> DI unsigned win(unsigned base) {
  if constexpr (OFF >= 65536u - 16384u) { unsigned b = base + (OFF & ~16383u); asm volatile("" : "+v"(b)); return b; }
  else return base;
}
template <unsigned OFF> constexpr unsigned rem() { return OFF >= 65536u - 16384u ? (OFF & 16383u) : OFF; }
#define AUX_SC1 16
constexpr unsigned SPIN_CAP = 1u << 16;

struct Ctx {
  LAS char* L;
  unsigned lbase;
  const char *x, *wqkv, *wphi, *wo, *w1, *b1, *w2, *b2;
  char* ws;
  unsigned short* out;
  __amdgpu_buffer_rsrc_t rs;
  unsigned tag;
  int tid, lane, w, m, nh;
  int rg, q, jj, hq, isQ, rt, ct;
  const char *u0, *u1, *u2, *ush;
  unsigned ld1, ld2;
  unsigned vX, vW, vF;
};

#define TS(i) do { } while (0)
#define TSX(i) do { } while (0)
#define STAMP(c, i) do { } while (0)

template <int I, int U> DI void issue_half(const Ctx& c) {
  const unsigned lds = c.lbase + L_RING + SC.slot[I] * 16384 + c.w * 2048 + U * 1024;
  if constexpr (I < 16 && (I & 1) == 0) {
    const char* b = c.x + (size_t)(128 * c.rg + 16 * c.w + 8 * U) * 1024 + (I >> 1) * 128;
    glds16(b, U ? c.vX ^ 64u : c.vX, lds);
  } else if constexpr (I < 16) {
    const char* b = ((c.w >> 2) ? c.u1 : c.u0) + (size_t)(64 * (I >> 1) + 16 * (c.w & 3) + 8 * U) * c.ld1;
    glds16(b, U ? c.vW ^ 64u : c.vW, lds);
  } else if constexpr (I == 16) {
    const char* b = c.ush + (size_t)(64 * c.w + 32 * U) * 3584;
    const unsigned rr = c.lane >> 1;
    glds16(b, (rr ^ ((rr & 8) >> 1)) * 3584 + (c.lane & 1) * 16, lds);
  } else if constexpr (I < 21) {
    const char* b = c.u2 + (size_t)(128 * (I - 17) + 16 * c.w + 8 * U) * c.ld2;
    const unsigned r8 = c.lane >> 3, c8 = c.lane & 7;
    glds16(b, r8 * c.ld2 + ((c8 ^ (r8 & 2) ^ (4 * U)) * 16), lds);
  } else {
    constexpr int C = I - 21;
    const char* b;
    if constexpr (C < 14) {
      b = c.w2 + (size_t)((c.w >> 2) ? pos_unit(2 * C + 1) : pos_unit(2 * C)) * 65536;
    } else {
      b = c.wo + (size_t)(128 * (C - 14) + 64 * (c.w >> 2)) * 1024;
    }
    b += (size_t)(16 * (c.w & 3) + 8 * U) * 1024 + c.ct * 128;
    glds16(b, c.vF, lds);
  }
}
constexpr int nth_item(int st, int k) {
  for (int i = 0; i < NITEM; ++i) if (SC.issue[i] == st) { if (k == 0) return i; --k; }
  return -1;
}
template <int ST, int K> DI void issue_op(const Ctx& c) {
  constexpr int I = nth_item(ST, K >> 1);
  if constexpr (I >= 0) issue_half<I, (K & 1)>(c);
}
template <int ST, int K = 0> DI void issue_ring(const Ctx& c) {
  if constexpr (nth_item(ST, K >> 1) >= 0) { issue_op<ST, K>(c); issue_ring<ST, K + 1>(c); }
}
template <int C> DI void issue_h(const Ctx& c, v4u (&hf)[DH + 1][2]) {
  const char* b = c.ws + WS_H + (size_t)((4 * c.rt + c.m) * 72 + 4 * C + 2 * c.nh) * 1024;
  gld16_sc1_off<0>(hf[C % (DH + 1)][0], b, (unsigned)c.lane * 16);
  gld16_sc1_off<1024>(hf[C % (DH + 1)][1], b, (unsigned)c.lane * 16);
}

DI void spin_flags(const Ctx& c, const char* line, int n) {
#pragma unroll 1
  for (unsigned it = 0; it < SPIN_CAP; ++it) {
    unsigned v;
    gld4_sc1(v, line, (unsigned)(c.lane & 31) * 4);
    asm volatile("s_waitcnt vmcnt(0)" : "+v"(v)::"memory");
    if (__all(v == c.tag || (c.lane & 31) >= n)) return;
    __builtin_amdgcn_s_sleep(2);
  }
}
DI bool tags_ok(v4u a, unsigned tag) { return a.y == tag && a.w == tag; }

DI v4u ffn_pair(const Ctx& c, f4 a0, f4 a1, unsigned ci, unsigned b1off, f2 st) {
  const int g = c.lane >> 4;
  f4 cs0 = ldf4(c.L, L_CS + (ci + 4 * g) * 4), cs1 = ldf4(c.L, L_CS + (ci + 16 + 4 * g) * 4);
  v2u bb0 = ld64(c.L, L_B1 + b1off + 8 * g), bb1 = ld64(c.L, L_B1 + b1off + 32 + 8 * g);
  float b0[4] = {bflo(bb0.x), bfhi(bb0.x), bflo(bb0.y), bfhi(bb0.y)}, b1[4] = {bflo(bb1.x), bfhi(bb1.x), bflo(bb1.y), bfhi(bb1.y)};
  float h0[4], h1[4];
#pragma unroll
  for (int r = 0; r < 4; ++r) {
    h0[r] = gelu(__builtin_fmaf(a0[r], st.x, __builtin_fmaf(-st.y, cs0[r], b0[r])));
    h1[r] = gelu(__builtin_fmaf(a1[r], st.x, __builtin_fmaf(-st.y, cs1[r], b1[r])));
  }
  return (v4u){pk(h0[0], h0[1]), pk(h0[2], h0[3]), pk(h1[0], h1[1]), pk(h1[2], h1[3])};
}
DI v2u ln_pack(const Ctx& c, f4 a, unsigned ci, f2 st) {
  f4 cs = ldf4(c.L, L_CS + (ci + 4 * (c.lane >> 4)) * 4);
  float v[4];
#pragma unroll
  for (int r = 0; r < 4; ++r) v[r] = __builtin_fmaf(a[r], st.x, -st.y * cs[r]);
  return (v2u){pk(v[0], v[1]), pk(v[2], v[3])};
}
DI void shared_tile_epilogue(const Ctx& c, f4 accS, f2 st0) {
  const int g = c.lane >> 4;
  f4 cs = ldf4(c.L, L_CS + (192 + 4 * g) * 4);
  v2u bb = ld64(c.L, L_B1 + 256 + 8 * g);
  float b[4] = {bflo(bb.x), bfhi(bb.x), bflo(bb.y), bfhi(bb.y)}, h[4];
#pragma unroll
  for (int r = 0; r < 4; ++r) h[r] = gelu(__builtin_fmaf(accS[r], st0.x, __builtin_fmaf(-st0.y, cs[r], b[r])));
  v2u o = {pk(h[0], h[1]), pk(h[2], h[3])};
  const unsigned tt = 8 * c.rg + 2 * c.m + c.nh;
  const unsigned ks = 2 * unit_pos(c.q, 6) + (c.jj >> 1);
  __builtin_amdgcn_raw_buffer_store_b64(o, c.rs, WS_H + (tt * 72 + ks) * 1024 + c.lane * 16 + (c.jj & 1) * 8, 0, AUX_SC1);
}

__global__ __launch_bounds__(512, 2) void fused_kernel(const char* __restrict__ x, const char* __restrict__ wqkv, const char* __restrict__ wphi,
                                                       const char* __restrict__ wo, const char* __restrict__ w1, const char* __restrict__ b1,
                                                       const char* __restrict__ w2, const char* __restrict__ b2, unsigned short* __restrict__ out,
                                                       char* __restrict__ ws) {
  extern __shared__ __attribute__((aligned(16))) char smem[];
  Ctx c;
  c.L = (LAS char*)smem;
  c.lbase = (unsigned)(size_t)c.L;
  c.x = x; c.wqkv = wqkv; c.wphi = wphi; c.wo = wo; c.w1 = w1; c.b1 = b1; c.w2 = w2; c.b2 = b2; c.ws = ws; c.out = out;
  c.tid = threadIdx.x; c.lane = c.tid & 63; c.w = __builtin_amdgcn_readfirstlane(c.tid >> 6); c.m = c.w & 3; c.nh = c.w >> 2;
  const int blk = blockIdx.x, xcd = blk & 7, idx = blk >> 3;
  c.rg = (xcd >> 2) * 8 + (idx >> 2); c.q = xcd & 3; c.jj = idx & 3; c.isQ = c.jj >> 1; c.hq = 2 * c.q + (c.jj & 1);
  c.rt = xcd * 4 + (idx >> 3); c.ct = idx & 7;
  c.rs = __builtin_amdgcn_make_buffer_rsrc((void*)ws, 0, WS_END, 0x00020000);
  int lane = c.lane, g = lane >> 4, l15 = lane & 15, q4 = l15 >> 2, p4 = lane & 3;
#define REFRESH() do { int t_ = threadIdx.x; asm volatile("" : "+v"(t_)); c.tid = t_; lane = t_ & 63; c.lane = lane; g = lane >> 4; l15 = lane & 15; q4 = l15 >> 2; p4 = lane & 3; } while (0)
#define FENCE() __builtin_amdgcn_sched_barrier(0)
  LAS char* L = c.L;
  STAMP(c, 0);
  c.tag = *(const __attribute__((address_space(4))) unsigned*)(ws + WS_EPOCH) + 1u;
  if (c.isQ) {
    c.u0 = w1 + (7 * c.q + 2 + 2 * (c.jj & 1)) * 128; c.u1 = c.u0 + 128; c.ld1 = 3584;
    c.u2 = wqkv + c.hq * 128; c.ld2 = 3072;
  } else {
    c.u0 = wqkv + (8 + c.hq) * 128; c.u1 = wqkv + (16 + c.hq) * 128; c.ld1 = 3072;
    c.u2 = w1 + (7 * c.q + c.jj) * 128; c.ld2 = 3584;
  }
  c.ush = w1 + (7 * c.q + 6) * 128 + c.jj * 32;

  c.vX = (lane >> 3) * 1024 + (((lane & 7) ^ (lane >> 4)) * 16);
  c.vW = (lane >> 3) * c.ld1 + (((lane & 7) ^ ((lane >> 3) & 2)) * 16);
  v4u xr, cw0, cw1;
  const int j16 = 4 * c.q + c.jj;
  gld16(xr, x + (size_t)(128 * c.rg + 8 * j16 + c.w) * 1024, lane * 16);
  const int job = (xcd >> 2) * 32 + idx;
  const int jobc = job < 52 ? job : 51;
  const int li = jobc >> 2, kq = jobc & 3;
  const int gunit = li < 6 ? (li >> 1) * 8 + 2 * c.q + (li & 1) : 24 + 7 * c.q + (li - 6);
  {
    const char* wb = gunit < 24 ? wqkv + gunit * 128 : w1 + (gunit - 24) * 128;
    const unsigned ld = gunit < 24 ? 3072 : 3584;
    const char* bb = wb + (size_t)(128 * kq + 16 * c.w) * ld;
    gld16(cw0, bb, (lane >> 3) * ld + (lane & 7) * 16);
    gld16(cw1, bb + 8 * ld, (lane >> 3) * ld + (lane & 7) * 16);
  }
  {
    const int fa = c.isQ ? 7 * c.q + 2 + 2 * (c.jj & 1) : 7 * c.q + c.jj;
    if (c.w == 0) glds4(b1 + fa * 128, lane * 4, c.lbase + L_B1);
    else if (c.w == 1) glds4(b1 + (7 * c.q + 6) * 128 + c.jj * 32, (lane & 7) * 4, c.lbase + L_B1 + 256);
    else if (c.w == 2) glds16(wphi + c.hq * 2048, lane * 16, c.lbase + L_WPHI);
    else if (c.w == 3) glds16(wphi + c.hq * 2048 + 1024, lane * 16, c.lbase + L_WPHI + 1024);
    else glds4(b2 + c.ct * 128, (lane & 31) * 4, c.lbase + L_B2);
  }
  issue_ring<-1>(c);
  {
    const int id = c.tid, half = id & 1, li = id >> 1;
    const char* a;
    if (li < 160) { const int r = li / 5, sg = li - 5 * r; a = x + (size_t)(128 * c.rg + 32 * c.jj + r) * 1024 + (3 + sg) * 128; }
    else { const int lj = li < 240 ? li - 160 : 79, un = lj >= 40, r = lj - 40 * un; a = (un ? c.u1 : c.u0) + (size_t)(192 + 40 * (c.rg & 7) + r) * c.ld1; }
    glds4v(a + half * 64, c.lbase + L_CS);
  }

  asm volatile("s_waitcnt vmcnt(15)" : "+v"(xr), "+v"(cw0), "+v"(cw1)::"memory");
  {
    float v[8] = {bflo(xr.x), bfhi(xr.x), bflo(xr.y), bfhi(xr.y), bflo(xr.z), bfhi(xr.z), bflo(xr.w), bfhi(xr.w)};
    float s = ((v[0] + v[1]) + (v[2] + v[3])) + ((v[4] + v[5]) + (v[6] + v[7]));
    float mean = wavesum(s) * (1.0f / 512.0f);
    float qq = 0.f;
#pragma unroll
    for (int e = 0; e < 8; ++e) { float d = v[e] - mean; qq = __builtin_fmaf(d, d, qq); }
    float var = wavesum(qq) * (1.0f / 512.0f);
    float rstd = __builtin_amdgcn_rsqf(var + 1e-5f);
    if (lane == 0)
      __builtin_amdgcn_raw_buffer_store_b128((v4u){fbits(mean), c.tag, fbits(rstd), c.tag}, c.rs, WS_STATS + (128 * c.rg + 8 * j16 + c.w) * 16, 0, AUX_SC1);
    float cs[8] = {bflo(cw0.x) + bflo(cw1.x), bfhi(cw0.x) + bfhi(cw1.x), bflo(cw0.y) + bflo(cw1.y), bfhi(cw0.y) + bfhi(cw1.y),
                   bflo(cw0.z) + bflo(cw1.z), bfhi(cw0.z) + bfhi(cw1.z), bflo(cw0.w) + bflo(cw1.w), bfhi(cw0.w) + bfhi(cw1.w)};
#pragma unroll
    for (int e = 0; e < 8; ++e) { float t = cs[e] + dpp<0x128>(cs[e]); cs[e] = xsum(t); }
    if (lane < 8) {
      *(LAS f4*)(L + L_SCR + c.w * 256 + lane * 32) = (f4){cs[0], cs[1], cs[2], cs[3]};
      *(LAS f4*)(L + L_SCR + c.w * 256 + lane * 32 + 16) = (f4){cs[4], cs[5], cs[6], cs[7]};
    }
  }
  STAMP(c, 1);
  FENCE();

  v4u xf[2][16];
  f4 acc[2][2][2];
#pragma unroll
  for (int a = 0; a < 2; ++a)
#pragma unroll
    for (int b = 0; b < 2; ++b)
#pragma unroll
      for (int t = 0; t < 2; ++t) acc[a][b][t] = (f4){0.f, 0.f, 0.f, 0.f};
  REFRESH();
  const unsigned xa0 = (unsigned)((32 * c.m + l15) * 128 + ((g ^ (l15 >> 1)) * 16));
  const unsigned xtt0 = c.nh * 2048, xtt1 = (1 - c.nh) * 2048;
  const unsigned wa0 = (unsigned)((8 * g + q4) * 128 + (((4 * c.nh + (p4 >> 1)) ^ ((((q4 >> 1) & 1) | ((g & 1) << 1)) << 1)) * 16) + (p4 & 1) * 8);
  v4u g1a, g1b;
  auto g1_off = [&]() -> unsigned {
    const int t = c.tid;
    if (t < 128) return WS_STATS + (128 * c.rg + t) * 16;
    const int ci = t - 128 < 208 ? t - 128 : 207;
    const int un = ci >> 6;
    int gu;
    if (c.isQ) gu = un == 0 ? 24 + 7 * c.q + 2 + 2 * (c.jj & 1) : un == 1 ? 24 + 7 * c.q + 3 + 2 * (c.jj & 1) : un == 2 ? c.hq : 24 + 7 * c.q + 6;
    else gu = un == 0 ? 8 + c.hq : un == 1 ? 16 + c.hq : un == 2 ? 24 + 7 * c.q + c.jj : 24 + 7 * c.q + 6;
    const int col = gu * 64 + (un == 3 ? 16 * c.jj + (ci & 15) : (ci & 63));
    return WS_CS + col * 32;
  };

  v4u wfa[2][2], wfb[2][2];
  const unsigned xb0 = xa0 + xtt0, xb1 = xa0 + xtt1, xb2 = (xa0 ^ 64) + xtt0, xb3 = (xa0 ^ 64) + xtt1, wa1 = wa0 ^ 32;
  auto pass1_step = [&](auto KC) {
    constexpr int kc = decltype(KC)::value;
    constexpr unsigned sx = L_RING + SC.slot[2 * kc] * 16384, sw = L_RING + SC.slot[2 * kc + 1] * 16384;
    if constexpr (kc == ST_G1_CHECK) {
      TS(3 * kc);
      asm volatile("s_waitcnt vmcnt(%2) lgkmcnt(0)" : "+v"(g1a), "+v"(g1b) : "n"(imin(top_wait(kc), g1_wait())) : "memory");
      REFRESH();
      const bool isst = c.tid < 128;
      if (!__all(tags_ok(g1a, c.tag) && (isst || tags_ok(g1b, c.tag)))) {
        const unsigned off = g1_off();
#pragma unroll 1
        for (unsigned it = 0; it < SPIN_CAP; ++it) {
          __builtin_amdgcn_s_sleep(2);
          gld16_sc1(g1a, ws, off);
          gld16_sc1(g1b, ws, isst ? off : off + 16);
          asm volatile("s_waitcnt vmcnt(0)" : "+v"(g1a), "+v"(g1b)::"memory");
          if (__all(tags_ok(g1a, c.tag) && (isst || tags_ok(g1b, c.tag)))) break;
        }
      }
      if (isst) *(LAS f2*)(L + L_ST + c.tid * 8) = (f2){bitsf(g1a.z), bitsf(g1a.x) * bitsf(g1a.z)};
      else if (c.tid < 128 + 208) *(LAS float*)(L + L_CS + (c.tid - 128) * 4) = (bitsf(g1a.x) + bitsf(g1a.z)) + (bitsf(g1b.x) + bitsf(g1b.z));
      asm volatile("s_waitcnt lgkmcnt(0)" ::: "memory");
    } else {
      TS(3 * kc);
      wait_top<top_wait(kc)>();
    }
    TS(3 * kc + 1);
    bar();
    TS(3 * kc + 2);
    FENCE();
    if constexpr (kc == 5 || kc == 6 || kc == 7) REFRESH();
    const unsigned x0 = win<sx>(xb0), x1 = win<sx>(xb1), x2 = win<sx>(xb2), x3 = win<sx>(xb3), w0 = win<sw>(wa0), w1 = win<sw>(wa1);
    constexpr unsigned rx = rem<sx>(), rw = rem<sw>();
#define P1_MMA(F, KS, UN, I, T) acc[UN][I][T] = mfma32(F[UN][I], xf[T][KS], acc[UN][I][T])
#define P1_HALF(FR, FM, KSR, KSM, XA, XB, SOFF, G0, G1, DOM)                                        \
    xf[0][KSR] = ld128(L, XA + rx);                                                                \
    xf[1][KSR] = ld128(L, XB + rx);                                                                \
    FR[0][0] = trd2(L, w0 + rw + SOFF, w0 + rw + SOFF + 512);                                      \
    FENCE();                                                                                       \
    if constexpr (DOM) P1_MMA(FM, KSM, 0, 0, 0);                                                   \
    FR[0][1] = trd2(L, w1 + rw + SOFF, w1 + rw + SOFF + 512);                                      \
    FENCE();                                                                                       \
    if constexpr (DOM) P1_MMA(FM, KSM, 0, 0, 1);                                                   \
    FR[1][0] = trd2(L, w0 + rw + SOFF + 8192, w0 + rw + SOFF + 8192 + 512);                        \
    FENCE();                                                                                       \
    if constexpr (DOM) P1_MMA(FM, KSM, 0, 1, 0);                                                   \
    FENCE();                                                                                       \
    issue_op<kc, G0>(c);                                                                           \
    FENCE();                                                                                       \
    FR[1][1] = trd2(L, w1 + rw + SOFF + 8192, w1 + rw + SOFF + 8192 + 512);                        \
    FENCE();                                                                                       \
    if constexpr (DOM) { P1_MMA(FM, KSM, 0, 1, 1); P1_MMA(FM, KSM, 1, 0, 0); P1_MMA(FM, KSM, 1, 0, 1); } \
    FENCE();                                                                                       \
    issue_op<kc, G1>(c);                                                                           \
    FENCE();                                                                                       \
    if constexpr (DOM) { P1_MMA(FM, KSM, 1, 1, 0); P1_MMA(FM, KSM, 1, 1, 1); }                     \
    FENCE();
    P1_HALF(wfa, wfb, 2 * kc, (kc > 0 ? 2 * kc - 1 : 0), x0, x1, 0, 0, 1, (kc > 0))
    if constexpr (kc == 0) {
      if (c.w == 7 && job < 52) {
        float s = 0.f;
#pragma unroll
        for (int ww = 0; ww < 8; ++ww) s += *(LAS float*)(L + L_SCR + ww * 256 + lane * 4);
        __builtin_amdgcn_raw_buffer_store_b64((v2u){fbits(s), c.tag}, c.rs, WS_CS + (gunit * 64 + lane) * 32 + kq * 8, 0, AUX_SC1);
      }
      FENCE();
    }
    P1_HALF(wfb, wfa, 2 * kc + 1, 2 * kc, x2, x3, 4096, 2, 3, true)
    if constexpr (kc == ST_G1_ISSUE) {
      REFRESH();
      const unsigned off = g1_off();
      gld16_sc1(g1a, ws, off);
      gld16_sc1(g1b, ws, c.tid < 128 ? off : off + 16);
      FENCE();
    }
  };
  pass1_step(std::integral_constant<int, 0>{});
  STAMP(c, 2);
  pass1_step(std::integral_constant<int, 1>{});
  pass1_step(std::integral_constant<int, 2>{});
  pass1_step(std::integral_constant<int, 3>{});
  pass1_step(std::integral_constant<int, 4>{});
  pass1_step(std::integral_constant<int, 5>{});
  pass1_step(std::integral_constant<int, 6>{});
  pass1_step(std::integral_constant<int, 7>{});
  STAMP(c, 3);

  TS(3 * ST_E1);
  wait_top<top_wait(ST_E1)>();
  TS(3 * ST_E1 + 1);
  bar();
  TS(3 * ST_E1 + 2);
  FENCE();
  REFRESH();
#pragma unroll
  for (int un = 0; un < 2; ++un)
#pragma unroll
    for (int i = 0; i < 2; ++i) {
      acc[un][i][0] = mfma32(wfb[un][i], xf[0][15], acc[un][i][0]);
      acc[un][i][1] = mfma32(wfb[un][i], xf[1][15], acc[un][i][1]);
    }
  FENCE();
  c.vF = (lane >> 3) * 1024 + (((lane & 7) ^ ((lane >> 3) & 6)) * 16);
  issue_op<ST_E1, 0>(c);
  issue_op<ST_E1, 1>(c);
  FENCE();
  TSX(7);
  const unsigned gt0 = 8 * c.rg + 2 * c.m + c.nh, gt1 = 8 * c.rg + 2 * c.m + 1 - c.nh;
  const f4 z4 = {0.f, 0.f, 0.f, 0.f};
  if (c.isQ) {
    const f2 st0 = ldf2(L, L_ST + (16 * (2 * c.m + c.nh) + l15) * 8), st1 = ldf2(L, L_ST + (16 * (2 * c.m + 1 - c.nh) + l15) * 8);
#pragma unroll
    for (int un = 0; un < 2; ++un) {
      const unsigned ks = 2 * unit_pos(c.q, 2 + 2 * (c.jj & 1) + un) + c.nh;
      const v4u h0 = ffn_pair(c, acc[un][0][0], acc[un][1][0], 64 * un + 32 * c.nh, un * 128 + c.nh * 64, st0);
      __builtin_amdgcn_raw_buffer_store_b128(h0, c.rs, WS_H + (gt0 * 72 + ks) * 1024 + lane * 16, 0, AUX_SC1);
      const v4u h1 = ffn_pair(c, acc[un][0][1], acc[un][1][1], 64 * un + 32 * c.nh, un * 128 + c.nh * 64, st1);
      __builtin_amdgcn_raw_buffer_store_b128(h1, c.rs, WS_H + (gt1 * 72 + ks) * 1024 + lane * 16, 0, AUX_SC1);
      if (un == 0) { FENCE(); issue_op<ST_E1, 2>(c); issue_op<ST_E1, 3>(c); FENCE(); }
    }
  } else {
    const f2 st0 = ldf2(L, L_ST + (16 * (2 * c.m + c.nh) + l15) * 8), st1 = ldf2(L, L_ST + (16 * (2 * c.m + 1 - c.nh) + l15) * 8);
    const unsigned scr = L_SCR + c.w * SCRW;
    const unsigned img = scr + 2176;
    const v4u ones = {0x3f803f80u, 0x3f803f80u, 0x3f803f80u, 0x3f803f80u};
    TSX(0);
    f4 pk4[4];
    v2u vb[4], fp[4], fn[4];
#pragma unroll
    for (int i = 0; i < 2; ++i) {
      const v2u wt = trd(L, L_WPHI + (2 * c.nh + i) * 512 + (4 * g + q4) * 32 + p4 * 8);
#pragma unroll
      for (int tt = 0; tt < 2; ++tt) {
        const f2 st = tt ? st1 : st0;
        const v2u kb = ln_pack(c, acc[0][i][tt], 32 * c.nh + 16 * i, st);
        vb[2 * i + tt] = ln_pack(c, acc[1][i][tt], 64 + 32 * c.nh + 16 * i, st);
        pk4[2 * i + tt] = mfma16(wt, kb, z4);
      }
    }
    FENCE();
    TSX(1);
    issue_op<ST_E1, 2>(c);
    issue_op<ST_E1, 3>(c);
    FENCE();
    TSX(2);
    features4(pk4, fp, fn);
    FENCE();
    TSX(3);
#pragma unroll
    for (int i = 0; i < 2; ++i) {
#pragma unroll
      for (int tt = 0; tt < 2; ++tt) {
        const unsigned wa = img + (16 * tt + l15) * 32 + ((g ^ q4) * 8);
        *(LAS v2u*)(L + wa) = fp[2 * i + tt];
        *(LAS v2u*)(L + wa + 1024) = fn[2 * i + tt];
        *(LAS v2u*)(L + wa + 2048) = vb[2 * i + tt];
      }
      const unsigned ra = img + (4 * g + q4) * 32 + ((p4 ^ g) * 8);
      const v4u Ap = trd2(L, ra, ra + 512), An = trd2(L, ra + 1024, ra + 1536), Bv = trd2(L, ra + 2048, ra + 2560);
      const f4 kvp = mfma32(Ap, Bv, z4), kvn = mfma32(An, Bv, z4), zp = mfma32(Ap, ones, z4), zn = mfma32(An, ones, z4);
      *(LAS f4*)(L + scr + i * 2176 + lane * 16) = kvp;
      *(LAS f4*)(L + scr + i * 2176 + 1024 + lane * 16) = kvn;
      if (l15 == 0) {
        *(LAS f4*)(L + scr + i * 2176 + 2048 + g * 16) = zp;
        *(LAS f4*)(L + scr + i * 2176 + 2112 + g * 16) = zn;
      }
    }
    asm volatile("s_waitcnt lgkmcnt(0)" ::: "memory");
    TSX(4);
    bar();
    TSX(5);
    const unsigned pbase = WS_P + (c.hq * 16 + c.rg) * PART_BYTES;
#pragma unroll
    for (int rep = 0; rep < 2; ++rep) {
      const unsigned pc = c.tid + rep * 512;
      const unsigned hl = pc >> 8, off = (hl & 1) * 2176 + (pc & 255) * 8, w0 = (hl >> 1) * 4;
      f2 s = ldf2(L, L_SCR + (w0 + 0) * SCRW + off);
      s += ldf2(L, L_SCR + (w0 + 1) * SCRW + off);
      s += ldf2(L, L_SCR + (w0 + 2) * SCRW + off);
      s += ldf2(L, L_SCR + (w0 + 3) * SCRW + off);
      __builtin_amdgcn_raw_buffer_store_b128((v4u){fbits(s.x), c.tag, fbits(s.y), c.tag}, c.rs, pbase + pc * 16, 0, AUX_SC1);
    }
    if (c.w == 0) {
      const unsigned zc = lane, hl = zc >> 4, off = (hl & 1) * 2176 + 2048 + (zc & 15) * 8, w0 = (hl >> 1) * 4;
      f2 s = ldf2(L, L_SCR + (w0 + 0) * SCRW + off);
      s += ldf2(L, L_SCR + (w0 + 1) * SCRW + off);
      s += ldf2(L, L_SCR + (w0 + 2) * SCRW + off);
      s += ldf2(L, L_SCR + (w0 + 3) * SCRW + off);
      __builtin_amdgcn_raw_buffer_store_b128((v4u){fbits(s.x), c.tag, fbits(s.y), c.tag}, c.rs, pbase + (1024 + zc) * 16, 0, AUX_SC1);
    }
  }
  TSX(6);
  STAMP(c, 4);
  FENCE();

  f4 acc2[2][2], accS = z4;
#pragma unroll
  for (int b = 0; b < 2; ++b)
#pragma unroll
    for (int t = 0; t < 2; ++t) acc2[b][t] = z4;
  REFRESH();
  const unsigned wb0 = (unsigned)((8 * g + q4) * 128 + (((4 * c.nh + (p4 >> 1)) ^ ((((q4 >> 1) & 1) | ((g & 1) << 1)) << 1)) * 16) + (p4 & 1) * 8);
  const unsigned sa0 = (unsigned)((8 * g + ((g & 1) << 2) + q4) * 32 + p4 * 8);
  unsigned pollv;
  v4u rk0, rk1, rz0, rz1;
  const char* flagA = ws + WS_FLAG + (0 * 16 + (c.rt >> 1)) * 128;
  const char* flagB = ws + WS_FLAG + (1 * 16 + (c.rt >> 1)) * 128;
  const char* flagO = ws + WS_FLAG + (2 * 16 + (c.rt >> 1)) * 128;
  const char* rsrc = ws + WS_P + (c.hq * 16 + 2 * c.w) * PART_BYTES;

  v4u pwa[2], psa, pwb[2], psb;
  const unsigned wb1 = wb0 ^ 32, sa1 = sa0 ^ 128;
  auto p2_mma = [&](auto KS_, const v4u (&w2)[2], const v4u& sfr) {
    constexpr int ks = decltype(KS_)::value;
#pragma unroll
    for (int i = 0; i < 2; ++i) {
      acc2[i][0] = mfma32(w2[i], xf[0][ks], acc2[i][0]);
      acc2[i][1] = mfma32(w2[i], xf[1][ks], acc2[i][1]);
    }
    accS = mfma32(sfr, xf[0][ks], accS);
  };
  auto pass2_step = [&](auto KC) {
    constexpr int kc = decltype(KC)::value, st = 9 + kc;
    constexpr unsigned sp = L_RING + SC.slot[17 + kc] * 16384, ss = L_RING + SC.slot[16] * 16384;
    TS(3 * st);
    wait_top<top_wait(st)>();
    TS(3 * st + 1);
    bar();
    TS(3 * st + 2);
    FENCE();
    const unsigned w0 = win<sp>(wb0), w1 = win<sp>(wb1), s0 = win<ss>(sa0), s1 = win<ss>(sa1);
    constexpr unsigned rp = rem<sp>(), rs_ = rem<ss>();
#define P2_Q(FR, SR, KR, FM, SM, KM, DOM)                                                          \
    FR[0] = trd2(L, w0 + rp + ((KR) & 3) * 4096, w0 + rp + ((KR) & 3) * 4096 + 512);               \
    FENCE();                                                                                       \
    if constexpr (DOM) acc2[0][0] = mfma32(FM[0], xf[0][KM], acc2[0][0]);                          \
    FR[1] = trd2(L, w1 + rp + ((KR) & 3) * 4096, w1 + rp + ((KR) & 3) * 4096 + 512);               \
    FENCE();                                                                                       \
    if constexpr (DOM) acc2[0][1] = mfma32(FM[0], xf[1][KM], acc2[0][1]);                          \
    SR = trd2(L, s0 + rs_ + (KR) * 1024, s1 + rs_ + (KR) * 1024);                                  \
    FENCE();                                                                                       \
    if constexpr (DOM) { acc2[1][0] = mfma32(FM[1], xf[0][KM], acc2[1][0]); acc2[1][1] = mfma32(FM[1], xf[1][KM], acc2[1][1]); accS = mfma32(SM, xf[0][KM], accS); } \
    FENCE();
    P2_Q(pwa, psa, 4 * kc, pwb, psb, (kc > 0 ? 4 * kc - 1 : 0), (kc > 0))
    issue_op<st, 0>(c);
    if constexpr (st == ST_PUBA) {
      if (c.isQ && c.tid == 0) __builtin_amdgcn_raw_buffer_store_b32(c.tag, c.rs, WS_FLAG + (0 * 16 + c.rg) * 128 + c.hq * 4, 0, AUX_SC1);
    }
    FENCE();
    P2_Q(pwb, psb, 4 * kc + 1, pwa, psa, 4 * kc, true)
    issue_op<st, 1>(c);
    FENCE();
    P2_Q(pwa, psa, 4 * kc + 2, pwb, psb, 4 * kc + 1, true)
    if constexpr (st == ST_POLLA_ISSUE) { REFRESH(); gld4_sc1(pollv, flagA, (lane & 31) * 4); }
    if constexpr (st == ST_R_ISSUE) {
      const unsigned vk = c.isQ ? (64 * c.rg + lane) * 16 : 0, vz = c.isQ ? (1024 + 4 * c.rg + (lane & 3)) * 16 : 0;
      gld16_sc1(rk0, rsrc, vk);
      gld16_sc1(rk1, rsrc + PART_BYTES, vk);
      gld16_sc1(rz0, rsrc, vz);
      gld16_sc1(rz1, rsrc + PART_BYTES, vz);
    }
    FENCE();
    P2_Q(pwb, psb, 4 * kc + 3, pwa, psa, 4 * kc + 2, true)
  };
  pass2_step(std::integral_constant<int, 0>{});
  pass2_step(std::integral_constant<int, 1>{});
  pass2_step(std::integral_constant<int, 2>{});
  pass2_step(std::integral_constant<int, 3>{});
  STAMP(c, 5);

  v4u hf[DH + 1][2];
  v4u phq[2][2];
  TS(3 * ST_E2);
  wait_top<top_wait(ST_E2)>();
  asm volatile("s_waitcnt vmcnt(%5)" : "+v"(pollv), "+v"(rk0), "+v"(rk1), "+v"(rz0), "+v"(rz1) : "n"(extra_wait(ST_R_ISSUE, ST_E2)) : "memory");
  static_assert(ST_POLLA_ISSUE == ST_R_ISSUE, "one wait covers both");
  REFRESH();
  if (c.w == 0) {
    if (!__all(pollv == c.tag || (lane & 31) >= 8)) spin_flags(c, flagA, 8);
  }
  if (c.isQ) {
    if (!__all(tags_ok(rk0, c.tag) && tags_ok(rk1, c.tag) && tags_ok(rz0, c.tag) && tags_ok(rz1, c.tag))) {
      const unsigned vk = (64 * c.rg + lane) * 16, vz = (1024 + 4 * c.rg + (lane & 3)) * 16;
#pragma unroll 1
      for (unsigned it = 0; it < SPIN_CAP; ++it) {
        __builtin_amdgcn_s_sleep(2);
        gld16_sc1(rk0, rsrc, vk);
        gld16_sc1(rk1, rsrc + PART_BYTES, vk);
        gld16_sc1(rz0, rsrc, vz);
        gld16_sc1(rz1, rsrc + PART_BYTES, vz);
        asm volatile("s_waitcnt vmcnt(0)" : "+v"(rk0), "+v"(rk1), "+v"(rz0), "+v"(rz1)::"memory");
        if (__all(tags_ok(rk0, c.tag) && tags_ok(rk1, c.tag) && tags_ok(rz0, c.tag) && tags_ok(rz1, c.tag))) break;
      }
    }
    *(LAS f2*)(L + L_SCR + c.w * 1024 + lane * 8) = (f2){bitsf(rk0.x) + bitsf(rk1.x), bitsf(rk0.z) + bitsf(rk1.z)};
    if (lane < 4) *(LAS f2*)(L + L_SCR + c.w * 1024 + 512 + lane * 8) = (f2){bitsf(rz0.x) + bitsf(rz1.x), bitsf(rz0.z) + bitsf(rz1.z)};
    asm volatile("s_waitcnt lgkmcnt(0)" ::: "memory");
  }
  TS(3 * ST_E2 + 1);
  bar();
  TS(3 * ST_E2 + 2);
  FENCE();
  p2_mma(std::integral_constant<int, 15>{}, pwb, psb);
  FENCE();
  issue_op<ST_E2, 0>(c);
  issue_h<0>(c, hf);
  issue_op<ST_E2, 1>(c);
  issue_h<1>(c, hf);
  static_assert(DH == 4, "initial h loads");
  FENCE();
  {
    const f2 st0 = ldf2(L, L_ST + (16 * (2 * c.m + c.nh) + l15) * 8), st1 = ldf2(L, L_ST + (16 * (2 * c.m + 1 - c.nh) + l15) * 8);
    if (c.isQ) {
      if (c.w == 0) {
        f2 s = {0.f, 0.f}, sz = {0.f, 0.f};
#pragma unroll
        for (int ww = 0; ww < 8; ++ww) { s += ldf2(L, L_SCR + ww * 1024 + lane * 8); sz += ldf2(L, L_SCR + ww * 1024 + 512 + (lane & 3) * 8); }
        const unsigned rb = WS_R + c.hq * PART_BYTES;
        __builtin_amdgcn_raw_buffer_store_b128((v4u){fbits(s.x), c.tag, fbits(s.y), c.tag}, c.rs, rb + (64 * c.rg + lane) * 16, 0, AUX_SC1);
        if (lane < 4) __builtin_amdgcn_raw_buffer_store_b128((v4u){fbits(sz.x), c.tag, fbits(sz.y), c.tag}, c.rs, rb + (1024 + 4 * c.rg + lane) * 16, 0, AUX_SC1);
      }
      f4 pq4[4];
      v2u fp[4], fn[4];
#pragma unroll
      for (int i = 0; i < 2; ++i) {
        const v2u wt = trd(L, L_WPHI + (2 * c.nh + i) * 512 + (4 * g + q4) * 32 + p4 * 8);
#pragma unroll
        for (int tt = 0; tt < 2; ++tt) pq4[2 * i + tt] = mfma16(wt, ln_pack(c, acc2[i][tt], 128 + 32 * c.nh + 16 * i, tt ? st1 : st0), z4);
      }
      features4(pq4, fp, fn);
#pragma unroll
      for (int i = 0; i < 2; ++i)
#pragma unroll
        for (int tt = 0; tt < 2; ++tt) phq[i][tt] = (v4u){fp[2 * i + tt].x, fp[2 * i + tt].y, fn[2 * i + tt].x, fn[2 * i + tt].y};
    } else {
      const unsigned ks = 2 * unit_pos(c.q, c.jj) + c.nh;
      const v4u h0 = ffn_pair(c, acc2[0][0], acc2[1][0], 128 + 32 * c.nh, c.nh * 64, st0);
      __builtin_amdgcn_raw_buffer_store_b128(h0, c.rs, WS_H + (gt0 * 72 + ks) * 1024 + lane * 16, 0, AUX_SC1);
      const v4u h1 = ffn_pair(c, acc2[0][1], acc2[1][1], 128 + 32 * c.nh, c.nh * 64, st1);
      __builtin_amdgcn_raw_buffer_store_b128(h1, c.rs, WS_H + (gt1 * 72 + ks) * 1024 + lane * 16, 0, AUX_SC1);
#pragma unroll
      for (int i = 0; i < 2; ++i)
#pragma unroll
        for (int tt = 0; tt < 2; ++tt) phq[i][tt] = (v4u){0u, 0u, 0u, 0u};
    }
    FENCE();
    issue_op<ST_E2, 2>(c);
    issue_h<2>(c, hf);
    issue_op<ST_E2, 3>(c);
    issue_h<3>(c, hf);
    FENCE();
    shared_tile_epilogue(c, accS, st0);
  }
  STAMP(c, 6);
  FENCE();

  f4 accF[4];
#pragma unroll
  for (int t = 0; t < 4; ++t) accF[t] = z4;
  REFRESH();
  const unsigned fa0 = (unsigned)((64 * c.nh + 4 * g + q4) * 128 + (((p4 >> 1) ^ ((((g & 1) << 1) | (q4 >> 1)) << 1)) * 16) + (p4 & 1) * 8);
  v4u rg0, rg1, rgz0, rgz1, xres, fwa[4], fwb[4], hprev;
  const unsigned fa1 = fa0 ^ 32, fa2 = fa0 ^ 64, fa3 = fa0 ^ 96;
  const char* rres = ws + WS_R + c.hq * PART_BYTES;

  auto phase2_step = [&](auto CC) {
    constexpr int ch = decltype(CC)::value, st = ST_F0 + ch;
    constexpr unsigned sf = L_RING + SC.slot[21 + ch] * 16384;
    TS(3 * st);
    asm volatile("s_waitcnt vmcnt(%2) lgkmcnt(0)" : "+v"(hf[ch % (DH + 1)][0]), "+v"(hf[ch % (DH + 1)][1]) : "n"(top_wait(st)) : "memory");
    if constexpr (st == ST_POLLB_CHECK) {
      asm volatile("s_waitcnt vmcnt(%1)" : "+v"(pollv) : "n"(extra_wait(ST_POLLB_ISSUE, st)) : "memory");
      if (c.w == 0) { if (!__all(pollv == c.tag || (lane & 31) >= 16)) spin_flags(c, flagB, 16); }
    }
    if constexpr (st == ST_POLLO_CHECK) {
      asm volatile("s_waitcnt vmcnt(%1)" : "+v"(pollv) : "n"(extra_wait(ST_POLLO_ISSUE, st)) : "memory");
      if (c.w == 0) { if (!__all(pollv == c.tag || (lane & 31) >= 8)) spin_flags(c, flagO, 8); }
    }
    if constexpr (st == ST_RG_CHECK) {
      asm volatile("s_waitcnt vmcnt(%4)" : "+v"(rg0), "+v"(rg1), "+v"(rgz0), "+v"(rgz1) : "n"(extra_wait(ST_RG_ISSUE, st)) : "memory");
      if (c.isQ) {
        const unsigned t = c.tid;
        if (!__all(tags_ok(rg0, c.tag) && tags_ok(rg1, c.tag) && tags_ok(rgz0, c.tag) && tags_ok(rgz1, c.tag))) {
  #pragma unroll 1
      for (unsigned it = 0; it < SPIN_CAP; ++it) {
            __builtin_amdgcn_s_sleep(2);
            gld16_sc1(rg0, rres, t * 32);
            gld16_sc1(rg1, rres + 16, t * 32);
            gld16_sc1(rgz0, rres + 16384, (t & 31) * 32);
            gld16_sc1(rgz1, rres + 16384 + 16, (t & 31) * 32);
            asm volatile("s_waitcnt vmcnt(0)" : "+v"(rg0), "+v"(rg1), "+v"(rgz0), "+v"(rgz1)::"memory");
            if (__all(tags_ok(rg0, c.tag) && tags_ok(rg1, c.tag) && tags_ok(rgz0, c.tag) && tags_ok(rgz1, c.tag))) break;
          }
        }
        *(LAS v2u*)(L + L_SCR + (t >> 7) * 1024 + (t & 63) * 16 + ((t >> 6) & 1) * 8) =
            (v2u){pk(bitsf(rg0.x), bitsf(rg0.z)), pk(bitsf(rg1.x), bitsf(rg1.z))};
        if (t < 32)
          *(LAS v2u*)(L + L_SCR + 4096 + (t >> 3) * 64 + (t & 3) * 16 + ((t >> 2) & 1) * 8) =
              (v2u){pk(bitsf(rgz0.x), bitsf(rgz0.z)), pk(bitsf(rgz1.x), bitsf(rgz1.z))};
        asm volatile("s_waitcnt lgkmcnt(0)" ::: "memory");
      }
    }
    TS(3 * st + 1);
    bar();
    TS(3 * st + 2);
    FENCE();
    const unsigned f0 = win<sf>(fa0), f1 = win<sf>(fa1), f2 = win<sf>(fa2), f3 = win<sf>(fa3);
    constexpr unsigned rf = rem<sf>();
    fwa[0] = trd2(L, f0 + rf, f0 + rf + 2048);
    fwa[1] = trd2(L, f1 + rf, f1 + rf + 2048);
    FENCE();
    if constexpr (ch > 0) accF[0] = mfma32(fwb[0], hprev, accF[0]);
    fwa[2] = trd2(L, f2 + rf, f2 + rf + 2048);
    FENCE();
    if constexpr (ch > 0) accF[1] = mfma32(fwb[1], hprev, accF[1]);
    fwa[3] = trd2(L, f3 + rf, f3 + rf + 2048);
    FENCE();
    if constexpr (ch > 0) accF[2] = mfma32(fwb[2], hprev, accF[2]);
    FENCE();
    issue_op<st, 0>(c);
    if constexpr (st == ST_PUBB) {
      if (c.tid == 0) __builtin_amdgcn_raw_buffer_store_b32(c.tag, c.rs, WS_FLAG + (1 * 16 + c.rg) * 128 + (4 * c.q + c.jj) * 4, 0, AUX_SC1);
    }
    if constexpr (st == ST_PUBO) {
      if (c.isQ && c.tid == 0) __builtin_amdgcn_raw_buffer_store_b32(c.tag, c.rs, WS_FLAG + (2 * 16 + c.rg) * 128 + c.hq * 4, 0, AUX_SC1);
    }
    FENCE();
    if constexpr (ch > 0) accF[3] = mfma32(fwb[3], hprev, accF[3]);
    FENCE();
    issue_op<st, 1>(c);
    FENCE();
    fwb[0] = trd2(L, f0 + rf + 4096, f0 + rf + 4096 + 2048);
    fwb[1] = trd2(L, f1 + rf + 4096, f1 + rf + 4096 + 2048);
    FENCE();
    accF[0] = mfma32(fwa[0], hf[ch % (DH + 1)][0], accF[0]);
    fwb[2] = trd2(L, f2 + rf + 4096, f2 + rf + 4096 + 2048);
    FENCE();
    accF[1] = mfma32(fwa[1], hf[ch % (DH + 1)][0], accF[1]);
    fwb[3] = trd2(L, f3 + rf + 4096, f3 + rf + 4096 + 2048);
    FENCE();
    accF[2] = mfma32(fwa[2], hf[ch % (DH + 1)][0], accF[2]);
    FENCE();
    if constexpr (ch + DH < 18) issue_h<ch + DH>(c, hf);
    if constexpr (st == ST_POLLB_ISSUE) gld4_sc1(pollv, flagB, (lane & 31) * 4);
    if constexpr (st == ST_POLLO_ISSUE) gld4_sc1(pollv, flagO, (lane & 31) * 4);
    if constexpr (st == ST_RG_ISSUE) {
      const unsigned vk = c.isQ ? c.tid * 32 : 0, vz = c.isQ ? (c.tid & 31) * 32 : 0;
      gld16_sc1(rg0, rres, vk);
      gld16_sc1(rg1, rres + 16, vk);
      gld16_sc1(rgz0, rres + 16384, vz);
      gld16_sc1(rgz1, rres + 16384 + 16, vz);
    }
    if constexpr (ch == 14) gld16(xres, x + (size_t)(64 * c.rt) * 1024 + c.ct * 128, (c.tid >> 3) * 1024 + (c.tid & 7) * 16);
    FENCE();
    accF[3] = mfma32(fwa[3], hf[ch % (DH + 1)][0], accF[3]);
    hprev = hf[ch % (DH + 1)][1];
    FENCE();
    if constexpr (st == ST_O) {
      if (c.isQ) {
        v2u ob[2][2];
#pragma unroll
        for (int i = 0; i < 2; ++i) {
          const int hl = 2 * c.nh + i;
          const v4u Akv = ld128(L, L_SCR + hl * 1024 + lane * 16), Az = ld128(L, L_SCR + 4096 + hl * 64 + g * 16);
#pragma unroll
          for (int tt = 0; tt < 2; ++tt) {
            const f4 num = mfma32(Akv, phq[i][tt], z4), den = mfma32(Az, phq[i][tt], z4);
            const float inv = __builtin_amdgcn_rcpf(den[0] + 1e-6f);
            ob[i][tt] = (v2u){pk(num[0] * inv, num[1] * inv), pk(num[2] * inv, num[3] * inv)};
          }
        }
        const unsigned ks = 56 + 2 * c.hq + c.nh;
        __builtin_amdgcn_raw_buffer_store_b128((v4u){ob[0][0].x, ob[0][0].y, ob[1][0].x, ob[1][0].y}, c.rs, WS_H + (gt0 * 72 + ks) * 1024 + lane * 16, 0, AUX_SC1);
        __builtin_amdgcn_raw_buffer_store_b128((v4u){ob[0][1].x, ob[0][1].y, ob[1][1].x, ob[1][1].y}, c.rs, WS_H + (gt1 * 72 + ks) * 1024 + lane * 16, 0, AUX_SC1);
      }
    }
  };
  phase2_step(std::integral_constant<int, 0>{});
  phase2_step(std::integral_constant<int, 1>{});
  phase2_step(std::integral_constant<int, 2>{});
  phase2_step(std::integral_constant<int, 3>{});
  phase2_step(std::integral_constant<int, 4>{});
  phase2_step(std::integral_constant<int, 5>{});
  STAMP(c, 7);
  phase2_step(std::integral_constant<int, 6>{});
  phase2_step(std::integral_constant<int, 7>{});
  phase2_step(std::integral_constant<int, 8>{});
  phase2_step(std::integral_constant<int, 9>{});
  phase2_step(std::integral_constant<int, 10>{});
  phase2_step(std::integral_constant<int, 11>{});
  STAMP(c, 8);
  phase2_step(std::integral_constant<int, 12>{});
  phase2_step(std::integral_constant<int, 13>{});
  phase2_step(std::integral_constant<int, 14>{});
  phase2_step(std::integral_constant<int, 15>{});
  phase2_step(std::integral_constant<int, 16>{});
  phase2_step(std::integral_constant<int, 17>{});
  STAMP(c, 9);

#pragma unroll
  for (int t = 0; t < 4; ++t) accF[t] = mfma32(fwb[t], hprev, accF[t]);
  asm volatile("s_waitcnt lgkmcnt(0)" ::: "memory");
  bar();
#pragma unroll
  for (int t = 0; t < 4; ++t) *(LAS f4*)(L + L_RING + c.nh * 17408 + (16 * c.m + l15) * 272 + (16 * t + 4 * g) * 4) = accF[t];
  asm volatile("s_waitcnt vmcnt(0) lgkmcnt(0)" : "+v"(xres)::"memory");
  bar();
  {
    const int tok = c.tid >> 3, cc = c.tid & 7;
    const unsigned a = L_RING + tok * 272 + cc * 32;
    f4 s0 = ldf4(L, a) + ldf4(L, a + 17408), s1 = ldf4(L, a + 16) + ldf4(L, a + 16 + 17408);
    const v4u bb = ld128(L, L_B2 + cc * 16);
    v4u o;
    o.x = pk(s0[0] + bflo(xres.x) + bflo(bb.x), s0[1] + bfhi(xres.x) + bfhi(bb.x));
    o.y = pk(s0[2] + bflo(xres.y) + bflo(bb.y), s0[3] + bfhi(xres.y) + bfhi(bb.y));
    o.z = pk(s1[0] + bflo(xres.z) + bflo(bb.z), s1[1] + bfhi(xres.z) + bfhi(bb.z));
    o.w = pk(s1[2] + bflo(xres.w) + bflo(bb.w), s1[3] + bfhi(xres.w) + bfhi(bb.w));
    *(v4u*)((char*)out + (size_t)(64 * c.rt + tok) * 1024 + c.ct * 128 + cc * 16) = o;
  }
  if (blk == 0 && c.tid == 0) *(unsigned*)(ws + WS_EPOCH) = c.tag;
  STAMP(c, 10);
}

}

extern "C" void kernel_launch(void* const* d_in, const int* in_sizes, int n_in, void* d_out, int out_size, void* d_ws, size_t ws_size,
                              hipStream_t stream) {
  (void)in_sizes; (void)n_in; (void)out_size; (void)ws_size;
  constexpr unsigned LDSB = hh::L_END;
  (void)hipFuncSetAttribute((const void*)hh::fused_kernel, hipFuncAttributeMaxDynamicSharedMemorySize, LDSB);
  hh::fused_kernel<<<256, 512, LDSB, stream>>>((const char*)d_in[0], (const char*)d_in[1], (const char*)d_in[2], (const char*)d_in[3],
                                                    (const char*)d_in[5], (const char*)d_in[6], (const char*)d_in[7], (const char*)d_in[8],
                                                    (unsigned short*)d_out, (char*)d_ws);
}
```

```cpp
#include <hip/hip_runtime.h>
#include <stdint.h>
#include <type_traits>

namespace hh {

#define LAS __attribute__((address_space(3)))
typedef unsigned v4u __attribute__((ext_vector_type(4)));
typedef unsigned v2u __attribute__((ext_vector_type(2)));
typedef float f4 __attribute__((ext_vector_type(4)));
typedef float f2 __attribute__((ext_vector_type(2)));
typedef short s4 __attribute__((ext_vector_type(4)));
typedef __bf16 b8 __attribute__((ext_vector_type(8)));
typedef __bf16 b2 __attribute__((ext_vector_type(2)));
template <int V> using IC = std::integral_constant<int, V>;

constexpr unsigned WS_EPOCH = 0;
constexpr unsigned WS_FLAG = 256;
constexpr unsigned WS_STATS = 8192;
constexpr unsigned WS_CS = WS_STATS + 2048 * 16;
constexpr unsigned PART_BYTES = 17408;
constexpr unsigned WS_P = WS_CS + 3328 * 32;
constexpr unsigned WS_R = WS_P + 8 * 16 * PART_BYTES;
constexpr unsigned WS_H = (WS_R + 8 * PART_BYTES + 4095u) & ~4095u;
constexpr unsigned WS_DBG = WS_H + 128 * 72 * 1024;
constexpr unsigned WS_END = WS_DBG;

constexpr int NSLOT = 7;
constexpr unsigned L_RING = 0;
constexpr unsigned L_SCR = NSLOT * 16384;
constexpr unsigned SCRW = 5248;
constexpr unsigned L_CS = L_SCR + 8 * SCRW;
constexpr unsigned L_ST = L_CS + 1024;
constexpr unsigned L_B1 = L_ST + 1024;
constexpr unsigned L_WPHI = L_B1 + 512;
constexpr unsigned L_B2 = L_WPHI + 2048;
constexpr unsigned L_DUMMY = L_B2 + 256;
constexpr unsigned L_END = L_DUMMY + 256;
static_assert(L_END <= 163840, "LDS");

constexpr int NITEM = 39, NSTEP = 32;
constexpr int ST_E1 = 8, ST_E2 = 13, ST_F0 = 14;
constexpr int IT_P0 = 16, IT_F0 = 20, IT_S = 21;
constexpr int item_of_F(int c) { return c == 0 ? IT_F0 : IT_S + c; }
#define HPRE_N 4
constexpr int HPRE = HPRE_N;
constexpr int DH = 3;
constexpr int ST_G1_ISSUE = 5, ST_G1_CHECK = 8;
#define PUBA_STEP 10
constexpr int ST_PUBA = PUBA_STEP;
constexpr int ST_POLLA_ISSUE = 12;
constexpr int ST_R_ISSUE = 11;
constexpr int ST_PUBB = 15;
constexpr int E2_OPS_AFTER_STORES = 4 + 2 * (HPRE - 4);
constexpr int ST_POLLB_ISSUE = 17, ST_POLLB_CHECK = 19;
constexpr int ST_RG_ISSUE = 13, ST_RG_CHECK = 16;
constexpr int ST_O = 17;
constexpr int ST_PUBO = 20;
constexpr int ST_POLLO_ISSUE = 23, ST_POLLO_CHECK = 25;
static_assert(ST_F0 + 8 - DH == ST_POLLB_CHECK && ST_F0 + 14 - DH == ST_POLLO_CHECK, "flag checks sit in front of the first dependent loads");

struct Sched {
  int slot[NITEM], issue[NITEM], first[NITEM], last[NITEM];
  int cum[NSTEP + 1];
  int pos_item[2][NITEM];
  int pos_h[2][18];
  int h_issue[18];
  int pos_g1[2];
};
constexpr int PRO_REG_LAST = 6;
constexpr Sched make_sched() {
  Sched s{};
  for (int i = 0; i < NITEM; ++i) {
    if (i < 16) { s.first[i] = i >> 1; s.last[i] = i >> 1; }
    else if (i < IT_F0) { s.first[i] = 9 + (i - IT_P0); s.last[i] = s.first[i]; }
    else if (i == IT_F0) { s.first[i] = ST_F0; s.last[i] = ST_F0; }
    else if (i == IT_S) { s.first[i] = 9; s.last[i] = 12; }
    else { s.first[i] = ST_F0 + (i - IT_S); s.last[i] = s.first[i]; }
  }
  s.last[15] = ST_E1;
  for (int c = 0; c < 18; ++c) s.h_issue[c] = c < HPRE ? ST_E2 : c < 8 ? ST_F0 + c - HPRE : ST_F0 + c - DH;
  int n = 0;
  int next = 0;
  int owner[NSLOT] = {};
  for (int k = 0; k < NSLOT; ++k) {
    s.slot[next] = k; s.issue[next] = -1; owner[k] = next; n += 2; s.pos_item[0][next] = s.pos_item[1][next] = n - 1; ++next;
    if (k == 1) n += 4;
  }
  for (int st = 0; st < NSTEP; ++st) {
    s.cum[st] = n;
    const int first_new = next;
    for (int rep = 0; rep < NSLOT; ++rep) {
      int best = -1;
      for (int k = 0; k < NSLOT; ++k)
        if (owner[k] >= 0 && s.last[owner[k]] < st && (best < 0 || owner[k] < owner[best])) best = k;
      if (best < 0) break;
      if (next < NITEM) { s.slot[next] = best; s.issue[next] = st; owner[best] = next; ++next; }
      else owner[best] = -1;
    }
    int nreg = 0;
    for (int c = 0; c < 18; ++c) if (s.h_issue[c] == st) nreg += 2;
    if (st == ST_G1_ISSUE) nreg += 2;
    const int nring = 2 * (next - first_new);
    for (int o = 0; o < 2; ++o) {
      int p = n + (o == 0 ? 0 : nreg);
      for (int i = first_new; i < next; ++i) { p += 2; s.pos_item[o][i] = p - 1; }
      p = n + (o == 0 ? nring : 0);
      for (int c = 0; c < 18; ++c) if (s.h_issue[c] == st) { p += 2; s.pos_h[o][c] = p - 1; }
      if (st == ST_G1_ISSUE) { p += 2; s.pos_g1[o] = p - 1; }
    }
    n += nring + nreg;
  }
  s.cum[NSTEP] = n;
  return s;
}
constexpr Sched SC = make_sched();
constexpr int imin(int a, int b) { return a < b ? a : b; }
constexpr int imax(int a, int b) { return a > b ? a : b; }
constexpr int top_wait(int st) {
  int need = -1;
  for (int o = 0; o < 2; ++o) {
    for (int i = 0; i < NITEM; ++i) if (SC.first[i] == st) need = imax(need, SC.pos_item[o][i]);
    if (st >= ST_F0) need = imax(need, SC.pos_h[o][st - ST_F0]);
    if (st == ST_G1_CHECK) need = imax(need, SC.pos_g1[o]);
  }
  int n = need < 0 ? 63 : SC.cum[st] - 1 - need;
  if (st == 9) n += 2;
  if (st == ST_PUBA) n = imin(n, SC.cum[st] - SC.cum[ST_E1 + 1]);
  if (st == ST_PUBB) n = imin(n, SC.cum[st] - SC.cum[ST_E2 + 1] + E2_OPS_AFTER_STORES);
  if (st == ST_PUBO) n = imin(n, SC.cum[st] - SC.cum[ST_O + 1]);
  return n > 63 ? 63 : n;
}
constexpr int extra_wait(int a, int b) { int n = SC.cum[b] - SC.cum[a + 1]; return n > 63 ? 63 : n; }
constexpr int h_at(int st) {
  for (int c = 0; c < 18; ++c) if (SC.h_issue[c] == st) return c;
  return -1;
}
constexpr int nth_item(int st, int k) {
  for (int i = 0; i < NITEM; ++i) if (SC.issue[i] == st) { if (k == 0) return i; --k; }
  return -1;
}
static_assert(nth_item(ST_E1, 0) == IT_S && nth_item(ST_E1, 1) < 0 && nth_item(ST_E2, 2) < 0 && nth_item(9, 1) < 0 && nth_item(10, 1) < 0 && nth_item(11, 1) < 0 && nth_item(12, 1) < 0, "schedule shape");

__host__ __device__ constexpr int unit_pos(int q, int off) { return off >= 2 && off <= 5 ? 4 * q + (off - 2) : 16 + 3 * q + (off == 6 ? 2 : off); }
__host__ __device__ constexpr int pos_unit(int pos) { return pos < 16 ? 7 * (pos >> 2) + 2 + (pos & 3) : 7 * ((pos - 16) / 3) + (((pos - 16) % 3) == 2 ? 6 : (pos - 16) % 3); }

#define DI __device__ __forceinline__
DI void glds16(const void* sbase, unsigned voff, unsigned lds) {
  asm volatile("s_mov_b32 m0, %2\n\ts_nop 0\n\tglobal_load_lds_dwordx4 %0, %1" ::"v"(voff), "s"(sbase), "s"(lds) : "memory");
}
DI void set_m0(unsigned lds) { asm volatile("s_mov_b32 m0, %0" ::"s"(lds) : "memory"); }
DI void glds16_m0(const void* sbase, unsigned voff) {
  asm volatile("global_load_lds_dwordx4 %0, %1" ::"v"(voff), "s"(sbase) : "memory");
}
DI void glds4(const void* sbase, unsigned voff, unsigned lds) {
  asm volatile("s_mov_b32 m0, %2\n\ts_nop 0\n\tglobal_load_lds_dword %0, %1" ::"v"(voff), "s"(sbase), "s"(lds) : "memory");
}
DI void glds4v(const void* vaddr, unsigned lds) {
  asm volatile("s_mov_b32 m0, %1\n\ts_nop 0\n\tglobal_load_lds_dword %0, off" ::"v"(vaddr), "s"(lds) : "memory");
}
DI void gld16(v4u& d, const void* sbase, unsigned voff) {
  asm volatile("global_load_dwordx4 %0, %1, %2" : "=v"(d) : "v"(voff), "s"(sbase) : "memory");
}
DI void gld16_sc1(v4u& d, const void* sbase, unsigned voff) {
  asm volatile("global_load_dwordx4 %0, %1, %2 sc1" : "=v"(d) : "v"(voff), "s"(sbase) : "memory");
}
template <int OFF> DI void gld16_sc1_off(v4u& d, const void* sbase, unsigned voff) {
  asm volatile("global_load_dwordx4 %0, %1, %2 offset:%3 sc1" : "=v"(d) : "v"(voff), "s"(sbase), "n"(OFF) : "memory");
}
DI void gld4_sc1(unsigned& d, const void* sbase, unsigned voff) {
  asm volatile("global_load_dword %0, %1, %2 sc1" : "=v"(d) : "v"(voff), "s"(sbase) : "memory");
}
template <int N> DI void wait_top() { asm volatile("s_waitcnt vmcnt(%0) lgkmcnt(0)" ::"n"(N) : "memory"); }
template <int N> DI void wait_vm() { asm volatile("s_waitcnt vmcnt(%0)" ::"n"(N) : "memory"); }
DI void wait_lgkm() { asm volatile("s_waitcnt lgkmcnt(0)" ::: "memory"); }
DI void bar() { asm volatile("" ::: "memory"); __builtin_amdgcn_s_barrier(); asm volatile("" ::: "memory"); }
#define FENCE() __builtin_amdgcn_sched_barrier(0)

DI unsigned pk(float a, float b) { b2 r = {(__bf16)a, (__bf16)b}; return __builtin_bit_cast(unsigned, r); }
DI float bflo(unsigned u) { return __builtin_bit_cast(float, u << 16); }
DI float bfhi(unsigned u) { return __builtin_bit_cast(float, u & 0xffff0000u); }
DI unsigned fbits(float f) { return __builtin_bit_cast(unsigned, f); }
DI float bitsf(unsigned u) { return __builtin_bit_cast(float, u); }
DI f4 mfma32(v4u a, v4u b, f4 c) { return __builtin_amdgcn_mfma_f32_16x16x32_bf16(__builtin_bit_cast(b8, a), __builtin_bit_cast(b8, b), c, 0, 0, 0); }
DI f4 mfma16(v2u a, v2u b, f4 c) { return __builtin_amdgcn_mfma_f32_16x16x16bf16_1k(__builtin_bit_cast(s4, a), __builtin_bit_cast(s4, b), c, 0, 0, 0); }
DI v2u trd(LAS char* L, unsigned a) { return __builtin_bit_cast(v2u, __builtin_amdgcn_ds_read_tr16_b64_v4i16((LAS s4*)(L + a))); }
DI v4u trd2(LAS char* L, unsigned a0, unsigned a1) { v2u x = trd(L, a0), y = trd(L, a1); return (v4u){x.x, x.y, y.x, y.y}; }
DI v4u ld128(LAS char* L, unsigned a) { return *(LAS v4u*)(L + a); }
DI f4 ldf4(LAS char* L, unsigned a) { return *(LAS f4*)(L + a); }
DI f2 ldf2(LAS char* L, unsigned a) { return *(LAS f2*)(L + a); }
DI v2u ld64(LAS char* L, unsigned a) { return *(LAS v2u*)(L + a); }

template <int CTRL> DI float dpp(float v) { return bitsf((unsigned)__builtin_amdgcn_update_dpp(0, (int)fbits(v), CTRL, 0xf, 0xf, true)); }
DI void sw32(float& a, float& b) { auto r = __builtin_amdgcn_permlane32_swap(fbits(a), fbits(b), false, false); a = bitsf((unsigned)r[0]); b = bitsf((unsigned)r[1]); }
DI void sw16(float& a, float& b) { auto r = __builtin_amdgcn_permlane16_swap(fbits(a), fbits(b), false, false); a = bitsf((unsigned)r[0]); b = bitsf((unsigned)r[1]); }
DI float xsum(float v) { float a = v, b = v; sw16(a, b); v = a + b; a = v; b = v; sw32(a, b); return a + b; }
DI float rowsum16(float v) { v += dpp<0x128>(v); v += dpp<0x124>(v); v += dpp<0x122>(v); v += dpp<0x121>(v); return v; }
DI float wavesum(float v) { return xsum(rowsum16(v)); }

constexpr float LOG2E = 1.4426950408889634f;
DI float vmax(float a, float b) { return __builtin_elementwise_maximum(a, b); }
DI float vmin(float a, float b) { return __builtin_elementwise_minimum(a, b); }
template <class OP> DI float fold4(float a0, float a1, float a2, float a3, OP op) {
  sw32(a0, a1); sw32(a2, a3);
  float m01 = op(a0, a1), m23 = op(a2, a3);
  sw16(m01, m23);
  return op(m01, m23);
}
DI void unfold4(float m, float (&o)[4]) {
  float a = m, b = m;
  sw16(a, b);
  float a2 = a, b2 = b;
  sw32(a, a2); sw32(b, b2);
  o[0] = a; o[1] = a2; o[2] = b; o[3] = b2;
}
struct Feat { float a[4], b[4]; f2 ep[4][2], en[4][2]; };
DI void tie8(f2 (&a)[4][2]) { asm volatile("" : "+v"(a[0][0]), "+v"(a[0][1]), "+v"(a[1][0]), "+v"(a[1][1]), "+v"(a[2][0]), "+v"(a[2][1]), "+v"(a[3][0]), "+v"(a[3][1])); }
DI void feat_max(const f4 (&p)[4], Feat& f) {
  float mx[4], mn[4];
#pragma unroll
  for (int t = 0; t < 4; ++t) {
    mx[t] = vmax(vmax(vmax(p[t][0], p[t][1]), p[t][2]), p[t][3]);
    mn[t] = vmin(vmin(vmin(p[t][0], p[t][1]), p[t][2]), p[t][3]);
  }
  const float MX = fold4(mx[0], mx[1], mx[2], mx[3], [](float a, float b) { return vmax(a, b); }) * -LOG2E;
  const float MN = fold4(mn[0], mn[1], mn[2], mn[3], [](float a, float b) { return vmin(a, b); }) * LOG2E;
  unfold4(MX, f.a);
  unfold4(MN, f.b);
}
DI void feat_exp(const f4 (&p)[4], Feat& f) {
#pragma unroll
  for (int t = 0; t < 4; ++t)
#pragma unroll
    for (int h = 0; h < 2; ++h) {
      const f2 v = {p[t][2 * h], p[t][2 * h + 1]};
      f.ep[t][h] = __builtin_elementwise_fma(v, (f2){LOG2E, LOG2E}, (f2){f.a[t], f.a[t]});
      f.en[t][h] = __builtin_elementwise_fma(v, (f2){-LOG2E, -LOG2E}, (f2){f.b[t], f.b[t]});
    }
  tie8(f.ep); tie8(f.en);
#pragma unroll
  for (int t = 0; t < 4; ++t)
#pragma unroll
    for (int h = 0; h < 2; ++h) {
      f.ep[t][h] = (f2){__builtin_amdgcn_exp2f(f.ep[t][h].x), __builtin_amdgcn_exp2f(f.ep[t][h].y)};
      f.en[t][h] = (f2){__builtin_amdgcn_exp2f(f.en[t][h].x), __builtin_amdgcn_exp2f(f.en[t][h].y)};
    }
  tie8(f.ep); tie8(f.en);
}
DI void feat_norm(const Feat& f, v2u (&pos)[4], v2u (&neg)[4]) {
  float sp[4], sn[4];
#pragma unroll
  for (int t = 0; t < 4; ++t) {
    const f2 a = f.ep[t][0] + f.ep[t][1], b = f.en[t][0] + f.en[t][1];
    sp[t] = a.x + a.y;
    sn[t] = b.x + b.y;
  }
  const float IP = __builtin_amdgcn_rcpf(fold4(sp[0], sp[1], sp[2], sp[3], [](float a_, float b_) { return a_ + b_; }));
  const float IN = __builtin_amdgcn_rcpf(fold4(sn[0], sn[1], sn[2], sn[3], [](float a_, float b_) { return a_ + b_; }));
  float ip[4], in[4];
  unfold4(IP, ip);
  unfold4(IN, in);
#pragma unroll
  for (int t = 0; t < 4; ++t) {
    const f2 a0 = f.ep[t][0] * ip[t], a1 = f.ep[t][1] * ip[t], b0 = f.en[t][0] * in[t], b1 = f.en[t][1] * in[t];
    pos[t] = (v2u){pk(a0.x, a0.y), pk(a1.x, a1.y)};
    neg[t] = (v2u){pk(b0.x, b0.y), pk(b1.x, b1.y)};
  }
}

template <unsigned OFF> DI unsigned win(unsigned base) {
  if constexpr (OFF >= 65536u - 16384u) { unsigned b = base + (OFF & ~16383u); asm volatile("" : "+v"(b)); return b; }
  else return base;
}
template <unsigned OFF> constexpr unsigned rem() { return OFF >= 65536u - 16384u ? (OFF & 16383u) : OFF; }
constexpr unsigned HIWIN = 49152;
#define AUX_SC1 16
constexpr unsigned SPIN_CAP = 1u << 16;

struct Ctx {
  LAS char* L;
  unsigned lbase;
  const char *x, *wo, *w2;
  char* ws;
  __amdgpu_buffer_rsrc_t rs;
  unsigned tag;
  int tid, lane, w, m, nh;
  int rg, q, jj, hq, isQ, rt, ct;
  const char *u0, *u1, *u2, *ush;
  unsigned ld1, ld2;
  unsigned vX0, vX1, vW0, vW1, vP0, vP1, vF0, vF1;
  const char *pX, *pW, *pP, *pF1, *pF5, *pWo;
  const char* pH;
  unsigned vH;
  unsigned ldsw;
};

#define TS(i) do { } while (0)
#define TSX(i) do { } while (0)
#define STAMP(c, i) do { } while (0)

template <int I, int U> DI void half_m0(const Ctx& c) { set_m0(c.ldsw + SC.slot[I] * 16384 + U * 1024); }
template <int I, int U> DI void half_go(const Ctx& c) {
  if constexpr (I < 16 && (I & 1) == 0) {
    glds16_m0(c.pX + (I >> 1) * 128, U ? c.vX1 : c.vX0);
  } else if constexpr (I < 16) {
    glds16_m0(c.pW + (size_t)(64 * (I >> 1)) * c.ld1, U ? c.vW1 : c.vW0);
  } else if constexpr (I == IT_S) {
    const unsigned rr = c.lane >> 1;
    glds16_m0(c.ush + (size_t)(64 * c.w + 32 * U) * 3584, (rr ^ ((rr & 8) >> 1)) * 3584 + (c.lane & 1) * 16);
  } else if constexpr (I < IT_F0) {
    glds16_m0(c.pP + (size_t)(128 * (I - IT_P0)) * c.ld2, U ? c.vP1 : c.vP0);
  } else {
    constexpr int C = I == IT_F0 ? 0 : I - IT_S;
    if constexpr (C < 14) {
      constexpr int d = pos_unit(2 * C + 1) - pos_unit(2 * C);
      static_assert(d == 1 || d == 5, "unit distance inside a chunk");
      glds16_m0((d == 1 ? c.pF1 : c.pF5) + pos_unit(2 * C) * 65536, U ? c.vF1 : c.vF0);
    } else {
      glds16_m0(c.pWo + (C - 14) * 131072, U ? c.vF1 : c.vF0);
    }
  }
}
template <int I, int U> DI void issue_half(const Ctx& c) { half_m0<I, U>(c); asm volatile("s_nop 0"); half_go<I, U>(c); }
template <int ST, int K> DI void op_m0(const Ctx& c) { constexpr int I = nth_item(ST, K >> 1); if constexpr (I >= 0) half_m0<I, (K & 1)>(c); }
template <int ST, int K> DI void op_go(const Ctx& c) { constexpr int I = nth_item(ST, K >> 1); if constexpr (I >= 0) half_go<I, (K & 1)>(c); }
template <int ST, int K> DI void issue_op(const Ctx& c) { constexpr int I = nth_item(ST, K >> 1); if constexpr (I >= 0) issue_half<I, (K & 1)>(c); }
template <int C> DI void issue_h0(const Ctx& c, v4u (&hf)[18][2]) { gld16_sc1_off<0>(hf[C][0], c.pH + C * 4096, c.vH); }
template <int C> DI void issue_h1(const Ctx& c, v4u (&hf)[18][2]) { gld16_sc1_off<1024>(hf[C][1], c.pH + C * 4096, c.vH); }
template <int C> DI void issue_h(const Ctx& c, v4u (&hf)[18][2]) { issue_h0<C>(c, hf); issue_h1<C>(c, hf); }

DI void spin_flags(const Ctx& c, const char* line, int n) {
#pragma unroll 1
  for (unsigned it = 0; it < SPIN_CAP; ++it) {
    unsigned v;
    gld4_sc1(v, line, (unsigned)(c.lane & 31) * 4);
    asm volatile("s_waitcnt vmcnt(0)" : "+v"(v)::"memory");
    if (__all(v == c.tag || (c.lane & 31) >= n)) return;
    __builtin_amdgcn_s_sleep(2);
  }
}
DI bool tags_ok(v4u a, unsigned tag) { return a.y == tag && a.w == tag; }

template <int N> DI void tie(f2 (&v)[N]) {
  if constexpr (N == 2) asm volatile("" : "+v"(v[0]), "+v"(v[1]));
  else if constexpr (N == 4) asm volatile("" : "+v"(v[0]), "+v"(v[1]), "+v"(v[2]), "+v"(v[3]));
  else static_assert(N == 2 || N == 4, "tie");
}
template <int N> DI void gelu_n(f2 (&u)[N]) {
  const float c0 = -2.0f * LOG2E * 0.7978845608028654f, c1 = c0 * 0.044715f;
  f2 e[N];
  tie(u);
#pragma unroll
  for (int r = 0; r < N; ++r) e[r] = u[r] * u[r];
#pragma unroll
  for (int r = 0; r < N; ++r) e[r] = __builtin_elementwise_fma(e[r], (f2){c1, c1}, (f2){c0, c0});
#pragma unroll
  for (int r = 0; r < N; ++r) e[r] = u[r] * e[r];
  tie(e);
#pragma unroll
  for (int r = 0; r < N; ++r) e[r] = (f2){__builtin_amdgcn_exp2f(e[r].x), __builtin_amdgcn_exp2f(e[r].y)};
  tie(e);
#pragma unroll
  for (int r = 0; r < N; ++r) e[r] = e[r] + (f2){1.0f, 1.0f};
  tie(e);
#pragma unroll
  for (int r = 0; r < N; ++r) e[r] = (f2){__builtin_amdgcn_rcpf(e[r].x), __builtin_amdgcn_rcpf(e[r].y)};
  tie(e);
#pragma unroll
  for (int r = 0; r < N; ++r) u[r] *= e[r];
}
DI void ln_bias(f2* u, f4 a, f4 cs, v2u bb, f2 st) {
  const f2 b0 = {bflo(bb.x), bfhi(bb.x)}, b1 = {bflo(bb.y), bfhi(bb.y)}, sx = {st.x, st.x}, sy = {-st.y, -st.y};
  u[0] = __builtin_elementwise_fma((f2){a[0], a[1]}, sx, __builtin_elementwise_fma(sy, (f2){cs[0], cs[1]}, b0));
  u[1] = __builtin_elementwise_fma((f2){a[2], a[3]}, sx, __builtin_elementwise_fma(sy, (f2){cs[2], cs[3]}, b1));
}
DI v2u ffn_tile(f4 a, f4 cs, v2u bb, f2 st) {
  f2 u[2];
  ln_bias(u, a, cs, bb, st);
  gelu_n<2>(u);
  return (v2u){pk(u[0].x, u[0].y), pk(u[1].x, u[1].y)};
}
DI v4u ffn_pair(f4 a0, f4 a1, f4 cs0, f4 cs1, v2u bb0, v2u bb1, f2 st) {
  f2 u[4];
  ln_bias(u, a0, cs0, bb0, st);
  ln_bias(u + 2, a1, cs1, bb1, st);
  gelu_n<4>(u);
  return (v4u){pk(u[0].x, u[0].y), pk(u[1].x, u[1].y), pk(u[2].x, u[2].y), pk(u[3].x, u[3].y)};
}
DI v2u ln_pack(f4 a, f4 cs, f2 st) {
  float v[4];
#pragma unroll
  for (int r = 0; r < 4; ++r) v[r] = __builtin_fmaf(a[r], st.x, -st.y * cs[r]);
  return (v2u){pk(v[0], v[1]), pk(v[2], v[3])};
}

__global__ __launch_bounds__(512, 2) void fused_kernel(const char* __restrict__ x, const char* __restrict__ wqkv, const char* __restrict__ wphi,
                                                       const char* __restrict__ wo, const char* __restrict__ w1, const char* __restrict__ b1,
                                                       const char* __restrict__ w2, const char* __restrict__ b2, unsigned short* __restrict__ out,
                                                       char* __restrict__ ws) {
  extern __shared__ __attribute__((aligned(16))) char smem[];
  Ctx c;
  c.L = (LAS char*)smem;
  c.lbase = (unsigned)(size_t)c.L;
  c.x = x; c.wo = wo; c.w2 = w2; c.ws = ws;
  c.tid = threadIdx.x; c.lane = c.tid & 63; c.w = __builtin_amdgcn_readfirstlane(c.tid >> 6); c.m = c.w & 3; c.nh = c.w >> 2;
  const int blk = blockIdx.x, xcd = blk & 7, idx = blk >> 3;
  c.rg = (xcd >> 2) * 8 + (idx >> 2); c.q = xcd & 3; c.jj = idx & 3; c.isQ = c.jj >> 1; c.hq = 2 * c.q + (c.jj & 1);
  c.rt = xcd * 4 + (idx >> 3); c.ct = idx & 7;
  c.rs = __builtin_amdgcn_make_buffer_rsrc((void*)ws, 0, WS_END, 0x00020000);
  int lane = c.lane, g = lane >> 4, l15 = lane & 15, q4 = l15 >> 2, p4 = lane & 3;
#define REFRESH() do { int t_ = threadIdx.x; asm volatile("" : "+v"(t_)); c.tid = t_; lane = t_ & 63; c.lane = lane; g = lane >> 4; l15 = lane & 15; q4 = l15 >> 2; p4 = lane & 3; } while (0)
  LAS char* L = c.L;
  STAMP(c, 0);
  asm volatile("" ::"s"(x), "s"(wqkv), "s"(wphi), "s"(wo), "s"(w1), "s"(b1), "s"(w2), "s"(b2), "s"(out), "s"(ws));
  const int f0u = 7 * c.q;
  const int fa = f0u + (c.isQ ? 2 + 2 * (c.jj & 1) : c.jj);
  c.u0 = c.isQ ? w1 + fa * 128 : wqkv + (8 + c.hq) * 128;
  c.u1 = c.u0 + (c.isQ ? 128 : 1024);
  c.u2 = c.isQ ? wqkv + c.hq * 128 : w1 + fa * 128;
  c.ld1 = c.isQ ? 3584 : 3072;
  c.ld2 = c.isQ ? 3072 : 3584;
  c.ush = w1 + (f0u + 6) * 128 + c.jj * 32;
  c.vX0 = (lane >> 3) * 1024 + (((lane & 7) ^ (lane >> 4)) * 16);
  c.vX1 = (c.vX0 ^ 64u) + 8 * 1024;
  c.vW0 = (lane >> 3) * c.ld1 + (((lane & 7) ^ ((lane >> 3) & 2)) * 16);
  c.vW1 = (c.vW0 ^ 64u) + 8 * c.ld1;
  c.pX = x + (size_t)(128 * c.rg + 16 * c.w) * 1024;
  c.pW = (c.nh ? c.u1 : c.u0) + (size_t)(16 * c.m) * c.ld1;
  c.ldsw = c.lbase + L_RING + c.w * 2048;

  issue_half<0, 0>(c); issue_half<0, 1>(c); issue_half<1, 0>(c); issue_half<1, 1>(c);
  c.tag = *(const __attribute__((address_space(4))) unsigned*)(ws + WS_EPOCH) + 1u;
  v4u xr, cw0, cw1;
  const int j16 = 4 * c.q + c.jj;
  gld16(xr, x + (size_t)(128 * c.rg + 8 * j16 + c.w) * 1024, lane * 16);
  const int job = (xcd >> 2) * 32 + idx;
  const int jobc = job < 52 ? job : 51;
  const int li = jobc >> 2, kq = jobc & 3;
  const int gunit = li < 6 ? (li >> 1) * 8 + 2 * c.q + (li & 1) : 24 + f0u + (li - 6);
  {
    const char* wb = li < 6 ? wqkv + gunit * 128 : w1 + (gunit - 24) * 128;
    const unsigned ld = li < 6 ? 3072 : 3584;
    const char* bb = wb + (size_t)(128 * kq + 16 * c.w) * ld;
    const unsigned v = (lane >> 3) * ld + (lane & 7) * 16;
    gld16(cw0, bb, v);
    gld16(cw1, bb + 8 * ld, v);
  }
  {
    const char* p = c.w == 0 ? b1 + fa * 128 : c.w == 1 ? b1 + (f0u + 6) * 128 + c.jj * 32 : c.w < 4 ? wphi + c.hq * 2048 + (c.w - 2) * 1024 : b2 + c.ct * 128;
    const unsigned d = c.lbase + (c.w == 0 ? L_B1 : c.w == 1 ? L_B1 + 256 : c.w < 4 ? L_WPHI + (c.w - 2) * 1024 : L_B2);
    if (c.w == 2 || c.w == 3) glds16(p, lane * 16, d);
    else glds4(p, (lane & (c.w == 0 ? 63 : c.w == 1 ? 7 : 31)) * 4, d);
  }
  issue_half<2, 0>(c); issue_half<2, 1>(c); issue_half<3, 0>(c); issue_half<3, 1>(c); issue_half<4, 0>(c); issue_half<4, 1>(c);
  issue_half<5, 0>(c); issue_half<5, 1>(c); issue_half<6, 0>(c); issue_half<6, 1>(c);
  {
    const int id = c.tid, half = id & 1, ln = id >> 1;
    const char* ax; const char* aw;
    { const int r = (ln * 205) >> 10, sg = ln - 5 * r; ax = x + (size_t)(128 * c.rg + 32 * c.jj + r) * 1024 + (3 + sg) * 128; }
    { const int lj = ln < 160 ? 0 : ln < 240 ? ln - 160 : 79, un = lj >= 40, r = lj - 40 * un; aw = (un ? c.u1 : c.u0) + (size_t)(192 + 40 * (c.rg & 7) + r) * c.ld1; }
    glds4v((c.w < 5 ? ax : aw) + half * 64, c.lbase + L_DUMMY);
  }
  unsigned g1off;
  {
    const int t = c.tid;
    const int ci = t - 128 < 208 ? t - 128 : 207;
    const int un = ci >> 6;
    const int ufa = 24 + fa;
    const int ua = c.isQ ? ufa : 8 + c.hq, ub = c.isQ ? ufa + 1 : 16 + c.hq, uc = c.isQ ? c.hq : ufa;
    const int gu = un == 0 ? ua : un == 1 ? ub : un == 2 ? uc : 24 + f0u + 6;
    const int col = gu * 64 + (un == 3 ? 16 * c.jj + (ci & 15) : (ci & 63));
    g1off = t < 128 ? WS_STATS + (128 * c.rg + t) * 16 : WS_CS + col * 32;
    asm volatile("" : "+v"(g1off));
  }
  STAMP(c, 1);

  v4u xf[2][16];
  f4 acc[2][2][2];
  const f4 z4 = {0.f, 0.f, 0.f, 0.f};
#pragma unroll
  for (int a = 0; a < 2; ++a)
#pragma unroll
    for (int b = 0; b < 2; ++b)
#pragma unroll
      for (int t = 0; t < 2; ++t) acc[a][b][t] = z4;
  const unsigned xa0 = (unsigned)((32 * c.m + l15) * 128 + ((g ^ (l15 >> 1)) * 16));
  const unsigned xtt0 = c.nh * 2048, xtt1 = (1 - c.nh) * 2048;
  const unsigned wa0 = (unsigned)((8 * g + q4) * 128 + (((4 * c.nh + (p4 >> 1)) ^ ((((q4 >> 1) & 1) | ((g & 1) << 1)) << 1)) * 16) + (p4 & 1) * 8);
  unsigned xb[2][4] = {{xa0 + xtt0, xa0 + xtt1, (xa0 ^ 64) + xtt0, (xa0 ^ 64) + xtt1}, {}}, wa[2][2] = {{wa0, wa0 ^ 32}, {}};
#pragma unroll
  for (int i = 0; i < 4; ++i) { xb[1][i] = xb[0][i] + HIWIN; asm volatile("" : "+v"(xb[1][i])); }
#pragma unroll
  for (int i = 0; i < 2; ++i) { wa[1][i] = wa[0][i] + HIWIN; asm volatile("" : "+v"(wa[1][i])); }
  v4u g1a, g1b;
  v4u wfa[2][2], wfb[2][2];
  auto pass1_step = [&](auto KC) {
    constexpr int kc = decltype(KC)::value;
    constexpr unsigned sx = L_RING + SC.slot[2 * kc] * 16384, sw = L_RING + SC.slot[2 * kc + 1] * 16384;
    TS(3 * kc);
    if constexpr (kc > 0)
      asm volatile("" : "+v"(wfb[0][0]), "+v"(wfb[0][1]), "+v"(wfb[1][0]), "+v"(wfb[1][1]), "+v"(xf[0][kc > 0 ? 2 * kc - 1 : 0]), "+v"(xf[1][kc > 0 ? 2 * kc - 1 : 0]));
    if constexpr (kc == 0) {
      wait_top<top_wait(kc)>();
    } else {
      wait_vm<top_wait(kc)>();
    }
    TS(3 * kc + 1);
    bar();
    TS(3 * kc + 2);
    FENCE();
    if constexpr (kc == 5) {
      REFRESH();
      c.vP0 = (lane >> 3) * c.ld2 + (((lane & 7) ^ ((lane >> 3) & 2)) * 16);
      c.vP1 = (c.vP0 ^ 64u) + 8 * c.ld2;
      c.pP = c.u2 + (size_t)(16 * c.w) * c.ld2;
    }
    if constexpr (kc == 7) {
      REFRESH();
      c.vF0 = (lane >> 3) * 1024 + (((lane & 7) ^ ((lane >> 3) & 6)) * 16);
      c.vF1 = c.vF0 + 8 * 1024;
      const unsigned o = (16 * c.m) * 1024 + c.ct * 128;
      c.pF1 = w2 + o + c.nh * 65536; c.pF5 = w2 + o + c.nh * 5 * 65536; c.pWo = wo + o + c.nh * 65536;
    }
    constexpr int hx = sx >= HIWIN, hw = sw >= HIWIN;
    const unsigned x0 = xb[hx][0], x1 = xb[hx][1], x2 = xb[hx][2], x3 = xb[hx][3], w0 = wa[hw][0], w1_ = wa[hw][1];
    constexpr unsigned rx = sx - hx * HIWIN, rw = sw - hw * HIWIN;
#define P1_MMA(F, KS, UN, I, T) do { if constexpr (DOM_) acc[UN][I][T] = mfma32(F[UN][I], xf[T][KS], acc[UN][I][T]); FENCE(); } while (0)
#define P1_HALF(FR, FM, KSR, KSM, XA, XB, SOFF, G0, G1, DOM, TIE) {                                 \
    constexpr bool DOM_ = DOM;                                                                     \
    v2u t0, t1;                                                                                    \
    xf[0][KSR] = ld128(L, XA + rx); FENCE();                                                       \
    if constexpr (TIE) {                                                                           \
      xf[1][KSR] = ld128(L, XB + rx); FENCE();                                                     \
      t0 = trd(L, w0 + rw + SOFF); FENCE();                                                        \
      asm volatile("" : "+v"(FM[0][0]), "+v"(FM[0][1]), "+v"(FM[1][0]), "+v"(FM[1][1]), "+v"(xf[0][KSM]), "+v"(xf[1][KSM]));   \
      FENCE();                                                                                     \
      P1_MMA(FM, KSM, 0, 0, 0);                                                                    \
    } else {                                                                                       \
      P1_MMA(FM, KSM, 0, 0, 0);                                                                    \
      xf[1][KSR] = ld128(L, XB + rx); FENCE();                                                     \
      t0 = trd(L, w0 + rw + SOFF); FENCE();                                                        \
    }                                                                                              \
    P1_MMA(FM, KSM, 0, 0, 1);                                                                      \
    t1 = trd(L, w0 + rw + SOFF + 512); FR[0][0] = (v4u){t0.x, t0.y, t1.x, t1.y}; FENCE();          \
    op_m0<kc, G0>(c); FENCE();                                                                     \
    P1_MMA(FM, KSM, 0, 1, 0);                                                                      \
    FR[0][1] = trd2(L, w1_ + rw + SOFF, w1_ + rw + SOFF + 512); FENCE();                           \
    P1_MMA(FM, KSM, 0, 1, 1);                                                                      \
    t0 = trd(L, w0 + rw + SOFF + 8192); FENCE();                                                   \
    op_go<kc, G0>(c); FENCE();                                                                     \
    P1_MMA(FM, KSM, 1, 0, 0);                                                                      \
    t1 = trd(L, w0 + rw + SOFF + 8192 + 512); FR[1][0] = (v4u){t0.x, t0.y, t1.x, t1.y}; FENCE();   \
    op_m0<kc, G1>(c); FENCE();                                                                     \
    P1_MMA(FM, KSM, 1, 0, 1);                                                                      \
    FR[1][1] = trd2(L, w1_ + rw + SOFF + 8192, w1_ + rw + SOFF + 8192 + 512); FENCE();             \
    P1_MMA(FM, KSM, 1, 1, 0);                                                                      \
    op_go<kc, G1>(c); FENCE();                                                                     \
    P1_MMA(FM, KSM, 1, 1, 1);                                                                      \
  }
    P1_HALF(wfa, wfb, 2 * kc, (kc > 0 ? 2 * kc - 1 : 0), x0, x1, 0, 0, 1, (kc > 0), false)
    if constexpr (kc == 0) {
      asm volatile("s_waitcnt vmcnt(%3)" : "+v"(xr), "+v"(cw0), "+v"(cw1) : "n"(SC.cum[1] - 1 - PRO_REG_LAST) : "memory");
      const float x0v = bitsf(__builtin_amdgcn_readfirstlane(xr.x << 16));
      float d[8] = {bflo(xr.x) - x0v, bfhi(xr.x) - x0v, bflo(xr.y) - x0v, bfhi(xr.y) - x0v, bflo(xr.z) - x0v, bfhi(xr.z) - x0v, bflo(xr.w) - x0v, bfhi(xr.w) - x0v};
      float s = ((d[0] + d[1]) + (d[2] + d[3])) + ((d[4] + d[5]) + (d[6] + d[7]));
      float qq = d[0] * d[0];
#pragma unroll
      for (int e = 1; e < 8; ++e) qq = __builtin_fmaf(d[e], d[e], qq);
      s += dpp<0x128>(s); qq += dpp<0x128>(qq);
      s += dpp<0x124>(s); qq += dpp<0x124>(qq);
      s += dpp<0x122>(s); qq += dpp<0x122>(qq);
      s += dpp<0x121>(s); qq += dpp<0x121>(qq);
      sw32(s, qq);
      float sq = s + qq;
      { float a = sq, b = sq; sw16(a, b); sq = a + b; }
      const float sd = sq * (1.0f / 512.0f);
      const float m2 = bitsf(__builtin_amdgcn_readlane(fbits(sd), 32));
      if (lane == 0) {
        const float var = __builtin_fmaf(-sd, sd, m2);
        const float rstd = __builtin_amdgcn_rsqf(vmax(var, 0.f) + 1e-5f);
        __builtin_amdgcn_raw_buffer_store_b128((v4u){fbits(x0v + sd), c.tag, fbits(rstd), c.tag}, c.rs, WS_STATS + (128 * c.rg + 8 * j16 + c.w) * 16, 0, AUX_SC1);
      }
      FENCE();
    }
    if constexpr (kc == 1) {
      float cs[8] = {bflo(cw0.x) + bflo(cw1.x), bfhi(cw0.x) + bfhi(cw1.x), bflo(cw0.y) + bflo(cw1.y), bfhi(cw0.y) + bfhi(cw1.y),
                     bflo(cw0.z) + bflo(cw1.z), bfhi(cw0.z) + bfhi(cw1.z), bflo(cw0.w) + bflo(cw1.w), bfhi(cw0.w) + bfhi(cw1.w)};
#pragma unroll
      for (int e = 0; e < 4; ++e) { sw32(cs[e], cs[e + 4]); cs[e] += cs[e + 4]; }
#pragma unroll
      for (int e = 0; e < 2; ++e) { sw16(cs[e], cs[e + 2]); cs[e] += cs[e + 2]; }
      cs[0] += dpp<0x128>(cs[0]); cs[1] += dpp<0x128>(cs[1]);
      if ((lane & 8) == 0) *(LAS f2*)(L + L_SCR + c.w * 256 + ((lane & 7) * 8 + (lane >> 5) * 4 + ((lane >> 4) & 1) * 2) * 4) = (f2){cs[0], cs[1]};
      FENCE();
    }
    if constexpr (kc == 3) {
      if (c.w < 3) {
        const int id = threadIdx.x < 144 ? threadIdx.x : 143;
        const int ln = blk * 72 + (id >> 1);
        const char* a = ln < 14336 ? w2 + (size_t)ln * 128 : wo + (size_t)(ln - 14336) * 128;
        glds4v(a + (id & 1) * 64, c.lbase + L_DUMMY);
      }
      FENCE();
    }
    if constexpr (kc == 2) {
      if (c.w == 7 && job < 52) {
        float s = 0.f;
#pragma unroll
        for (int ww = 0; ww < 8; ++ww) s += *(LAS float*)(L + L_SCR + ww * 256 + lane * 4);
        __builtin_amdgcn_raw_buffer_store_b64((v2u){fbits(s), c.tag}, c.rs, WS_CS + (gunit * 64 + lane) * 32 + kq * 8, 0, AUX_SC1);
      }
      FENCE();
    }
    P1_HALF(wfb, wfa, 2 * kc + 1, 2 * kc, x2, x3, 4096, 2, 3, true, true)
    if constexpr (kc == ST_G1_ISSUE) {
      gld16_sc1(g1a, ws, g1off);
      gld16_sc1(g1b, ws, threadIdx.x < 128 ? g1off : g1off + 16);
      FENCE();
    }
  };
  pass1_step(IC<0>{});
  STAMP(c, 2);
  pass1_step(IC<1>{});
  pass1_step(IC<2>{});
  pass1_step(IC<3>{});
  pass1_step(IC<4>{});
  pass1_step(IC<5>{});
  pass1_step(IC<6>{});
  pass1_step(IC<7>{});
  STAMP(c, 3);

  TS(3 * ST_E1);
  asm volatile("" : "+v"(wfb[0][0]), "+v"(wfb[0][1]), "+v"(wfb[1][0]), "+v"(wfb[1][1]), "+v"(xf[0][15]), "+v"(xf[1][15]));
  {
      asm volatile("s_waitcnt vmcnt(%2) lgkmcnt(0)" : "+v"(g1a), "+v"(g1b) : "n"(top_wait(ST_E1)) : "memory");
      REFRESH();
      const bool isst = c.tid < 128;
      if (!__all(tags_ok(g1a, c.tag) && (isst || tags_ok(g1b, c.tag)))) {
        const unsigned off = g1off;
#pragma unroll 1
        for (unsigned it = 0; it < SPIN_CAP; ++it) {
          __builtin_amdgcn_s_sleep(2);
          gld16_sc1(g1a, ws, off);
          gld16_sc1(g1b, ws, isst ? off : off + 16);
          asm volatile("s_waitcnt vmcnt(0)" : "+v"(g1a), "+v"(g1b)::"memory");
          if (__all(tags_ok(g1a, c.tag) && (isst || tags_ok(g1b, c.tag)))) break;
        }
      }
      if (isst) *(LAS f2*)(L + L_ST + c.tid * 8) = (f2){bitsf(g1a.z), bitsf(g1a.x) * bitsf(g1a.z)};
      else if (c.tid < 128 + 208) *(LAS float*)(L + L_CS + (c.tid - 128) * 4) = (bitsf(g1a.x) + bitsf(g1a.z)) + (bitsf(g1b.x) + bitsf(g1b.z));
      wait_lgkm();
  }
  TS(3 * ST_E1 + 1);
  bar();
  TS(3 * ST_E1 + 2);
  FENCE();
  REFRESH();
  const unsigned gt0 = 8 * c.rg + 2 * c.m + c.nh, gt1 = 8 * c.rg + 2 * c.m + 1 - c.nh;
  {
    f4 cs4[2][2];
#pragma unroll
    for (int un = 0; un < 2; ++un)
#pragma unroll
      for (int i = 0; i < 2; ++i) cs4[un][i] = ldf4(L, L_CS + (64 * un + 32 * c.nh + 16 * i + 4 * g) * 4);
    const f2 st0 = ldf2(L, L_ST + (16 * (2 * c.m + c.nh) + l15) * 8), st1 = ldf2(L, L_ST + (16 * (2 * c.m + 1 - c.nh) + l15) * 8);
    FENCE();
#pragma unroll
    for (int un = 0; un < 2; ++un)
#pragma unroll
      for (int i = 0; i < 2; ++i) {
        acc[un][i][0] = mfma32(wfb[un][i], xf[0][15], acc[un][i][0]);
        acc[un][i][1] = mfma32(wfb[un][i], xf[1][15], acc[un][i][1]);
      }
    FENCE();
    TSX(0);
    if (c.isQ) {
      v2u bb[2][2];
#pragma unroll
      for (int un = 0; un < 2; ++un)
#pragma unroll
        for (int i = 0; i < 2; ++i) bb[un][i] = ld64(L, L_B1 + un * 128 + c.nh * 64 + i * 32 + 8 * g);
      const unsigned ks0 = 2 * unit_pos(c.q, 2 + 2 * (c.jj & 1)) + c.nh;
      const unsigned ho0 = WS_H + (gt0 * 72 + ks0) * 1024 + lane * 16, ho1 = WS_H + (gt1 * 72 + ks0) * 1024 + lane * 16;
      {
        __builtin_amdgcn_raw_buffer_store_b128(ffn_pair(acc[0][0][0], acc[0][1][0], cs4[0][0], cs4[0][1], bb[0][0], bb[0][1], st0), c.rs, ho0, 0, AUX_SC1);
      }
      FENCE(); issue_op<ST_E1, 0>(c); FENCE();
      {
        __builtin_amdgcn_raw_buffer_store_b128(ffn_pair(acc[0][0][1], acc[0][1][1], cs4[0][0], cs4[0][1], bb[0][0], bb[0][1], st1), c.rs, ho1, 0, AUX_SC1);
      }
      FENCE(); issue_op<ST_E1, 1>(c); FENCE();
      {
        __builtin_amdgcn_raw_buffer_store_b128(ffn_pair(acc[1][0][0], acc[1][1][0], cs4[1][0], cs4[1][1], bb[1][0], bb[1][1], st0), c.rs, ho0 + 2048, 0, AUX_SC1);
      }
      FENCE();
      {
        __builtin_amdgcn_raw_buffer_store_b128(ffn_pair(acc[1][0][1], acc[1][1][1], cs4[1][0], cs4[1][1], bb[1][0], bb[1][1], st1), c.rs, ho1 + 2048, 0, AUX_SC1);
      }
    } else {
      const v4u ones = {0x3f803f80u, 0x3f803f80u, 0x3f803f80u, 0x3f803f80u};
      const v2u wt0 = trd(L, L_WPHI + (2 * c.nh) * 512 + (4 * g + q4) * 32 + p4 * 8), wt1 = trd(L, L_WPHI + (2 * c.nh + 1) * 512 + (4 * g + q4) * 32 + p4 * 8);
      f4 pk4[4];
      v2u vb[4], fp[4], fn[4];
#pragma unroll
      for (int i = 0; i < 2; ++i)
#pragma unroll
        for (int tt = 0; tt < 2; ++tt) pk4[2 * i + tt] = mfma16(i ? wt1 : wt0, ln_pack(acc[0][i][tt], cs4[0][i], tt ? st1 : st0), z4);
#pragma unroll
      for (int i = 0; i < 2; ++i)
#pragma unroll
        for (int tt = 0; tt < 2; ++tt) vb[2 * i + tt] = ln_pack(acc[1][i][tt], cs4[1][i], tt ? st1 : st0);
      TSX(1);
      FENCE(); issue_op<ST_E1, 0>(c); FENCE();
      Feat ft;
      feat_max(pk4, ft);
      TSX(2);
      FENCE(); issue_op<ST_E1, 1>(c); FENCE();
      feat_exp(pk4, ft);
      TSX(3);
      feat_norm(ft, fp, fn);
      FENCE();
      TSX(4);
      constexpr unsigned IMG_P = L_SCR, IMG_N = L_SCR + 16384, IMG_V = L_RING + SC.slot[15] * 16384;
      const unsigned wo_ = (2 * c.nh) * 4096 + (32 * c.m + l15) * 32 + ((g ^ q4) * 8);
#pragma unroll
      for (int i = 0; i < 2; ++i)
#pragma unroll
        for (int tt = 0; tt < 2; ++tt) {
          *(LAS v2u*)(L + IMG_P + wo_ + i * 4096 + tt * 512) = fp[2 * i + tt];
          *(LAS v2u*)(L + IMG_N + wo_ + i * 4096 + tt * 512) = fn[2 * i + tt];
          *(LAS v2u*)(L + IMG_V + wo_ + i * 4096 + tt * 512) = vb[2 * i + tt];
        }
      wait_lgkm();
      TSX(5);
      bar();
      TSX(6);
      const unsigned ro = (c.w >> 1) * 4096 + (4 * g + q4) * 32 + ((p4 ^ g) * 8);
      const unsigned ra = IMG_P + (c.w & 1) * 16384 + ro, rb = IMG_V + ro;
      v4u A[4], B[4];
#pragma unroll
      for (int j = 0; j < 4; ++j) { A[j] = trd2(L, ra + j * 1024, ra + j * 1024 + 512); B[j] = trd2(L, rb + j * 1024, rb + j * 1024 + 512); }
      f4 kv0 = mfma32(A[0], B[0], z4), kv1 = mfma32(A[1], B[1], z4), z0 = mfma32(A[0], ones, z4), z1 = mfma32(A[1], ones, z4);
      kv0 = mfma32(A[2], B[2], kv0); kv1 = mfma32(A[3], B[3], kv1); z0 = mfma32(A[2], ones, z0); z1 = mfma32(A[3], ones, z1);
      kv0 += kv1; z0 += z1;
      TSX(7);
      const unsigned pbase = WS_P + (c.hq * 16 + c.rg) * PART_BYTES;
      __builtin_amdgcn_raw_buffer_store_b128((v4u){fbits(kv0[0]), c.tag, fbits(kv0[1]), c.tag}, c.rs, pbase + (c.w * 128 + lane) * 16, 0, AUX_SC1);
      __builtin_amdgcn_raw_buffer_store_b128((v4u){fbits(kv0[2]), c.tag, fbits(kv0[3]), c.tag}, c.rs, pbase + (c.w * 128 + 64 + lane) * 16, 0, AUX_SC1);
      if (l15 == 0) {
        __builtin_amdgcn_raw_buffer_store_b128((v4u){fbits(z0[0]), c.tag, fbits(z0[1]), c.tag}, c.rs, pbase + (1024 + c.w * 8 + g * 2) * 16, 0, AUX_SC1);
        __builtin_amdgcn_raw_buffer_store_b128((v4u){fbits(z0[2]), c.tag, fbits(z0[3]), c.tag}, c.rs, pbase + (1024 + c.w * 8 + g * 2 + 1) * 16, 0, AUX_SC1);
      }
    }
  }
  TSX(8);
  STAMP(c, 4);
  FENCE();

  f4 acc2[2][2], accS = z4;
#pragma unroll
  for (int b = 0; b < 2; ++b)
#pragma unroll
    for (int t = 0; t < 2; ++t) acc2[b][t] = z4;
  REFRESH();
  const unsigned wb0 = (unsigned)((8 * g + q4) * 128 + (((4 * c.nh + (p4 >> 1)) ^ ((((q4 >> 1) & 1) | ((g & 1) << 1)) << 1)) * 16) + (p4 & 1) * 8);
  const unsigned sa0 = (unsigned)((8 * g + ((g & 1) << 2) + q4) * 32 + p4 * 8);
  constexpr unsigned SS = L_RING + SC.slot[IT_S] * 16384;
  unsigned wbb[2][2] = {{wb0, wb0 ^ 32}, {}};
#pragma unroll
  for (int i = 0; i < 2; ++i) { wbb[1][i] = wbb[0][i] + HIWIN; asm volatile("" : "+v"(wbb[1][i])); }
  unsigned s0 = sa0 + (SS >= HIWIN ? HIWIN : 0), s1 = (sa0 ^ 128) + (SS >= HIWIN ? HIWIN : 0);
  asm volatile("" : "+v"(s0), "+v"(s1));
  unsigned pollv;
  v4u rk0, rk1, rz0, rz1;
  const char* flagA = ws + WS_FLAG + (0 * 16 + (c.rt >> 1)) * 128;
  const char* flagB = ws + WS_FLAG + (1 * 16 + (c.rt >> 1)) * 128;
  const char* flagO = ws + WS_FLAG + (2 * 16 + (c.rt >> 1)) * 128;
  const char* rsrc = ws + WS_P + (c.hq * 16 + 2 * c.w) * PART_BYTES;
  v4u pwa[2], psa, pwb[2], psb;

  auto pass2_step = [&](auto KC) {
    constexpr int kc = decltype(KC)::value, st = 9 + kc;
    constexpr unsigned sp = L_RING + SC.slot[IT_P0 + kc] * 16384, ss = L_RING + SC.slot[IT_S] * 16384;
    TS(3 * st);
    if constexpr (kc > 0) { asm volatile("" : "+v"(pwb[0]), "+v"(pwb[1]), "+v"(psb)); wait_vm<top_wait(st)>(); }
    else wait_top<top_wait(st)>();
    TS(3 * st + 1);
    bar();
    TS(3 * st + 2);
    FENCE();
    constexpr int hp = sp >= HIWIN;
    const unsigned w0 = wbb[hp][0], w1_ = wbb[hp][1];
    constexpr unsigned rp = sp - hp * HIWIN, rs_ = ss - (ss >= HIWIN ? HIWIN : 0);
#define P2_MMA(A, F, T, KM) do { if constexpr (DOM_) A = mfma32(F, xf[T][KM], A); FENCE(); } while (0)
#define P2_Q(FR, SR, KR, FM, SM, KM, DOM, TIE) {                                                    \
    constexpr bool DOM_ = DOM;                                                                     \
    v2u t0, t1;                                                                                    \
    t0 = trd(L, w0 + rp + ((KR) & 3) * 4096); FENCE();                                             \
    if constexpr (TIE) { asm volatile("" : "+v"(FM[0]), "+v"(FM[1]), "+v"(SM)); FENCE(); }         \
    P2_MMA(acc2[0][0], FM[0], 0, KM);                                                              \
    t1 = trd(L, w0 + rp + ((KR) & 3) * 4096 + 512); FR[0] = (v4u){t0.x, t0.y, t1.x, t1.y}; FENCE(); \
    P2_MMA(acc2[0][1], FM[0], 1, KM);                                                              \
    FR[1] = trd2(L, w1_ + rp + ((KR) & 3) * 4096, w1_ + rp + ((KR) & 3) * 4096 + 512); FENCE();    \
    P2_MMA(acc2[1][0], FM[1], 0, KM);                                                              \
    t0 = trd(L, s0 + rs_ + (KR) * 1024); FENCE();                                                  \
    P2_MMA(acc2[1][1], FM[1], 1, KM);                                                              \
    t1 = trd(L, s1 + rs_ + (KR) * 1024); SR = (v4u){t0.x, t0.y, t1.x, t1.y}; FENCE();              \
    P2_MMA(accS, SM, 0, KM);                                                                       \
  }
    op_m0<st, 0>(c);
    P2_Q(pwa, psa, 4 * kc, pwb, psb, (kc > 0 ? 4 * kc - 1 : 0), (kc > 0), false)
    op_go<st, 0>(c);
    if constexpr (st == ST_PUBA) {
      if (c.isQ && threadIdx.x == 0) __builtin_amdgcn_raw_buffer_store_b32(c.tag, c.rs, WS_FLAG + (0 * 16 + c.rg) * 128 + c.hq * 4, 0, AUX_SC1);
    }
    FENCE();
    op_m0<st, 1>(c);
    P2_Q(pwb, psb, 4 * kc + 1, pwa, psa, 4 * kc, true, true)
    op_go<st, 1>(c);
    FENCE();
    P2_Q(pwa, psa, 4 * kc + 2, pwb, psb, 4 * kc + 1, true, true)
    if constexpr (st == ST_POLLA_ISSUE) { REFRESH(); gld4_sc1(pollv, flagA, (lane & 31) * 4); }
    if constexpr (st == ST_R_ISSUE) {
      REFRESH();
      const unsigned vk = c.isQ ? (64 * c.rg + lane) * 16 : 0, vz = c.isQ ? (1024 + 4 * c.rg + (lane & 3)) * 16 : 0;
      gld16_sc1(rk0, rsrc, vk);
      gld16_sc1(rk1, rsrc + PART_BYTES, vk);
      gld16_sc1(rz0, rsrc, vz);
      gld16_sc1(rz1, rsrc + PART_BYTES, vz);
    }
    FENCE();
    P2_Q(pwb, psb, 4 * kc + 3, pwa, psa, 4 * kc + 2, true, true)
  };
  pass2_step(IC<0>{});
  pass2_step(IC<1>{});
  pass2_step(IC<2>{});
  pass2_step(IC<3>{});
  STAMP(c, 5);

  v4u hf[18][2];
  v4u rg0, rg1, rgz0, rgz1;
  TS(3 * ST_E2);
  wait_top<top_wait(ST_E2)>();
  asm volatile("s_waitcnt vmcnt(%5)" : "+v"(pollv), "+v"(rk0), "+v"(rk1), "+v"(rz0), "+v"(rz1)
               : "n"(imin(extra_wait(ST_POLLA_ISSUE, ST_E2), extra_wait(ST_R_ISSUE, ST_E2))) : "memory");
  REFRESH();
  if (c.w == 0) {
    if (!__all(pollv == c.tag || (lane & 31) >= 8)) spin_flags(c, flagA, 8);
  }
  if (c.isQ) {
    if (!__all(tags_ok(rk0, c.tag) && tags_ok(rk1, c.tag) && tags_ok(rz0, c.tag) && tags_ok(rz1, c.tag))) {
      const unsigned vk = (64 * c.rg + lane) * 16, vz = (1024 + 4 * c.rg + (lane & 3)) * 16;
#pragma unroll 1
      for (unsigned it = 0; it < SPIN_CAP; ++it) {
        __builtin_amdgcn_s_sleep(2);
        gld16_sc1(rk0, rsrc, vk);
        gld16_sc1(rk1, rsrc + PART_BYTES, vk);
        gld16_sc1(rz0, rsrc, vz);
        gld16_sc1(rz1, rsrc + PART_BYTES, vz);
        asm volatile("s_waitcnt vmcnt(0)" : "+v"(rk0), "+v"(rk1), "+v"(rz0), "+v"(rz1)::"memory");
        if (__all(tags_ok(rk0, c.tag) && tags_ok(rk1, c.tag) && tags_ok(rz0, c.tag) && tags_ok(rz1, c.tag))) break;
      }
    }
    *(LAS f2*)(L + L_SCR + c.w * 1024 + lane * 8) = (f2){bitsf(rk0.x) + bitsf(rk1.x), bitsf(rk0.z) + bitsf(rk1.z)};
    if (lane < 4) *(LAS f2*)(L + L_SCR + c.w * 1024 + 512 + lane * 8) = (f2){bitsf(rz0.x) + bitsf(rz1.x), bitsf(rz0.z) + bitsf(rz1.z)};
    wait_lgkm();
  }
  TS(3 * ST_E2 + 1);
  bar();
  TS(3 * ST_E2 + 2);
  FENCE();
  c.pH = ws + WS_H + (size_t)((4 * c.rt + c.m) * 72 + 2 * c.nh) * 1024;
  c.vH = lane * 16;
  f2 st0, st1;
  f4 cs2[2];
  f4 pq4[4];
  Feat ftq;
  v2u fp[4], fn[4];
  {
#pragma unroll
    for (int i = 0; i < 2; ++i) {
      acc2[i][0] = mfma32(pwb[i], xf[0][15], acc2[i][0]);
      acc2[i][1] = mfma32(pwb[i], xf[1][15], acc2[i][1]);
    }
    accS = mfma32(psb, xf[0][15], accS);
    st0 = ldf2(L, L_ST + (16 * (2 * c.m + c.nh) + l15) * 8); st1 = ldf2(L, L_ST + (16 * (2 * c.m + 1 - c.nh) + l15) * 8);
    cs2[0] = ldf4(L, L_CS + (128 + 32 * c.nh + 4 * g) * 4); cs2[1] = ldf4(L, L_CS + (128 + 32 * c.nh + 16 + 4 * g) * 4);
    const f4 csS = ldf4(L, L_CS + (192 + 4 * g) * 4);
    const v2u bS = ld64(L, L_B1 + 256 + 8 * g);
    FENCE();
    issue_op<ST_E2, 0>(c);
    issue_h<0>(c, hf);
    FENCE();
    TSX(9);
    const v2u bb0 = ld64(L, L_B1 + c.nh * 64 + 8 * g), bb1 = ld64(L, L_B1 + c.nh * 64 + 32 + 8 * g);
    const unsigned ksu = 2 * unit_pos(c.q, c.jj) + c.nh;
    if (c.isQ) {
      if (c.w == 0) {
        f2 s = {0.f, 0.f}, sz = {0.f, 0.f};
#pragma unroll
        for (int ww = 0; ww < 8; ++ww) { s += ldf2(L, L_SCR + ww * 1024 + lane * 8); sz += ldf2(L, L_SCR + ww * 1024 + 512 + (lane & 3) * 8); }
        const unsigned rb = WS_R + c.hq * PART_BYTES;
        __builtin_amdgcn_raw_buffer_store_b128((v4u){fbits(s.x), c.tag, fbits(s.y), c.tag}, c.rs, rb + (64 * c.rg + lane) * 16, 0, AUX_SC1);
        if (lane < 4) __builtin_amdgcn_raw_buffer_store_b128((v4u){fbits(sz.x), c.tag, fbits(sz.y), c.tag}, c.rs, rb + (1024 + 4 * c.rg + lane) * 16, 0, AUX_SC1);
      }
      const v2u wt0 = trd(L, L_WPHI + (2 * c.nh) * 512 + (4 * g + q4) * 32 + p4 * 8), wt1 = trd(L, L_WPHI + (2 * c.nh + 1) * 512 + (4 * g + q4) * 32 + p4 * 8);
#pragma unroll
      for (int i = 0; i < 2; ++i)
#pragma unroll
        for (int tt = 0; tt < 2; ++tt) pq4[2 * i + tt] = mfma16(i ? wt1 : wt0, ln_pack(acc2[i][tt], cs2[i], tt ? st1 : st0), z4);
      feat_max(pq4, ftq);
    } else {
      __builtin_amdgcn_raw_buffer_store_b128(ffn_pair(acc2[0][0], acc2[1][0], cs2[0], cs2[1], bb0, bb1, st0), c.rs, WS_H + (gt0 * 72 + ksu) * 1024 + lane * 16, 0, AUX_SC1);
    }
    FENCE();
    issue_op<ST_E2, 1>(c);
    issue_h<1>(c, hf);
    FENCE();
    if (c.isQ) feat_exp(pq4, ftq);
    else __builtin_amdgcn_raw_buffer_store_b128(ffn_pair(acc2[0][1], acc2[1][1], cs2[0], cs2[1], bb0, bb1, st1), c.rs, WS_H + (gt1 * 72 + ksu) * 1024 + lane * 16, 0, AUX_SC1);
    FENCE();
    TSX(10);
    issue_h<2>(c, hf);
    FENCE();
    {
      const v2u o = ffn_tile(accS, csS, bS, st0);
      const unsigned ks = 2 * unit_pos(c.q, 6) + (c.jj >> 1);
      __builtin_amdgcn_raw_buffer_store_b64(o, c.rs, WS_H + (gt0 * 72 + ks) * 1024 + lane * 16 + (c.jj & 1) * 8, 0, AUX_SC1);
    }
    FENCE();
    TSX(11);
    issue_op<ST_E2, 2>(c);
    issue_h<3>(c, hf);
    issue_op<ST_E2, 3>(c);
    if constexpr (HPRE > 4) issue_h<4>(c, hf);
    FENCE();
    if (c.isQ) feat_norm(ftq, fp, fn);
    FENCE();
    TSX(12);
    if constexpr (HPRE > 5) issue_h<5>(c, hf);
    if constexpr (HPRE > 6) issue_h<6>(c, hf);
    if constexpr (HPRE > 7) issue_h<7>(c, hf);
    {
      const char* rres_ = ws + WS_R + c.hq * PART_BYTES;
      const unsigned vk = c.isQ ? (c.tid >> 6) * 2048 + (c.tid & 63) * 16 : 0, vz = c.isQ ? (c.tid & 31) * 32 : 0;
      gld16_sc1(rg0, rres_, vk);
      gld16_sc1(rg1, rres_ + 1024, vk);
      gld16_sc1(rgz0, rres_ + 16384, vz);
      gld16_sc1(rgz1, rres_ + 16384 + 16, vz);
    }
    TSX(13);
    static_assert(HPRE >= 4 && HPRE <= 8 && ST_RG_ISSUE == ST_E2, "initial h loads");
  }
  STAMP(c, 6);
  FENCE();

  f4 accF[4];
#pragma unroll
  for (int t = 0; t < 4; ++t) accF[t] = z4;
  REFRESH();
  const unsigned fa0 = (unsigned)((64 * c.nh + 4 * g + q4) * 128 + (((p4 >> 1) ^ ((((g & 1) << 1) | (q4 >> 1)) << 1)) * 16) + (p4 & 1) * 8);
  unsigned fb[2][4] = {{fa0, fa0 ^ 32, fa0 ^ 64, fa0 ^ 96}, {}};
#pragma unroll
  for (int i = 0; i < 4; ++i) { fb[1][i] = fb[0][i] + HIWIN; asm volatile("" : "+v"(fb[1][i])); }
  v4u xres, fwa[4], fwb[4], hprev;
  const char* rres = ws + WS_R + c.hq * PART_BYTES;

  auto phase2_step = [&](auto CC) {
    constexpr int ch = decltype(CC)::value, st = ST_F0 + ch;
    constexpr unsigned sf = L_RING + SC.slot[item_of_F(ch)] * 16384;
    TS(3 * st);
    if constexpr (ch > 0) asm volatile("" : "+v"(fwb[0]), "+v"(fwb[1]), "+v"(fwb[2]), "+v"(fwb[3]));
    if constexpr (ch > 0) asm volatile("s_waitcnt vmcnt(%2)" : "+v"(hf[ch][0]), "+v"(hf[ch][1]) : "n"(top_wait(st)) : "memory");
    else asm volatile("s_waitcnt vmcnt(%2) lgkmcnt(0)" : "+v"(hf[ch][0]), "+v"(hf[ch][1]) : "n"(top_wait(st)) : "memory");
    if constexpr (st == ST_POLLB_CHECK) {
      asm volatile("s_waitcnt vmcnt(%1)" : "+v"(pollv) : "n"(extra_wait(ST_POLLB_ISSUE, st)) : "memory");
      if (c.w == 0) { if (!__all(pollv == c.tag || (lane & 31) >= 16)) spin_flags(c, flagB, 16); }
    }
    if constexpr (st == ST_POLLO_CHECK) {
      asm volatile("s_waitcnt vmcnt(%1)" : "+v"(pollv) : "n"(extra_wait(ST_POLLO_ISSUE, st)) : "memory");
      if (c.w == 0) { if (!__all(pollv == c.tag || (lane & 31) >= 8)) spin_flags(c, flagO, 8); }
    }
    if constexpr (st == ST_RG_CHECK) {
      asm volatile("s_waitcnt vmcnt(%4)" : "+v"(rg0), "+v"(rg1), "+v"(rgz0), "+v"(rgz1) : "n"(extra_wait(ST_RG_ISSUE, st)) : "memory");
      if (c.isQ) {
        const unsigned t = c.tid;
        if (!__all(tags_ok(rg0, c.tag) && tags_ok(rg1, c.tag) && tags_ok(rgz0, c.tag) && tags_ok(rgz1, c.tag))) {
#pragma unroll 1
          for (unsigned it = 0; it < SPIN_CAP; ++it) {
            __builtin_amdgcn_s_sleep(2);
            gld16_sc1(rg0, rres, (t >> 6) * 2048 + (t & 63) * 16);
            gld16_sc1(rg1, rres + 1024, (t >> 6) * 2048 + (t & 63) * 16);
            gld16_sc1(rgz0, rres + 16384, (t & 31) * 32);
            gld16_sc1(rgz1, rres + 16384 + 16, (t & 31) * 32);
            asm volatile("s_waitcnt vmcnt(0)" : "+v"(rg0), "+v"(rg1), "+v"(rgz0), "+v"(rgz1)::"memory");
            if (__all(tags_ok(rg0, c.tag) && tags_ok(rg1, c.tag) && tags_ok(rgz0, c.tag) && tags_ok(rgz1, c.tag))) break;
          }
        }
        *(LAS v2u*)(L + L_SCR + (t >> 7) * 1024 + (t & 63) * 16 + ((t >> 6) & 1) * 8) =
            (v2u){pk(bitsf(rg0.x), bitsf(rg0.z)), pk(bitsf(rg1.x), bitsf(rg1.z))};
        if (t < 32)
          *(LAS v2u*)(L + L_SCR + 4096 + (t >> 3) * 64 + (t & 3) * 16 + ((t >> 2) & 1) * 8) =
              (v2u){pk(bitsf(rgz0.x), bitsf(rgz0.z)), pk(bitsf(rgz1.x), bitsf(rgz1.z))};
        wait_lgkm();
      }
    }
    TS(3 * st + 1);
    bar();
    TS(3 * st + 2);
    FENCE();
    constexpr int hi = sf >= HIWIN;
    const unsigned f0 = fb[hi][0], f1 = fb[hi][1], f2_ = fb[hi][2], f3 = fb[hi][3];
    constexpr unsigned rf = sf - hi * HIWIN;
    {
      v2u t0, t1;
      fwa[0] = trd2(L, f0 + rf, f0 + rf + 2048); FENCE();
      op_m0<st, 0>(c); FENCE();
      if constexpr (ch > 0) accF[0] = mfma32(fwb[0], hprev, accF[0]);
      FENCE();
      fwa[1] = trd2(L, f1 + rf, f1 + rf + 2048); FENCE();
      if constexpr (st == ST_PUBB) {
        if (threadIdx.x == 0) __builtin_amdgcn_raw_buffer_store_b32(c.tag, c.rs, WS_FLAG + (1 * 16 + c.rg) * 128 + (4 * c.q + c.jj) * 4, 0, AUX_SC1);
      }
      if constexpr (st == ST_PUBO) {
        if (c.isQ && threadIdx.x == 0) __builtin_amdgcn_raw_buffer_store_b32(c.tag, c.rs, WS_FLAG + (2 * 16 + c.rg) * 128 + c.hq * 4, 0, AUX_SC1);
      }
      op_go<st, 0>(c); FENCE();
      if constexpr (ch > 0) accF[1] = mfma32(fwb[1], hprev, accF[1]);
      FENCE();
      fwa[2] = trd2(L, f2_ + rf, f2_ + rf + 2048); FENCE();
      op_m0<st, 1>(c); FENCE();
      if constexpr (ch > 0) accF[2] = mfma32(fwb[2], hprev, accF[2]);
      FENCE();
      fwa[3] = trd2(L, f3 + rf, f3 + rf + 2048); FENCE();
      op_go<st, 1>(c); FENCE();
      if constexpr (ch > 0) accF[3] = mfma32(fwb[3], hprev, accF[3]);
      FENCE();
      fwb[0] = trd2(L, f0 + rf + 4096, f0 + rf + 4096 + 2048); FENCE();
      asm volatile("" : "+v"(fwa[0]), "+v"(fwa[1]), "+v"(fwa[2]), "+v"(fwa[3])); FENCE();
      accF[0] = mfma32(fwa[0], hf[ch][0], accF[0]); FENCE();
      fwb[1] = trd2(L, f1 + rf + 4096, f1 + rf + 4096 + 2048); FENCE();
      constexpr int hc = h_at(st);
      if constexpr (hc >= 0) { issue_h0<(hc >= 0 ? hc : 0)>(c, hf); FENCE(); }
      accF[1] = mfma32(fwa[1], hf[ch][0], accF[1]); FENCE();
      fwb[2] = trd2(L, f2_ + rf + 4096, f2_ + rf + 4096 + 2048); FENCE();
      accF[2] = mfma32(fwa[2], hf[ch][0], accF[2]); FENCE();
      fwb[3] = trd2(L, f3 + rf + 4096, f3 + rf + 4096 + 2048); FENCE();
      if constexpr (hc >= 0) { issue_h1<(hc >= 0 ? hc : 0)>(c, hf); FENCE(); }
      if constexpr (st == ST_POLLB_ISSUE) gld4_sc1(pollv, flagB, (lane & 31) * 4);
      if constexpr (st == ST_POLLO_ISSUE) gld4_sc1(pollv, flagO, (lane & 31) * 4);
      if constexpr (ch == 14) gld16(xres, x + (size_t)(64 * c.rt) * 1024 + c.ct * 128, (c.tid >> 3) * 1024 + (c.tid & 7) * 16);
      FENCE();
      accF[3] = mfma32(fwa[3], hf[ch][0], accF[3]);
      hprev = hf[ch][1];
      FENCE();
      (void)t0; (void)t1;
    }
    if constexpr (st == ST_O) {
      if (c.isQ) {
        v2u ob[2][2];
#pragma unroll
        for (int i = 0; i < 2; ++i) {
          const int hl = 2 * c.nh + i;
          const v4u Akv = ld128(L, L_SCR + hl * 1024 + lane * 16), Az = ld128(L, L_SCR + 4096 + hl * 64 + g * 16);
#pragma unroll
          for (int tt = 0; tt < 2; ++tt) {
            const v4u ph = {fp[2 * i + tt].x, fp[2 * i + tt].y, fn[2 * i + tt].x, fn[2 * i + tt].y};
            const f4 num = mfma32(Akv, ph, z4), den = mfma32(Az, ph, z4);
            const float inv = __builtin_amdgcn_rcpf(den[0] + 1e-6f);
            ob[i][tt] = (v2u){pk(num[0] * inv, num[1] * inv), pk(num[2] * inv, num[3] * inv)};
          }
        }
        const unsigned ks = 56 + 2 * c.hq + c.nh;
        __builtin_amdgcn_raw_buffer_store_b128((v4u){ob[0][0].x, ob[0][0].y, ob[1][0].x, ob[1][0].y}, c.rs, WS_H + (gt0 * 72 + ks) * 1024 + lane * 16, 0, AUX_SC1);
        __builtin_amdgcn_raw_buffer_store_b128((v4u){ob[0][1].x, ob[0][1].y, ob[1][1].x, ob[1][1].y}, c.rs, WS_H + (gt1 * 72 + ks) * 1024 + lane * 16, 0, AUX_SC1);
      }
    }
    FENCE();
  };
  phase2_step(IC<0>{});
  phase2_step(IC<1>{});
  phase2_step(IC<2>{});
  phase2_step(IC<3>{});
  phase2_step(IC<4>{});
  phase2_step(IC<5>{});
  STAMP(c, 7);
  phase2_step(IC<6>{});
  phase2_step(IC<7>{});
  phase2_step(IC<8>{});
  phase2_step(IC<9>{});
  phase2_step(IC<10>{});
  phase2_step(IC<11>{});
  STAMP(c, 8);
  phase2_step(IC<12>{});
  phase2_step(IC<13>{});
  phase2_step(IC<14>{});
  phase2_step(IC<15>{});
  phase2_step(IC<16>{});
  phase2_step(IC<17>{});
  STAMP(c, 9);

  TS(96);
#pragma unroll
  for (int t = 0; t < 4; ++t) accF[t] = mfma32(fwb[t], hprev, accF[t]);
  REFRESH();
#pragma unroll
  for (int t = 0; t < 4; ++t) *(LAS f4*)(L + L_SCR + c.nh * 17408 + (16 * c.m + l15) * 272 + (16 * t + 4 * g) * 4) = accF[t];
  asm volatile("s_waitcnt vmcnt(0) lgkmcnt(0)" : "+v"(xres)::"memory");
  TS(97);
  bar();
  {
    const int tok = c.tid >> 3, cc = c.tid & 7;
    const unsigned a = L_SCR + tok * 272 + cc * 32;
    f4 s0 = ldf4(L, a) + ldf4(L, a + 17408), s1 = ldf4(L, a + 16) + ldf4(L, a + 16 + 17408);
    const v4u bb = ld128(L, L_B2 + cc * 16);
    v4u o;
    o.x = pk(s0[0] + bflo(xres.x) + bflo(bb.x), s0[1] + bfhi(xres.x) + bfhi(bb.x));
    o.y = pk(s0[2] + bflo(xres.y) + bflo(bb.y), s0[3] + bfhi(xres.y) + bfhi(bb.y));
    o.z = pk(s1[0] + bflo(xres.z) + bflo(bb.z), s1[1] + bfhi(xres.z) + bfhi(bb.z));
    o.w = pk(s1[2] + bflo(xres.w) + bflo(bb.w), s1[3] + bfhi(xres.w) + bfhi(bb.w));
    *(v4u*)((char*)out + (size_t)(64 * c.rt + tok) * 1024 + c.ct * 128 + cc * 16) = o;
  }
  if (blk == 0 && c.tid == 0) *(unsigned*)(ws + WS_EPOCH) = c.tag;
  STAMP(c, 10);
}

}

extern "C" void kernel_launch(void* const* d_in, const int* in_sizes, int n_in, void* d_out, int out_size, void* d_ws, size_t ws_size,
                              hipStream_t stream) {
  (void)in_sizes; (void)n_in; (void)out_size; (void)ws_size;
  (void)hipFuncSetAttribute((const void*)hh::fused_kernel, hipFuncAttributeMaxDynamicSharedMemorySize, hh::L_END);
  hh::fused_kernel<<<256, 512, hh::L_END, stream>>>((const char*)d_in[0], (const char*)d_in[1], (const char*)d_in[2], (const char*)d_in[3],
                                                    (const char*)d_in[5], (const char*)d_in[6], (const char*)d_in[7], (const char*)d_in[8],
                                                    (unsigned short*)d_out, (char*)d_ws);
}
```

```cpp
#include <hip/hip_runtime.h>
#include <stdint.h>
#include <type_traits>

namespace hh {

#define LAS __attribute__((address_space(3)))
typedef unsigned v4u __attribute__((ext_vector_type(4)));
typedef unsigned v2u __attribute__((ext_vector_type(2)));
typedef float f4 __attribute__((ext_vector_type(4)));
typedef float f2 __attribute__((ext_vector_type(2)));
typedef short s4 __attribute__((ext_vector_type(4)));
typedef __bf16 b8 __attribute__((ext_vector_type(8)));
typedef __bf16 b2 __attribute__((ext_vector_type(2)));
template <int V> using IC = std::integral_constant<int, V>;

constexpr unsigned WS_EPOCH = 0;
constexpr unsigned WS_FLAG = 256;
constexpr unsigned WS_STATS = 8192;
constexpr unsigned WS_CS = WS_STATS + 2048 * 16;
constexpr unsigned PART_BYTES = 17408;
constexpr unsigned WS_P = WS_CS + 3328 * 32;
constexpr unsigned WS_R = WS_P + 8 * 16 * PART_BYTES;
constexpr unsigned WS_H = (WS_R + 8 * PART_BYTES + 4095u) & ~4095u;
constexpr unsigned WS_DBG = WS_H + 128 * 72 * 1024;
constexpr unsigned WS_END = WS_DBG;

constexpr int NSLOT = 7;
constexpr unsigned L_RING = 0;
constexpr unsigned L_SCR = NSLOT * 16384;
constexpr unsigned SCRW = 5248;
constexpr unsigned L_CS = L_SCR + 8 * SCRW;
constexpr unsigned L_ST = L_CS + 1024;
constexpr unsigned L_B1 = L_ST + 1024;
constexpr unsigned L_WPHI = L_B1 + 512;
constexpr unsigned L_B2 = L_WPHI + 2048;
constexpr unsigned L_DUMMY = L_B2 + 256;
constexpr unsigned L_END = L_DUMMY + 256;
static_assert(L_END <= 163840, "LDS");

constexpr int NITEM = 39, NSTEP = 32;
constexpr int ST_E1 = 8, ST_E2 = 13, ST_F0 = 14;
constexpr int IT_P0 = 16, IT_F0 = 20, IT_S = 21;
constexpr int item_of_F(int c) { return c == 0 ? IT_F0 : IT_S + c; }
#define HPRE_N 4
constexpr int HPRE = HPRE_N;
constexpr int DH = 3;
constexpr int ST_G1_ISSUE = 5, ST_G1_CHECK = 8;
#define PUBA_STEP 10
constexpr int ST_PUBA = PUBA_STEP;
constexpr int ST_POLLA_ISSUE = 12;
constexpr int ST_R_ISSUE = 11;
constexpr int ST_PUBB = 15;
constexpr int E2_OPS_AFTER_STORES = 4 + 2 * (HPRE - 4);
constexpr int ST_POLLB_ISSUE = 17, ST_POLLB_CHECK = 19;
constexpr int ST_RG_ISSUE = 13, ST_RG_CHECK = 16;
constexpr int ST_O = 17;
constexpr int ST_PUBO = 20;
constexpr int ST_POLLO_ISSUE = 23, ST_POLLO_CHECK = 25;
static_assert(ST_F0 + 8 - DH == ST_POLLB_CHECK && ST_F0 + 14 - DH == ST_POLLO_CHECK, "flag checks sit in front of the first dependent loads");

struct Sched {
  int slot[NITEM], issue[NITEM], first[NITEM], last[NITEM];
  int cum[NSTEP + 1];
  int pos_item[2][NITEM];
  int pos_h[2][18];
  int h_issue[18];
  int pos_g1[2];
};
constexpr int PRO_REG_LAST = 6;
constexpr Sched make_sched() {
  Sched s{};
  for (int i = 0; i < NITEM; ++i) {
    if (i < 16) { s.first[i] = i >> 1; s.last[i] = i >> 1; }
    else if (i < IT_F0) { s.first[i] = 9 + (i - IT_P0); s.last[i] = s.first[i]; }
    else if (i == IT_F0) { s.first[i] = ST_F0; s.last[i] = ST_F0; }
    else if (i == IT_S) { s.first[i] = 9; s.last[i] = 12; }
    else { s.first[i] = ST_F0 + (i - IT_S); s.last[i] = s.first[i]; }
  }
  s.last[15] = ST_E1;
  for (int c = 0; c < 18; ++c) s.h_issue[c] = c < HPRE ? ST_E2 : c < 8 ? ST_F0 + c - HPRE : ST_F0 + c - DH;
  int n = 0;
  int next = 0;
  int owner[NSLOT] = {};
  for (int k = 0; k < NSLOT; ++k) {
    s.slot[next] = k; s.issue[next] = -1; owner[k] = next; n += 2; s.pos_item[0][next] = s.pos_item[1][next] = n - 1; ++next;
    if (k == 1) n += 4;
  }
  for (int st = 0; st < NSTEP; ++st) {
    s.cum[st] = n;
    const int first_new = next;
    for (int rep = 0; rep < NSLOT; ++rep) {
      int best = -1;
      for (int k = 0; k < NSLOT; ++k)
        if (owner[k] >= 0 && s.last[owner[k]] < st && (best < 0 || owner[k] < owner[best])) best = k;
      if (best < 0) break;
      if (next < NITEM) { s.slot[next] = best; s.issue[next] = st; owner[best] = next; ++next; }
      else owner[best] = -1;
    }
    int nreg = 0;
    for (int c = 0; c < 18; ++c) if (s.h_issue[c] == st) nreg += 2;
    if (st == ST_G1_ISSUE) nreg += 2;
    const int nring = 2 * (next - first_new);
    for (int o = 0; o < 2; ++o) {
      int p = n + (o == 0 ? 0 : nreg);
      for (int i = first_new; i < next; ++i) { p += 2; s.pos_item[o][i] = p - 1; }
      p = n + (o == 0 ? nring : 0);
      for (int c = 0; c < 18; ++c) if (s.h_issue[c] == st) { p += 2; s.pos_h[o][c] = p - 1; }
      if (st == ST_G1_ISSUE) { p += 2; s.pos_g1[o] = p - 1; }
    }
    n += nring + nreg;
  }
  s.cum[NSTEP] = n;
  return s;
}
constexpr Sched SC = make_sched();
constexpr int imin(int a, int b) { return a < b ? a : b; }
constexpr int imax(int a, int b) { return a > b ? a : b; }
constexpr int top_wait(int st) {
  int need = -1;
  for (int o = 0; o < 2; ++o) {
    for (int i = 0; i < NITEM; ++i) if (SC.first[i] == st) need = imax(need, SC.pos_item[o][i]);
    if (st >= ST_F0) need = imax(need, SC.pos_h[o][st - ST_F0]);
    if (st == ST_G1_CHECK) need = imax(need, SC.pos_g1[o]);
  }
  int n = need < 0 ? 63 : SC.cum[st] - 1 - need;
  if (st == 9) n += 2;
  if (st == ST_PUBA) n = imin(n, SC.cum[st] - SC.cum[ST_E1 + 1]);
  if (st == ST_PUBB) n = imin(n, SC.cum[st] - SC.cum[ST_E2 + 1] + E2_OPS_AFTER_STORES);
  if (st == ST_PUBO) n = imin(n, SC.cum[st] - SC.cum[ST_O + 1]);
  return n > 63 ? 63 : n;
}
constexpr int extra_wait(int a, int b) { int n = SC.cum[b] - SC.cum[a + 1]; return n > 63 ? 63 : n; }
constexpr int h_at(int st) {
  for (int c = 0; c < 18; ++c) if (SC.h_issue[c] == st) return c;
  return -1;
}
constexpr int nth_item(int st, int k) {
  for (int i = 0; i < NITEM; ++i) if (SC.issue[i] == st) { if (k == 0) return i; --k; }
  return -1;
}
static_assert(nth_item(ST_E1, 0) == IT_S && nth_item(ST_E1, 1) < 0 && nth_item(ST_E2, 2) < 0 && nth_item(9, 1) < 0 && nth_item(10, 1) < 0 && nth_item(11, 1) < 0 && nth_item(12, 1) < 0, "schedule shape");

__host__ __device__ constexpr int unit_pos(int q, int off) { return off >= 2 && off <= 5 ? 4 * q + (off - 2) : 16 + 3 * q + (off == 6 ? 2 : off); }
__host__ __device__ constexpr int pos_unit(int pos) { return pos < 16 ? 7 * (pos >> 2) + 2 + (pos & 3) : 7 * ((pos - 16) / 3) + (((pos - 16) % 3) == 2 ? 6 : (pos - 16) % 3); }

#define DI __device__ __forceinline__
DI void glds16(const void* sbase, unsigned voff, unsigned lds) {
  asm volatile("s_mov_b32 m0, %2\n\ts_nop 0\n\tglobal_load_lds_dwordx4 %0, %1" ::"v"(voff), "s"(sbase), "s"(lds) : "memory");
}
DI void set_m0(unsigned lds) { asm volatile("s_mov_b32 m0, %0" ::"s"(lds) : "memory"); }
DI void glds16_m0(const void* sbase, unsigned voff) {
  asm volatile("global_load_lds_dwordx4 %0, %1" ::"v"(voff), "s"(sbase) : "memory");
}
DI void glds4(const void* sbase, unsigned voff, unsigned lds) {
  asm volatile("s_mov_b32 m0, %2\n\ts_nop 0\n\tglobal_load_lds_dword %0, %1" ::"v"(voff), "s"(sbase), "s"(lds) : "memory");
}
DI void glds4v(const void* vaddr, unsigned lds) {
  asm volatile("s_mov_b32 m0, %1\n\ts_nop 0\n\tglobal_load_lds_dword %0, off" ::"v"(vaddr), "s"(lds) : "memory");
}
DI void gld16(v4u& d, const void* sbase, unsigned voff) {
  asm volatile("global_load_dwordx4 %0, %1, %2" : "=v"(d) : "v"(voff), "s"(sbase) : "memory");
}
DI void gld16_sc1(v4u& d, const void* sbase, unsigned voff) {
  asm volatile("global_load_dwordx4 %0, %1, %2 sc1" : "=v"(d) : "v"(voff), "s"(sbase) : "memory");
}
template <int OFF> DI void gld16_sc1_off(v4u& d, const void* sbase, unsigned voff) {
  asm volatile("global_load_dwordx4 %0, %1, %2 offset:%3 sc1" : "=v"(d) : "v"(voff), "s"(sbase), "n"(OFF) : "memory");
}
DI void gld4_sc1(unsigned& d, const void* sbase, unsigned voff) {
  asm volatile("global_load_dword %0, %1, %2 sc1" : "=v"(d) : "v"(voff), "s"(sbase) : "memory");
}
template <int N> DI void wait_top() { asm volatile("s_waitcnt vmcnt(%0) lgkmcnt(0)" ::"n"(N) : "memory"); }
template <int N> DI void wait_vm() { asm volatile("s_waitcnt vmcnt(%0)" ::"n"(N) : "memory"); }
DI void wait_lgkm() { asm volatile("s_waitcnt lgkmcnt(0)" ::: "memory"); }
DI void bar() { asm volatile("" ::: "memory"); __builtin_amdgcn_s_barrier(); asm volatile("" ::: "memory"); }
#define FENCE() __builtin_amdgcn_sched_barrier(0)

DI unsigned pk(float a, float b) { b2 r = {(__bf16)a, (__bf16)b}; return __builtin_bit_cast(unsigned, r); }
DI float bflo(unsigned u) { return __builtin_bit_cast(float, u << 16); }
DI float bfhi(unsigned u) { return __builtin_bit_cast(float, u & 0xffff0000u); }
DI unsigned fbits(float f) { return __builtin_bit_cast(unsigned, f); }
DI float bitsf(unsigned u) { return __builtin_bit_cast(float, u); }
DI f4 mfma32(v4u a, v4u b, f4 c) { return __builtin_amdgcn_mfma_f32_16x16x32_bf16(__builtin_bit_cast(b8, a), __builtin_bit_cast(b8, b), c, 0, 0, 0); }
DI f4 mfma16(v2u a, v2u b, f4 c) { return __builtin_amdgcn_mfma_f32_16x16x16bf16_1k(__builtin_bit_cast(s4, a), __builtin_bit_cast(s4, b), c, 0, 0, 0); }
DI v2u trd(LAS char* L, unsigned a) { return __builtin_bit_cast(v2u, __builtin_amdgcn_ds_read_tr16_b64_v4i16((LAS s4*)(L + a))); }
DI v4u trd2(LAS char* L, unsigned a0, unsigned a1) { v2u x = trd(L, a0), y = trd(L, a1); return (v4u){x.x, x.y, y.x, y.y}; }
DI v4u ld128(LAS char* L, unsigned a) { return *(LAS v4u*)(L + a); }
DI f4 ldf4(LAS char* L, unsigned a) { return *(LAS f4*)(L + a); }
DI f2 ldf2(LAS char* L, unsigned a) { return *(LAS f2*)(L + a); }
DI v2u ld64(LAS char* L, unsigned a) { return *(LAS v2u*)(L + a); }

template <int CTRL> DI float dpp(float v) { return bitsf((unsigned)__builtin_amdgcn_update_dpp(0, (int)fbits(v), CTRL, 0xf, 0xf, true)); }
DI void sw32(float& a, float& b) { auto r = __builtin_amdgcn_permlane32_swap(fbits(a), fbits(b), false, false); a = bitsf((unsigned)r[0]); b = bitsf((unsigned)r[1]); }
DI void sw16(float& a, float& b) { auto r = __builtin_amdgcn_permlane16_swap(fbits(a), fbits(b), false, false); a = bitsf((unsigned)r[0]); b = bitsf((unsigned)r[1]); }
DI float xsum(float v) { float a = v, b = v; sw16(a, b); v = a + b; a = v; b = v; sw32(a, b); return a + b; }
DI float rowsum16(float v) { v += dpp<0x128>(v); v += dpp<0x124>(v); v += dpp<0x122>(v); v += dpp<0x121>(v); return v; }
DI float wavesum(float v) { return xsum(rowsum16(v)); }

constexpr float LOG2E = 1.4426950408889634f;
DI float vmax(float a, float b) { return __builtin_elementwise_maximum(a, b); }
DI float vmin(float a, float b) { return __builtin_elementwise_minimum(a, b); }
template <class OP> DI float fold4(float a0, float a1, float a2, float a3, OP op) {
  sw32(a0, a1); sw32(a2, a3);
  float m01 = op(a0, a1), m23 = op(a2, a3);
  sw16(m01, m23);
  return op(m01, m23);
}
DI void unfold4(float m, float (&o)[4]) {
  float a = m, b = m;
  sw16(a, b);
  float a2 = a, b2 = b;
  sw32(a, a2); sw32(b, b2);
  o[0] = a; o[1] = a2; o[2] = b; o[3] = b2;
}
struct Feat { float a[4], b[4]; f2 ep[4][2], en[4][2]; };
DI void tie8(f2 (&a)[4][2]) { asm volatile("" : "+v"(a[0][0]), "+v"(a[0][1]), "+v"(a[1][0]), "+v"(a[1][1]), "+v"(a[2][0]), "+v"(a[2][1]), "+v"(a[3][0]), "+v"(a[3][1])); }
DI void feat_max(const f4 (&p)[4], Feat& f) {
  float mx[4], mn[4];
#pragma unroll
  for (int t = 0; t < 4; ++t) {
    mx[t] = vmax(vmax(vmax(p[t][0], p[t][1]), p[t][2]), p[t][3]);
    mn[t] = vmin(vmin(vmin(p[t][0], p[t][1]), p[t][2]), p[t][3]);
  }
  const float MX = fold4(mx[0], mx[1], mx[2], mx[3], [](float a, float b) { return vmax(a, b); }) * -LOG2E;
  const float MN = fold4(mn[0], mn[1], mn[2], mn[3], [](float a, float b) { return vmin(a, b); }) * LOG2E;
  unfold4(MX, f.a);
  unfold4(MN, f.b);
}
DI void feat_exp(const f4 (&p)[4], Feat& f) {
#pragma unroll
  for (int t = 0; t < 4; ++t)
#pragma unroll
    for (int h = 0; h < 2; ++h) {
      const f2 v = {p[t][2 * h], p[t][2 * h + 1]};
      f.ep[t][h] = __builtin_elementwise_fma(v, (f2){LOG2E, LOG2E}, (f2){f.a[t], f.a[t]});
      f.en[t][h] = __builtin_elementwise_fma(v, (f2){-LOG2E, -LOG2E}, (f2){f.b[t], f.b[t]});
    }
  tie8(f.ep); tie8(f.en);
#pragma unroll
  for (int t = 0; t < 4; ++t)
#pragma unroll
    for (int h = 0; h < 2; ++h) {
      f.ep[t][h] = (f2){__builtin_amdgcn_exp2f(f.ep[t][h].x), __builtin_amdgcn_exp2f(f.ep[t][h].y)};
      f.en[t][h] = (f2){__builtin_amdgcn_exp2f(f.en[t][h].x), __builtin_amdgcn_exp2f(f.en[t][h].y)};
    }
  tie8(f.ep); tie8(f.en);
}
DI void feat_norm(const Feat& f, v2u (&pos)[4], v2u (&neg)[4]) {
  float sp[4], sn[4];
#pragma unroll
  for (int t = 0; t < 4; ++t) {
    const f2 a = f.ep[t][0] + f.ep[t][1], b = f.en[t][0] + f.en[t][1];
    sp[t] = a.x + a.y;
    sn[t] = b.x + b.y;
  }
  const float IP = __builtin_amdgcn_rcpf(fold4(sp[0], sp[1], sp[2], sp[3], [](float a_, float b_) { return a_ + b_; }));
  const float IN = __builtin_amdgcn_rcpf(fold4(sn[0], sn[1], sn[2], sn[3], [](float a_, float b_) { return a_ + b_; }));
  float ip[4], in[4];
  unfold4(IP, ip);
  unfold4(IN, in);
#pragma unroll
  for (int t = 0; t < 4; ++t) {
    const f2 a0 = f.ep[t][0] * ip[t], a1 = f.ep[t][1] * ip[t], b0 = f.en[t][0] * in[t], b1 = f.en[t][1] * in[t];
    pos[t] = (v2u){pk(a0.x, a0.y), pk(a1.x, a1.y)};
    neg[t] = (v2u){pk(b0.x, b0.y), pk(b1.x, b1.y)};
  }
}

template <unsigned OFF> DI unsigned win(unsigned base) {
  if constexpr (OFF >= 65536u - 16384u) { unsigned b = base + (OFF & ~16383u); asm volatile("" : "+v"(b)); return b; }
  else return base;
}
template <unsigned OFF> constexpr unsigned rem() { return OFF >= 65536u - 16384u ? (OFF & 16383u) : OFF; }
constexpr unsigned HIWIN = 49152;
#define AUX_SC1 16
constexpr unsigned SPIN_CAP = 1u << 16;

struct Ctx {
  LAS char* L;
  unsigned lbase;
  const char *x, *wo, *w2;
  char* ws;
  __amdgpu_buffer_rsrc_t rs;
  unsigned tag;
  int tid, lane, w, m, nh;
  int rg, q, jj, hq, isQ, rt, ct;
  const char *u0, *u1, *u2, *ush;
  unsigned ld1, ld2;
  unsigned vX0, vX1, vW0, vW1, vP0, vP1, vF0, vF1;
  const char *pX, *pW, *pP, *pF1, *pF5, *pWo;
  const char* pH;
  unsigned vH;
  unsigned ldsw;
};

#define TS(i) do { } while (0)
#define TSX(i) do { } while (0)
#define STAMP(c, i) do { } while (0)

template <int I, int U> DI void half_m0(const Ctx& c) { set_m0(c.ldsw + SC.slot[I] * 16384 + U * 1024); }
template <int I, int U> DI void half_go(const Ctx& c) {
  if constexpr (I < 16 && (I & 1) == 0) {
    glds16_m0(c.pX + (I >> 1) * 128, U ? c.vX1 : c.vX0);
  } else if constexpr (I < 16) {
    glds16_m0(c.pW + (size_t)(64 * (I >> 1)) * c.ld1, U ? c.vW1 : c.vW0);
  } else if constexpr (I == IT_S) {
    const unsigned rr = c.lane >> 1;
    glds16_m0(c.ush + (size_t)(64 * c.w + 32 * U) * 3584, (rr ^ ((rr & 8) >> 1)) * 3584 + (c.lane & 1) * 16);
  } else if constexpr (I < IT_F0) {
    glds16_m0(c.pP + (size_t)(128 * (I - IT_P0)) * c.ld2, U ? c.vP1 : c.vP0);
  } else {
    constexpr int C = I == IT_F0 ? 0 : I - IT_S;
    if constexpr (C < 14) {
      constexpr int d = pos_unit(2 * C + 1) - pos_unit(2 * C);
      static_assert(d == 1 || d == 5, "unit distance inside a chunk");
      glds16_m0((d == 1 ? c.pF1 : c.pF5) + pos_unit(2 * C) * 65536, U ? c.vF1 : c.vF0);
    } else {
      glds16_m0(c.pWo + (C - 14) * 131072, U ? c.vF1 : c.vF0);
    }
  }
}
template <int I, int U> DI void issue_half(const Ctx& c) { half_m0<I, U>(c); asm volatile("s_nop 0"); half_go<I, U>(c); }
template <int ST, int K> DI void op_m0(const Ctx& c) { constexpr int I = nth_item(ST, K >> 1); if constexpr (I >= 0) half_m0<I, (K & 1)>(c); }
template <int ST, int K> DI void op_go(const Ctx& c) { constexpr int I = nth_item(ST, K >> 1); if constexpr (I >= 0) half_go<I, (K & 1)>(c); }
template <int ST, int K> DI void issue_op(const Ctx& c) { constexpr int I = nth_item(ST, K >> 1); if constexpr (I >= 0) issue_half<I, (K & 1)>(c); }
template <int C> DI void issue_h0(const Ctx& c, v4u (&hf)[18][2]) { gld16_sc1_off<0>(hf[C][0], c.pH + C * 4096, c.vH); }
template <int C> DI void issue_h1(const Ctx& c, v4u (&hf)[18][2]) { gld16_sc1_off<1024>(hf[C][1], c.pH + C * 4096, c.vH); }
template <int C> DI void issue_h(const Ctx& c, v4u (&hf)[18][2]) { issue_h0<C>(c, hf); issue_h1<C>(c, hf); }

DI void spin_flags(const Ctx& c, const char* line, int n) {
#pragma unroll 1
  for (unsigned it = 0; it < SPIN_CAP; ++it) {
    unsigned v;
    gld4_sc1(v, line, (unsigned)(c.lane & 31) * 4);
    asm volatile("s_waitcnt vmcnt(0)" : "+v"(v)::"memory");
    if (__all(v == c.tag || (c.lane & 31) >= n)) return;
    __builtin_amdgcn_s_sleep(2);
  }
}
DI bool tags_ok(v4u a, unsigned tag) { return a.y == tag && a.w == tag; }

template <int N> DI void tie(f2 (&v)[N]) {
  if constexpr (N == 2) asm volatile("" : "+v"(v[0]), "+v"(v[1]));
  else if constexpr (N == 4) asm volatile("" : "+v"(v[0]), "+v"(v[1]), "+v"(v[2]), "+v"(v[3]));
  else static_assert(N == 2 || N == 4, "tie");
}
template <int N> DI void gelu_n(f2 (&u)[N]) {
  const float c0 = -2.0f * LOG2E * 0.7978845608028654f, c1 = c0 * 0.044715f;
  f2 e[N];
  tie(u);
#pragma unroll
  for (int r = 0; r < N; ++r) e[r] = u[r] * u[r];
#pragma unroll
  for (int r = 0; r < N; ++r) e[r] = __builtin_elementwise_fma(e[r], (f2){c1, c1}, (f2){c0, c0});
#pragma unroll
  for (int r = 0; r < N; ++r) e[r] = u[r] * e[r];
  tie(e);
#pragma unroll
  for (int r = 0; r < N; ++r) e[r] = (f2){__builtin_amdgcn_exp2f(e[r].x), __builtin_amdgcn_exp2f(e[r].y)};
  tie(e);
#pragma unroll
  for (int r = 0; r < N; ++r) e[r] = e[r] + (f2){1.0f, 1.0f};
  tie(e);
#pragma unroll
  for (int r = 0; r < N; ++r) e[r] = (f2){__builtin_amdgcn_rcpf(e[r].x), __builtin_amdgcn_rcpf(e[r].y)};
  tie(e);
#pragma unroll
  for (int r = 0; r < N; ++r) u[r] *= e[r];
}
DI void ln_bias(f2* u, f4 a, f4 cs, v2u bb, f2 st) {
  const f2 b0 = {bflo(bb.x), bfhi(bb.x)}, b1 = {bflo(bb.y), bfhi(bb.y)}, sx = {st.x, st.x}, sy = {-st.y, -st.y};
  u[0] = __builtin_elementwise_fma((f2){a[0], a[1]}, sx, __builtin_elementwise_fma(sy, (f2){cs[0], cs[1]}, b0));
  u[1] = __builtin_elementwise_fma((f2){a[2], a[3]}, sx, __builtin_elementwise_fma(sy, (f2){cs[2], cs[3]}, b1));
}
DI v2u ffn_tile(f4 a, f4 cs, v2u bb, f2 st) {
  f2 u[2];
  ln_bias(u, a, cs, bb, st);
  gelu_n<2>(u);
  return (v2u){pk(u[0].x, u[0].y), pk(u[1].x, u[1].y)};
}
DI v4u ffn_pair(f4 a0, f4 a1, f4 cs0, f4 cs1, v2u bb0, v2u bb1, f2 st) {
  f2 u[4];
  ln_bias(u, a0, cs0, bb0, st);
  ln_bias(u + 2, a1, cs1, bb1, st);
  gelu_n<4>(u);
  return (v4u){pk(u[0].x, u[0].y), pk(u[1].x, u[1].y), pk(u[2].x, u[2].y), pk(u[3].x, u[3].y)};
}
DI v2u ln_pack(f4 a, f4 cs, f2 st) {
  float v[4];
#pragma unroll
  for (int r = 0; r < 4; ++r) v[r] = __builtin_fmaf(a[r], st.x, -st.y * cs[r]);
  return (v2u){pk(v[0], v[1]), pk(v[2], v[3])};
}

__global__ __launch_bounds__(512, 2) void fused_kernel(const char* __restrict__ x, const char* __restrict__ wqkv, const char* __restrict__ wphi,
                                                       const char* __restrict__ wo, const char* __restrict__ w1, const char* __restrict__ b1,
                                                       const char* __restrict__ w2, const char* __restrict__ b2, unsigned short* __restrict__ out,
                                                       char* __restrict__ ws) {
  extern __shared__ __attribute__((aligned(16))) char smem[];
  Ctx c;
  c.L = (LAS char*)smem;
  c.lbase = (unsigned)(size_t)c.L;
  c.x = x; c.wo = wo; c.w2 = w2; c.ws = ws;
  c.tid = threadIdx.x; c.lane = c.tid & 63; c.w = __builtin_amdgcn_readfirstlane(c.tid >> 6); c.m = c.w & 3; c.nh = c.w >> 2;
  const int blk = blockIdx.x, xcd = blk & 7, idx = blk >> 3;
  c.rg = (xcd >> 2) * 8 + (idx >> 2); c.q = xcd & 3; c.jj = idx & 3; c.isQ = c.jj >> 1; c.hq = 2 * c.q + (c.jj & 1);
  c.rt = xcd * 4 + (idx >> 3); c.ct = idx & 7;
  c.rs = __builtin_amdgcn_make_buffer_rsrc((void*)ws, 0, WS_END, 0x00020000);
  int lane = c.lane, g = lane >> 4, l15 = lane & 15, q4 = l15 >> 2, p4 = lane & 3;
#define REFRESH() do { int t_ = threadIdx.x; asm volatile("" : "+v"(t_)); c.tid = t_; lane = t_ & 63; c.lane = lane; g = lane >> 4; l15 = lane & 15; q4 = l15 >> 2; p4 = lane & 3; } while (0)
  LAS char* L = c.L;
  STAMP(c, 0);
  asm volatile("" ::"s"(x), "s"(wqkv), "s"(wphi), "s"(wo), "s"(w1), "s"(b1), "s"(w2), "s"(b2), "s"(out), "s"(ws));
  const int f0u = 7 * c.q;
  const int fa = f0u + (c.isQ ? 2 + 2 * (c.jj & 1) : c.jj);
  c.u0 = c.isQ ? w1 + fa * 128 : wqkv + (8 + c.hq) * 128;
  c.u1 = c.u0 + (c.isQ ? 128 : 1024);
  c.u2 = c.isQ ? wqkv + c.hq * 128 : w1 + fa * 128;
  c.ld1 = c.isQ ? 3584 : 3072;
  c.ld2 = c.isQ ? 3072 : 3584;
  c.ush = w1 + (f0u + 6) * 128 + c.jj * 32;
  c.vX0 = (lane >> 3) * 1024 + (((lane & 7) ^ (lane >> 4)) * 16);
  c.vX1 = (c.vX0 ^ 64u) + 8 * 1024;
  c.vW0 = (lane >> 3) * c.ld1 + (((lane & 7) ^ ((lane >> 3) & 2)) * 16);
  c.vW1 = (c.vW0 ^ 64u) + 8 * c.ld1;
  c.pX = x + (size_t)(128 * c.rg + 16 * c.w) * 1024;
  c.pW = (c.nh ? c.u1 : c.u0) + (size_t)(16 * c.m) * c.ld1;
  c.ldsw = c.lbase + L_RING + c.w * 2048;

  issue_half<0, 0>(c); issue_half<0, 1>(c); issue_half<1, 0>(c); issue_half<1, 1>(c);
  c.tag = *(const __attribute__((address_space(4))) unsigned*)(ws + WS_EPOCH) + 1u;
  v4u xr, cw0, cw1;
  const int j16 = 4 * c.q + c.jj;
  gld16(xr, x + (size_t)(128 * c.rg + 8 * j16 + c.w) * 1024, lane * 16);
  const int job = (xcd >> 2) * 32 + idx;
  const int jobc = job < 52 ? job : 51;
  const int li = jobc >> 2, kq = jobc & 3;
  const int gunit = li < 6 ? (li >> 1) * 8 + 2 * c.q + (li & 1) : 24 + f0u + (li - 6);
  {
    const char* wb = li < 6 ? wqkv + gunit * 128 : w1 + (gunit - 24) * 128;
    const unsigned ld = li < 6 ? 3072 : 3584;
    const char* bb = wb + (size_t)(128 * kq + 16 * c.w) * ld;
    const unsigned v = (lane >> 3) * ld + (lane & 7) * 16;
    gld16(cw0, bb, v);
    gld16(cw1, bb + 8 * ld, v);
  }
  {
    const char* p = c.w == 0 ? b1 + fa * 128 : c.w == 1 ? b1 + (f0u + 6) * 128 + c.jj * 32 : c.w < 4 ? wphi + c.hq * 2048 + (c.w - 2) * 1024 : b2 + c.ct * 128;
    const unsigned d = c.lbase + (c.w == 0 ? L_B1 : c.w == 1 ? L_B1 + 256 : c.w < 4 ? L_WPHI + (c.w - 2) * 1024 : L_B2);
    if (c.w == 2 || c.w == 3) glds16(p, lane * 16, d);
    else glds4(p, (lane & (c.w == 0 ? 63 : c.w == 1 ? 7 : 31)) * 4, d);
  }
  issue_half<2, 0>(c); issue_half<2, 1>(c); issue_half<3, 0>(c); issue_half<3, 1>(c); issue_half<4, 0>(c); issue_half<4, 1>(c);
  issue_half<5, 0>(c); issue_half<5, 1>(c); issue_half<6, 0>(c); issue_half<6, 1>(c);
  {
    const int id = c.tid, half = id & 1, ln = id >> 1;
    const char* ax; const char* aw;
    { const int r = (ln * 205) >> 10, sg = ln - 5 * r; ax = x + (size_t)(128 * c.rg + 32 * c.jj + r) * 1024 + (3 + sg) * 128; }
    { const int lj = ln < 160 ? 0 : ln < 240 ? ln - 160 : 79, un = lj >= 40, r = lj - 40 * un; aw = (un ? c.u1 : c.u0) + (size_t)(192 + 40 * (c.rg & 7) + r) * c.ld1; }
    glds4v((c.w < 5 ? ax : aw) + half * 64, c.lbase + L_DUMMY);
  }
  {
    unsigned long long pc; unsigned len;
    asm volatile("s_getpc_b64 %0\n.Lhh_pc_here:\n\ts_mov_b32 %1, .Lhh_code_end-.Lhh_pc_here" : "=s"(pc), "=s"(len));
    if (c.w == 7) {
      unsigned o = (unsigned)(idx * 64 + lane) * 64;
      o = o < len ? o : 0;
      glds4v((const char*)pc + o, c.lbase + L_DUMMY);
    }
  }
  unsigned g1off;
  {
    const int t = c.tid;
    const int ci = t - 128 < 208 ? t - 128 : 207;
    const int un = ci >> 6;
    const int ufa = 24 + fa;
    const int ua = c.isQ ? ufa : 8 + c.hq, ub = c.isQ ? ufa + 1 : 16 + c.hq, uc = c.isQ ? c.hq : ufa;
    const int gu = un == 0 ? ua : un == 1 ? ub : un == 2 ? uc : 24 + f0u + 6;
    const int col = gu * 64 + (un == 3 ? 16 * c.jj + (ci & 15) : (ci & 63));
    g1off = t < 128 ? WS_STATS + (128 * c.rg + t) * 16 : WS_CS + col * 32;
    asm volatile("" : "+v"(g1off));
  }
  STAMP(c, 1);

  v4u xf[2][16];
  f4 acc[2][2][2];
  const f4 z4 = {0.f, 0.f, 0.f, 0.f};
#pragma unroll
  for (int a = 0; a < 2; ++a)
#pragma unroll
    for (int b = 0; b < 2; ++b)
#pragma unroll
      for (int t = 0; t < 2; ++t) acc[a][b][t] = z4;
  const unsigned xa0 = (unsigned)((32 * c.m + l15) * 128 + ((g ^ (l15 >> 1)) * 16));
  const unsigned xtt0 = c.nh * 2048, xtt1 = (1 - c.nh) * 2048;
  const unsigned wa0 = (unsigned)((8 * g + q4) * 128 + (((4 * c.nh + (p4 >> 1)) ^ ((((q4 >> 1) & 1) | ((g & 1) << 1)) << 1)) * 16) + (p4 & 1) * 8);
  unsigned xb[2][4] = {{xa0 + xtt0, xa0 + xtt1, (xa0 ^ 64) + xtt0, (xa0 ^ 64) + xtt1}, {}}, wa[2][2] = {{wa0, wa0 ^ 32}, {}};
#pragma unroll
  for (int i = 0; i < 4; ++i) { xb[1][i] = xb[0][i] + HIWIN; asm volatile("" : "+v"(xb[1][i])); }
#pragma unroll
  for (int i = 0; i < 2; ++i) { wa[1][i] = wa[0][i] + HIWIN; asm volatile("" : "+v"(wa[1][i])); }
  v4u g1a, g1b;
  v4u wfa[2][2], wfb[2][2];
  auto pass1_step = [&](auto KC) {
    constexpr int kc = decltype(KC)::value;
    constexpr unsigned sx = L_RING + SC.slot[2 * kc] * 16384, sw = L_RING + SC.slot[2 * kc + 1] * 16384;
    TS(3 * kc);
    if constexpr (kc > 0)
      asm volatile("" : "+v"(wfb[0][0]), "+v"(wfb[0][1]), "+v"(wfb[1][0]), "+v"(wfb[1][1]), "+v"(xf[0][kc > 0 ? 2 * kc - 1 : 0]), "+v"(xf[1][kc > 0 ? 2 * kc - 1 : 0]));
    if constexpr (kc == 0) {
      wait_top<top_wait(kc)>();
    } else {
      wait_vm<top_wait(kc)>();
    }
    TS(3 * kc + 1);
    bar();
    TS(3 * kc + 2);
    FENCE();
    if constexpr (kc == 5) {
      REFRESH();
      c.vP0 = (lane >> 3) * c.ld2 + (((lane & 7) ^ ((lane >> 3) & 2)) * 16);
      c.vP1 = (c.vP0 ^ 64u) + 8 * c.ld2;
      c.pP = c.u2 + (size_t)(16 * c.w) * c.ld2;
    }
    if constexpr (kc == 7) {
      REFRESH();
      c.vF0 = (lane >> 3) * 1024 + (((lane & 7) ^ ((lane >> 3) & 6)) * 16);
      c.vF1 = c.vF0 + 8 * 1024;
      const unsigned o = (16 * c.m) * 1024 + c.ct * 128;
      c.pF1 = w2 + o + c.nh * 65536; c.pF5 = w2 + o + c.nh * 5 * 65536; c.pWo = wo + o + c.nh * 65536;
    }
    constexpr int hx = sx >= HIWIN, hw = sw >= HIWIN;
    const unsigned x0 = xb[hx][0], x1 = xb[hx][1], x2 = xb[hx][2], x3 = xb[hx][3], w0 = wa[hw][0], w1_ = wa[hw][1];
    constexpr unsigned rx = sx - hx * HIWIN, rw = sw - hw * HIWIN;
#define P1_MMA(F, KS, UN, I, T) do { if constexpr (DOM_) acc[UN][I][T] = mfma32(F[UN][I], xf[T][KS], acc[UN][I][T]); FENCE(); } while (0)
#define P1_HALF(FR, FM, KSR, KSM, XA, XB, SOFF, G0, G1, DOM, TIE) {                                 \
    constexpr bool DOM_ = DOM;                                                                     \
    v2u t0, t1;                                                                                    \
    xf[0][KSR] = ld128(L, XA + rx); FENCE();                                                       \
    if constexpr (TIE) {                                                                           \
      xf[1][KSR] = ld128(L, XB + rx); FENCE();                                                     \
      t0 = trd(L, w0 + rw + SOFF); FENCE();                                                        \
      asm volatile("" : "+v"(FM[0][0]), "+v"(FM[0][1]), "+v"(FM[1][0]), "+v"(FM[1][1]), "+v"(xf[0][KSM]), "+v"(xf[1][KSM]));   \
      FENCE();                                                                                     \
      P1_MMA(FM, KSM, 0, 0, 0);                                                                    \
    } else {                                                                                       \
      P1_MMA(FM, KSM, 0, 0, 0);                                                                    \
      xf[1][KSR] = ld128(L, XB + rx); FENCE();                                                     \
      t0 = trd(L, w0 + rw + SOFF); FENCE();                                                        \
    }                                                                                              \
    P1_MMA(FM, KSM, 0, 0, 1);                                                                      \
    t1 = trd(L, w0 + rw + SOFF + 512); FR[0][0] = (v4u){t0.x, t0.y, t1.x, t1.y}; FENCE();          \
    op_m0<kc, G0>(c); FENCE();                                                                     \
    P1_MMA(FM, KSM, 0, 1, 0);                                                                      \
    FR[0][1] = trd2(L, w1_ + rw + SOFF, w1_ + rw + SOFF + 512); FENCE();                           \
    P1_MMA(FM, KSM, 0, 1, 1);                                                                      \
    t0 = trd(L, w0 + rw + SOFF + 8192); FENCE();                                                   \
    op_go<kc, G0>(c); FENCE();                                                                     \
    P1_MMA(FM, KSM, 1, 0, 0);                                                                      \
    t1 = trd(L, w0 + rw + SOFF + 8192 + 512); FR[1][0] = (v4u){t0.x, t0.y, t1.x, t1.y}; FENCE();   \
    op_m0<kc, G1>(c); FENCE();                                                                     \
    P1_MMA(FM, KSM, 1, 0, 1);                                                                      \
    FR[1][1] = trd2(L, w1_ + rw + SOFF + 8192, w1_ + rw + SOFF + 8192 + 512); FENCE();             \
    P1_MMA(FM, KSM, 1, 1, 0);                                                                      \
    op_go<kc, G1>(c); FENCE();                                                                     \
    P1_MMA(FM, KSM, 1, 1, 1);                                                                      \
  }
    P1_HALF(wfa, wfb, 2 * kc, (kc > 0 ? 2 * kc - 1 : 0), x0, x1, 0, 0, 1, (kc > 0), false)
    if constexpr (kc == 0) {
      asm volatile("s_waitcnt vmcnt(%3)" : "+v"(xr), "+v"(cw0), "+v"(cw1) : "n"(SC.cum[1] - 1 - PRO_REG_LAST) : "memory");
      const float x0v = bitsf(__builtin_amdgcn_readfirstlane(xr.x << 16));
      float d[8] = {bflo(xr.x) - x0v, bfhi(xr.x) - x0v, bflo(xr.y) - x0v, bfhi(xr.y) - x0v, bflo(xr.z) - x0v, bfhi(xr.z) - x0v, bflo(xr.w) - x0v, bfhi(xr.w) - x0v};
      float s = ((d[0] + d[1]) + (d[2] + d[3])) + ((d[4] + d[5]) + (d[6] + d[7]));
      float qq = d[0] * d[0];
#pragma unroll
      for (int e = 1; e < 8; ++e) qq = __builtin_fmaf(d[e], d[e], qq);
      s += dpp<0x128>(s); qq += dpp<0x128>(qq);
      s += dpp<0x124>(s); qq += dpp<0x124>(qq);
      s += dpp<0x122>(s); qq += dpp<0x122>(qq);
      s += dpp<0x121>(s); qq += dpp<0x121>(qq);
      sw32(s, qq);
      float sq = s + qq;
      { float a = sq, b = sq; sw16(a, b); sq = a + b; }
      const float sd = sq * (1.0f / 512.0f);
      const float m2 = bitsf(__builtin_amdgcn_readlane(fbits(sd), 32));
      if (lane == 0) {
        const float var = __builtin_fmaf(-sd, sd, m2);
        const float rstd = __builtin_amdgcn_rsqf(vmax(var, 0.f) + 1e-5f);
        __builtin_amdgcn_raw_buffer_store_b128((v4u){fbits(x0v + sd), c.tag, fbits(rstd), c.tag}, c.rs, WS_STATS + (128 * c.rg + 8 * j16 + c.w) * 16, 0, AUX_SC1);
      }
      FENCE();
    }
    if constexpr (kc == 1) {
      float cs[8] = {bflo(cw0.x) + bflo(cw1.x), bfhi(cw0.x) + bfhi(cw1.x), bflo(cw0.y) + bflo(cw1.y), bfhi(cw0.y) + bfhi(cw1.y),
                     bflo(cw0.z) + bflo(cw1.z), bfhi(cw0.z) + bfhi(cw1.z), bflo(cw0.w) + bflo(cw1.w), bfhi(cw0.w) + bfhi(cw1.w)};
#pragma unroll
      for (int e = 0; e < 4; ++e) { sw32(cs[e], cs[e + 4]); cs[e] += cs[e + 4]; }
#pragma unroll
      for (int e = 0; e < 2; ++e) { sw16(cs[e], cs[e + 2]); cs[e] += cs[e + 2]; }
      cs[0] += dpp<0x128>(cs[0]); cs[1] += dpp<0x128>(cs[1]);
      if ((lane & 8) == 0) *(LAS f2*)(L + L_SCR + c.w * 256 + ((lane & 7) * 8 + (lane >> 5) * 4 + ((lane >> 4) & 1) * 2) * 4) = (f2){cs[0], cs[1]};
      FENCE();
    }
    if constexpr (kc == 3) {
      if (c.w < 3) {
        const int id = threadIdx.x < 144 ? threadIdx.x : 143;
        const int ln = blk * 72 + (id >> 1);
        const char* a = ln < 14336 ? w2 + (size_t)ln * 128 : wo + (size_t)(ln - 14336) * 128;
        glds4v(a + (id & 1) * 64, c.lbase + L_DUMMY);
      }
      FENCE();
    }
    if constexpr (kc == 2) {
      if (c.w == 7 && job < 52) {
        float s = 0.f;
#pragma unroll
        for (int ww = 0; ww < 8; ++ww) s += *(LAS float*)(L + L_SCR + ww * 256 + lane * 4);
        __builtin_amdgcn_raw_buffer_store_b64((v2u){fbits(s), c.tag}, c.rs, WS_CS + (gunit * 64 + lane) * 32 + kq * 8, 0, AUX_SC1);
      }
      FENCE();
    }
    P1_HALF(wfb, wfa, 2 * kc + 1, 2 * kc, x2, x3, 4096, 2, 3, true, true)
    if constexpr (kc == ST_G1_ISSUE) {
      gld16_sc1(g1a, ws, g1off);
      gld16_sc1(g1b, ws, threadIdx.x < 128 ? g1off : g1off + 16);
      FENCE();
    }
  };
  pass1_step(IC<0>{});
  STAMP(c, 2);
  pass1_step(IC<1>{});
  pass1_step(IC<2>{});
  pass1_step(IC<3>{});
  pass1_step(IC<4>{});
  pass1_step(IC<5>{});
  pass1_step(IC<6>{});
  pass1_step(IC<7>{});
  STAMP(c, 3);

  TS(3 * ST_E1);
  asm volatile("" : "+v"(wfb[0][0]), "+v"(wfb[0][1]), "+v"(wfb[1][0]), "+v"(wfb[1][1]), "+v"(xf[0][15]), "+v"(xf[1][15]));
  {
      asm volatile("s_waitcnt vmcnt(%2) lgkmcnt(0)" : "+v"(g1a), "+v"(g1b) : "n"(top_wait(ST_E1)) : "memory");
      REFRESH();
      const bool isst = c.tid < 128;
      if (__builtin_expect(!__all(tags_ok(g1a, c.tag) && (isst || tags_ok(g1b, c.tag))), 0)) {
        const unsigned off = g1off;
#pragma unroll 1
        for (unsigned it = 0; it < SPIN_CAP; ++it) {
          __builtin_amdgcn_s_sleep(2);
          gld16_sc1(g1a, ws, off);
          gld16_sc1(g1b, ws, isst ? off : off + 16);
          asm volatile("s_waitcnt vmcnt(0)" : "+v"(g1a), "+v"(g1b)::"memory");
          if (__all(tags_ok(g1a, c.tag) && (isst || tags_ok(g1b, c.tag)))) break;
        }
      }
      if (isst) *(LAS f2*)(L + L_ST + c.tid * 8) = (f2){bitsf(g1a.z), bitsf(g1a.x) * bitsf(g1a.z)};
      else if (c.tid < 128 + 208) *(LAS float*)(L + L_CS + (c.tid - 128) * 4) = (bitsf(g1a.x) + bitsf(g1a.z)) + (bitsf(g1b.x) + bitsf(g1b.z));
      wait_lgkm();
  }
  TS(3 * ST_E1 + 1);
  bar();
  TS(3 * ST_E1 + 2);
  FENCE();
  REFRESH();
  const unsigned gt0 = 8 * c.rg + 2 * c.m + c.nh, gt1 = 8 * c.rg + 2 * c.m + 1 - c.nh;
  {
    f4 cs4[2][2];
#pragma unroll
    for (int un = 0; un < 2; ++un)
#pragma unroll
      for (int i = 0; i < 2; ++i) cs4[un][i] = ldf4(L, L_CS + (64 * un + 32 * c.nh + 16 * i + 4 * g) * 4);
    const f2 st0 = ldf2(L, L_ST + (16 * (2 * c.m + c.nh) + l15) * 8), st1 = ldf2(L, L_ST + (16 * (2 * c.m + 1 - c.nh) + l15) * 8);
    FENCE();
#pragma unroll
    for (int un = 0; un < 2; ++un)
#pragma unroll
      for (int i = 0; i < 2; ++i) {
        acc[un][i][0] = mfma32(wfb[un][i], xf[0][15], acc[un][i][0]);
        acc[un][i][1] = mfma32(wfb[un][i], xf[1][15], acc[un][i][1]);
      }
    FENCE();
    TSX(0);
    if (c.isQ) {
      v2u bb[2][2];
#pragma unroll
      for (int un = 0; un < 2; ++un)
#pragma unroll
        for (int i = 0; i < 2; ++i) bb[un][i] = ld64(L, L_B1 + un * 128 + c.nh * 64 + i * 32 + 8 * g);
      const unsigned ks0 = 2 * unit_pos(c.q, 2 + 2 * (c.jj & 1)) + c.nh;
      const unsigned ho0 = WS_H + (gt0 * 72 + ks0) * 1024 + lane * 16, ho1 = WS_H + (gt1 * 72 + ks0) * 1024 + lane * 16;
      {
        __builtin_amdgcn_raw_buffer_store_b128(ffn_pair(acc[0][0][0], acc[0][1][0], cs4[0][0], cs4[0][1], bb[0][0], bb[0][1], st0), c.rs, ho0, 0, AUX_SC1);
      }
      FENCE(); issue_op<ST_E1, 0>(c); FENCE();
      {
        __builtin_amdgcn_raw_buffer_store_b128(ffn_pair(acc[0][0][1], acc[0][1][1], cs4[0][0], cs4[0][1], bb[0][0], bb[0][1], st1), c.rs, ho1, 0, AUX_SC1);
      }
      FENCE(); issue_op<ST_E1, 1>(c); FENCE();
      {
        __builtin_amdgcn_raw_buffer_store_b128(ffn_pair(acc[1][0][0], acc[1][1][0], cs4[1][0], cs4[1][1], bb[1][0], bb[1][1], st0), c.rs, ho0 + 2048, 0, AUX_SC1);
      }
      FENCE();
      {
        __builtin_amdgcn_raw_buffer_store_b128(ffn_pair(acc[1][0][1], acc[1][1][1], cs4[1][0], cs4[1][1], bb[1][0], bb[1][1], st1), c.rs, ho1 + 2048, 0, AUX_SC1);
      }
    } else {
      const v4u ones = {0x3f803f80u, 0x3f803f80u, 0x3f803f80u, 0x3f803f80u};
      const v2u wt0 = trd(L, L_WPHI + (2 * c.nh) * 512 + (4 * g + q4) * 32 + p4 * 8), wt1 = trd(L, L_WPHI + (2 * c.nh + 1) * 512 + (4 * g + q4) * 32 + p4 * 8);
      f4 pk4[4];
      v2u vb[4], fp[4], fn[4];
#pragma unroll
      for (int i = 0; i < 2; ++i)
#pragma unroll
        for (int tt = 0; tt < 2; ++tt) pk4[2 * i + tt] = mfma16(i ? wt1 : wt0, ln_pack(acc[0][i][tt], cs4[0][i], tt ? st1 : st0), z4);
#pragma unroll
      for (int i = 0; i < 2; ++i)
#pragma unroll
        for (int tt = 0; tt < 2; ++tt) vb[2 * i + tt] = ln_pack(acc[1][i][tt], cs4[1][i], tt ? st1 : st0);
      TSX(1);
      FENCE(); issue_op<ST_E1, 0>(c); FENCE();
      Feat ft;
      feat_max(pk4, ft);
      TSX(2);
      FENCE(); issue_op<ST_E1, 1>(c); FENCE();
      feat_exp(pk4, ft);
      TSX(3);
      feat_norm(ft, fp, fn);
      FENCE();
      TSX(4);
      constexpr unsigned IMG_P = L_SCR, IMG_N = L_SCR + 16384, IMG_V = L_RING + SC.slot[15] * 16384;
      const unsigned wo_ = (2 * c.nh) * 4096 + (32 * c.m + l15) * 32 + ((g ^ q4) * 8);
#pragma unroll
      for (int i = 0; i < 2; ++i)
#pragma unroll
        for (int tt = 0; tt < 2; ++tt) {
          *(LAS v2u*)(L + IMG_P + wo_ + i * 4096 + tt * 512) = fp[2 * i + tt];
          *(LAS v2u*)(L + IMG_N + wo_ + i * 4096 + tt * 512) = fn[2 * i + tt];
          *(LAS v2u*)(L + IMG_V + wo_ + i * 4096 + tt * 512) = vb[2 * i + tt];
        }
      wait_lgkm();
      TSX(5);
      bar();
      TSX(6);
      const unsigned ro = (c.w >> 1) * 4096 + (4 * g + q4) * 32 + ((p4 ^ g) * 8);
      const unsigned ra = IMG_P + (c.w & 1) * 16384 + ro, rb = IMG_V + ro;
      v4u A[4], B[4];
#pragma unroll
      for (int j = 0; j < 4; ++j) { A[j] = trd2(L, ra + j * 1024, ra + j * 1024 + 512); B[j] = trd2(L, rb + j * 1024, rb + j * 1024 + 512); }
      f4 kv0 = mfma32(A[0], B[0], z4), kv1 = mfma32(A[1], B[1], z4), z0 = mfma32(A[0], ones, z4), z1 = mfma32(A[1], ones, z4);
      kv0 = mfma32(A[2], B[2], kv0); kv1 = mfma32(A[3], B[3], kv1); z0 = mfma32(A[2], ones, z0); z1 = mfma32(A[3], ones, z1);
      kv0 += kv1; z0 += z1;
      TSX(7);
      const unsigned pbase = WS_P + (c.hq * 16 + c.rg) * PART_BYTES;
      __builtin_amdgcn_raw_buffer_store_b128((v4u){fbits(kv0[0]), c.tag, fbits(kv0[1]), c.tag}, c.rs, pbase + (c.w * 128 + lane) * 16, 0, AUX_SC1);
      __builtin_amdgcn_raw_buffer_store_b128((v4u){fbits(kv0[2]), c.tag, fbits(kv0[3]), c.tag}, c.rs, pbase + (c.w * 128 + 64 + lane) * 16, 0, AUX_SC1);
      if (l15 == 0) {
        __builtin_amdgcn_raw_buffer_store_b128((v4u){fbits(z0[0]), c.tag, fbits(z0[1]), c.tag}, c.rs, pbase + (1024 + c.w * 8 + g * 2) * 16, 0, AUX_SC1);
        __builtin_amdgcn_raw_buffer_store_b128((v4u){fbits(z0[2]), c.tag, fbits(z0[3]), c.tag}, c.rs, pbase + (1024 + c.w * 8 + g * 2 + 1) * 16, 0, AUX_SC1);
      }
    }
  }
  TSX(8);
  STAMP(c, 4);
  FENCE();

  f4 acc2[2][2], accS = z4;
#pragma unroll
  for (int b = 0; b < 2; ++b)
#pragma unroll
    for (int t = 0; t < 2; ++t) acc2[b][t] = z4;
  REFRESH();
  const unsigned wb0 = (unsigned)((8 * g + q4) * 128 + (((4 * c.nh + (p4 >> 1)) ^ ((((q4 >> 1) & 1) | ((g & 1) << 1)) << 1)) * 16) + (p4 & 1) * 8);
  const unsigned sa0 = (unsigned)((8 * g + ((g & 1) << 2) + q4) * 32 + p4 * 8);
  constexpr unsigned SS = L_RING + SC.slot[IT_S] * 16384;
  unsigned wbb[2][2] = {{wb0, wb0 ^ 32}, {}};
#pragma unroll
  for (int i = 0; i < 2; ++i) { wbb[1][i] = wbb[0][i] + HIWIN; asm volatile("" : "+v"(wbb[1][i])); }
  unsigned s0 = sa0 + (SS >= HIWIN ? HIWIN : 0), s1 = (sa0 ^ 128) + (SS >= HIWIN ? HIWIN : 0);
  asm volatile("" : "+v"(s0), "+v"(s1));
  unsigned pollv;
  v4u rk0, rk1, rz0, rz1;
  const char* flagA = ws + WS_FLAG + (0 * 16 + (c.rt >> 1)) * 128;
  const char* flagB = ws + WS_FLAG + (1 * 16 + (c.rt >> 1)) * 128;
  const char* flagO = ws + WS_FLAG + (2 * 16 + (c.rt >> 1)) * 128;
  const char* rsrc = ws + WS_P + (c.hq * 16 + 2 * c.w) * PART_BYTES;
  v4u pwa[2], psa, pwb[2], psb;

  auto pass2_step = [&](auto KC) {
    constexpr int kc = decltype(KC)::value, st = 9 + kc;
    constexpr unsigned sp = L_RING + SC.slot[IT_P0 + kc] * 16384, ss = L_RING + SC.slot[IT_S] * 16384;
    TS(3 * st);
    if constexpr (kc > 0) { asm volatile("" : "+v"(pwb[0]), "+v"(pwb[1]), "+v"(psb)); wait_vm<top_wait(st)>(); }
    else wait_top<top_wait(st)>();
    TS(3 * st + 1);
    bar();
    TS(3 * st + 2);
    FENCE();
    constexpr int hp = sp >= HIWIN;
    const unsigned w0 = wbb[hp][0], w1_ = wbb[hp][1];
    constexpr unsigned rp = sp - hp * HIWIN, rs_ = ss - (ss >= HIWIN ? HIWIN : 0);
#define P2_MMA(A, F, T, KM) do { if constexpr (DOM_) A = mfma32(F, xf[T][KM], A); FENCE(); } while (0)
#define P2_Q(FR, SR, KR, FM, SM, KM, DOM, TIE) {                                                    \
    constexpr bool DOM_ = DOM;                                                                     \
    v2u t0, t1;                                                                                    \
    t0 = trd(L, w0 + rp + ((KR) & 3) * 4096); FENCE();                                             \
    if constexpr (TIE) { asm volatile("" : "+v"(FM[0]), "+v"(FM[1]), "+v"(SM)); FENCE(); }         \
    P2_MMA(acc2[0][0], FM[0], 0, KM);                                                              \
    t1 = trd(L, w0 + rp + ((KR) & 3) * 4096 + 512); FR[0] = (v4u){t0.x, t0.y, t1.x, t1.y}; FENCE(); \
    P2_MMA(acc2[0][1], FM[0], 1, KM);                                                              \
    FR[1] = trd2(L, w1_ + rp + ((KR) & 3) * 4096, w1_ + rp + ((KR) & 3) * 4096 + 512); FENCE();    \
    P2_MMA(acc2[1][0], FM[1], 0, KM);                                                              \
    t0 = trd(L, s0 + rs_ + (KR) * 1024); FENCE();                                                  \
    P2_MMA(acc2[1][1], FM[1], 1, KM);                                                              \
    t1 = trd(L, s1 + rs_ + (KR) * 1024); SR = (v4u){t0.x, t0.y, t1.x, t1.y}; FENCE();              \
    P2_MMA(accS, SM, 0, KM);                                                                       \
  }
    op_m0<st, 0>(c);
    P2_Q(pwa, psa, 4 * kc, pwb, psb, (kc > 0 ? 4 * kc - 1 : 0), (kc > 0), false)
    op_go<st, 0>(c);
    if constexpr (st == ST_PUBA) {
      if (c.isQ && threadIdx.x == 0) __builtin_amdgcn_raw_buffer_store_b32(c.tag, c.rs, WS_FLAG + (0 * 16 + c.rg) * 128 + c.hq * 4, 0, AUX_SC1);
    }
    FENCE();
    op_m0<st, 1>(c);
    P2_Q(pwb, psb, 4 * kc + 1, pwa, psa, 4 * kc, true, true)
    op_go<st, 1>(c);
    FENCE();
    P2_Q(pwa, psa, 4 * kc + 2, pwb, psb, 4 * kc + 1, true, true)
    if constexpr (st == ST_POLLA_ISSUE) { REFRESH(); gld4_sc1(pollv, flagA, (lane & 31) * 4); }
    if constexpr (st == ST_R_ISSUE) {
      REFRESH();
      const unsigned vk = c.isQ ? (64 * c.rg + lane) * 16 : 0, vz = c.isQ ? (1024 + 4 * c.rg + (lane & 3)) * 16 : 0;
      gld16_sc1(rk0, rsrc, vk);
      gld16_sc1(rk1, rsrc + PART_BYTES, vk);
      gld16_sc1(rz0, rsrc, vz);
      gld16_sc1(rz1, rsrc + PART_BYTES, vz);
    }
    FENCE();
    P2_Q(pwb, psb, 4 * kc + 3, pwa, psa, 4 * kc + 2, true, true)
  };
  pass2_step(IC<0>{});
  pass2_step(IC<1>{});
  pass2_step(IC<2>{});
  pass2_step(IC<3>{});
  STAMP(c, 5);

  v4u hf[18][2];
  v4u rg0, rg1, rgz0, rgz1;
  TS(3 * ST_E2);
  wait_top<top_wait(ST_E2)>();
  asm volatile("s_waitcnt vmcnt(%5)" : "+v"(pollv), "+v"(rk0), "+v"(rk1), "+v"(rz0), "+v"(rz1)
               : "n"(imin(extra_wait(ST_POLLA_ISSUE, ST_E2), extra_wait(ST_R_ISSUE, ST_E2))) : "memory");
  REFRESH();
  if (c.w == 0) {
    if (__builtin_expect(!__all(pollv == c.tag || (lane & 31) >= 8), 0)) spin_flags(c, flagA, 8);
  }
  if (c.isQ) {
    if (__builtin_expect(!__all(tags_ok(rk0, c.tag) && tags_ok(rk1, c.tag) && tags_ok(rz0, c.tag) && tags_ok(rz1, c.tag)), 0)) {
      const unsigned vk = (64 * c.rg + lane) * 16, vz = (1024 + 4 * c.rg + (lane & 3)) * 16;
#pragma unroll 1
      for (unsigned it = 0; it < SPIN_CAP; ++it) {
        __builtin_amdgcn_s_sleep(2);
        gld16_sc1(rk0, rsrc, vk);
        gld16_sc1(rk1, rsrc + PART_BYTES, vk);
        gld16_sc1(rz0, rsrc, vz);
        gld16_sc1(rz1, rsrc + PART_BYTES, vz);
        asm volatile("s_waitcnt vmcnt(0)" : "+v"(rk0), "+v"(rk1), "+v"(rz0), "+v"(rz1)::"memory");
        if (__all(tags_ok(rk0, c.tag) && tags_ok(rk1, c.tag) && tags_ok(rz0, c.tag) && tags_ok(rz1, c.tag))) break;
      }
    }
    *(LAS f2*)(L + L_SCR + c.w * 1024 + lane * 8) = (f2){bitsf(rk0.x) + bitsf(rk1.x), bitsf(rk0.z) + bitsf(rk1.z)};
    if (lane < 4) *(LAS f2*)(L + L_SCR + c.w * 1024 + 512 + lane * 8) = (f2){bitsf(rz0.x) + bitsf(rz1.x), bitsf(rz0.z) + bitsf(rz1.z)};
    wait_lgkm();
  }
  TS(3 * ST_E2 + 1);
  bar();
  TS(3 * ST_E2 + 2);
  FENCE();
  c.pH = ws + WS_H + (size_t)((4 * c.rt + c.m) * 72 + 2 * c.nh) * 1024;
  c.vH = lane * 16;
  f2 st0, st1;
  f4 cs2[2];
  f4 pq4[4];
  Feat ftq;
  v2u fp[4], fn[4];
  {
#pragma unroll
    for (int i = 0; i < 2; ++i) {
      acc2[i][0] = mfma32(pwb[i], xf[0][15], acc2[i][0]);
      acc2[i][1] = mfma32(pwb[i], xf[1][15], acc2[i][1]);
    }
    accS = mfma32(psb, xf[0][15], accS);
    st0 = ldf2(L, L_ST + (16 * (2 * c.m + c.nh) + l15) * 8); st1 = ldf2(L, L_ST + (16 * (2 * c.m + 1 - c.nh) + l15) * 8);
    cs2[0] = ldf4(L, L_CS + (128 + 32 * c.nh + 4 * g) * 4); cs2[1] = ldf4(L, L_CS + (128 + 32 * c.nh + 16 + 4 * g) * 4);
    const f4 csS = ldf4(L, L_CS + (192 + 4 * g) * 4);
    const v2u bS = ld64(L, L_B1 + 256 + 8 * g);
    FENCE();
    issue_op<ST_E2, 0>(c);
    issue_h<0>(c, hf);
    FENCE();
    TSX(9);
    const v2u bb0 = ld64(L, L_B1 + c.nh * 64 + 8 * g), bb1 = ld64(L, L_B1 + c.nh * 64 + 32 + 8 * g);
    const unsigned ksu = 2 * unit_pos(c.q, c.jj) + c.nh;
    if (c.isQ) {
      if (c.w == 0) {
        f2 s = {0.f, 0.f}, sz = {0.f, 0.f};
#pragma unroll
        for (int ww = 0; ww < 8; ++ww) { s += ldf2(L, L_SCR + ww * 1024 + lane * 8); sz += ldf2(L, L_SCR + ww * 1024 + 512 + (lane & 3) * 8); }
        const unsigned rb = WS_R + c.hq * PART_BYTES;
        __builtin_amdgcn_raw_buffer_store_b128((v4u){fbits(s.x), c.tag, fbits(s.y), c.tag}, c.rs, rb + (64 * c.rg + lane) * 16, 0, AUX_SC1);
        if (lane < 4) __builtin_amdgcn_raw_buffer_store_b128((v4u){fbits(sz.x), c.tag, fbits(sz.y), c.tag}, c.rs, rb + (1024 + 4 * c.rg + lane) * 16, 0, AUX_SC1);
      }
      const v2u wt0 = trd(L, L_WPHI + (2 * c.nh) * 512 + (4 * g + q4) * 32 + p4 * 8), wt1 = trd(L, L_WPHI + (2 * c.nh + 1) * 512 + (4 * g + q4) * 32 + p4 * 8);
#pragma unroll
      for (int i = 0; i < 2; ++i)
#pragma unroll
        for (int tt = 0; tt < 2; ++tt) pq4[2 * i + tt] = mfma16(i ? wt1 : wt0, ln_pack(acc2[i][tt], cs2[i], tt ? st1 : st0), z4);
      feat_max(pq4, ftq);
    } else {
      __builtin_amdgcn_raw_buffer_store_b128(ffn_pair(acc2[0][0], acc2[1][0], cs2[0], cs2[1], bb0, bb1, st0), c.rs, WS_H + (gt0 * 72 + ksu) * 1024 + lane * 16, 0, AUX_SC1);
    }
    FENCE();
    issue_op<ST_E2, 1>(c);
    issue_h<1>(c, hf);
    FENCE();
    if (c.isQ) feat_exp(pq4, ftq);
    else __builtin_amdgcn_raw_buffer_store_b128(ffn_pair(acc2[0][1], acc2[1][1], cs2[0], cs2[1], bb0, bb1, st1), c.rs, WS_H + (gt1 * 72 + ksu) * 1024 + lane * 16, 0, AUX_SC1);
    FENCE();
    TSX(10);
    issue_h<2>(c, hf);
    FENCE();
    {
      const v2u o = ffn_tile(accS, csS, bS, st0);
      const unsigned ks = 2 * unit_pos(c.q, 6) + (c.jj >> 1);
      __builtin_amdgcn_raw_buffer_store_b64(o, c.rs, WS_H + (gt0 * 72 + ks) * 1024 + lane * 16 + (c.jj & 1) * 8, 0, AUX_SC1);
    }
    FENCE();
    TSX(11);
    issue_op<ST_E2, 2>(c);
    issue_h<3>(c, hf);
    issue_op<ST_E2, 3>(c);
    if constexpr (HPRE > 4) issue_h<4>(c, hf);
    FENCE();
    if (c.isQ) feat_norm(ftq, fp, fn);
    FENCE();
    TSX(12);
    if constexpr (HPRE > 5) issue_h<5>(c, hf);
    if constexpr (HPRE > 6) issue_h<6>(c, hf);
    if constexpr (HPRE > 7) issue_h<7>(c, hf);
    {
      const char* rres_ = ws + WS_R + c.hq * PART_BYTES;
      const unsigned vk = c.isQ ? (c.tid >> 6) * 2048 + (c.tid & 63) * 16 : 0, vz = c.isQ ? (c.tid & 31) * 32 : 0;
      gld16_sc1(rg0, rres_, vk);
      gld16_sc1(rg1, rres_ + 1024, vk);
      gld16_sc1(rgz0, rres_ + 16384, vz);
      gld16_sc1(rgz1, rres_ + 16384 + 16, vz);
    }
    TSX(13);
    static_assert(HPRE >= 4 && HPRE <= 8 && ST_RG_ISSUE == ST_E2, "initial h loads");
  }
  STAMP(c, 6);
  FENCE();

  f4 accF[4];
#pragma unroll
  for (int t = 0; t < 4; ++t) accF[t] = z4;
  REFRESH();
  const unsigned fa0 = (unsigned)((64 * c.nh + 4 * g + q4) * 128 + (((p4 >> 1) ^ ((((g & 1) << 1) | (q4 >> 1)) << 1)) * 16) + (p4 & 1) * 8);
  unsigned fb[2][4] = {{fa0, fa0 ^ 32, fa0 ^ 64, fa0 ^ 96}, {}};
#pragma unroll
  for (int i = 0; i < 4; ++i) { fb[1][i] = fb[0][i] + HIWIN; asm volatile("" : "+v"(fb[1][i])); }
  v4u xres, fwa[4], fwb[4], hprev;
  const char* rres = ws + WS_R + c.hq * PART_BYTES;

  auto phase2_step = [&](auto CC) {
    constexpr int ch = decltype(CC)::value, st = ST_F0 + ch;
    constexpr unsigned sf = L_RING + SC.slot[item_of_F(ch)] * 16384;
    TS(3 * st);
    if constexpr (ch > 0) asm volatile("" : "+v"(fwb[0]), "+v"(fwb[1]), "+v"(fwb[2]), "+v"(fwb[3]));
    if constexpr (ch > 0) asm volatile("s_waitcnt vmcnt(%2)" : "+v"(hf[ch][0]), "+v"(hf[ch][1]) : "n"(top_wait(st)) : "memory");
    else asm volatile("s_waitcnt vmcnt(%2) lgkmcnt(0)" : "+v"(hf[ch][0]), "+v"(hf[ch][1]) : "n"(top_wait(st)) : "memory");
    if constexpr (st == ST_POLLB_CHECK) {
      asm volatile("s_waitcnt vmcnt(%1)" : "+v"(pollv) : "n"(extra_wait(ST_POLLB_ISSUE, st)) : "memory");
      if (c.w == 0) { if (__builtin_expect(!__all(pollv == c.tag || (lane & 31) >= 16), 0)) spin_flags(c, flagB, 16); }
    }
    if constexpr (st == ST_POLLO_CHECK) {
      asm volatile("s_waitcnt vmcnt(%1)" : "+v"(pollv) : "n"(extra_wait(ST_POLLO_ISSUE, st)) : "memory");
      if (c.w == 0) { if (__builtin_expect(!__all(pollv == c.tag || (lane & 31) >= 8), 0)) spin_flags(c, flagO, 8); }
    }
    if constexpr (st == ST_RG_CHECK) {
      asm volatile("s_waitcnt vmcnt(%4)" : "+v"(rg0), "+v"(rg1), "+v"(rgz0), "+v"(rgz1) : "n"(extra_wait(ST_RG_ISSUE, st)) : "memory");
      if (c.isQ) {
        const unsigned t = c.tid;
        if (__builtin_expect(!__all(tags_ok(rg0, c.tag) && tags_ok(rg1, c.tag) && tags_ok(rgz0, c.tag) && tags_ok(rgz1, c.tag)), 0)) {
#pragma unroll 1
          for (unsigned it = 0; it < SPIN_CAP; ++it) {
            __builtin_amdgcn_s_sleep(2);
            gld16_sc1(rg0, rres, (t >> 6) * 2048 + (t & 63) * 16);
            gld16_sc1(rg1, rres + 1024, (t >> 6) * 2048 + (t & 63) * 16);
            gld16_sc1(rgz0, rres + 16384, (t & 31) * 32);
            gld16_sc1(rgz1, rres + 16384 + 16, (t & 31) * 32);
            asm volatile("s_waitcnt vmcnt(0)" : "+v"(rg0), "+v"(rg1), "+v"(rgz0), "+v"(rgz1)::"memory");
            if (__all(tags_ok(rg0, c.tag) && tags_ok(rg1, c.tag) && tags_ok(rgz0, c.tag) && tags_ok(rgz1, c.tag))) break;
          }
        }
        *(LAS v2u*)(L + L_SCR + (t >> 7) * 1024 + (t & 63) * 16 + ((t >> 6) & 1) * 8) =
            (v2u){pk(bitsf(rg0.x), bitsf(rg0.z)), pk(bitsf(rg1.x), bitsf(rg1.z))};
        if (t < 32)
          *(LAS v2u*)(L + L_SCR + 4096 + (t >> 3) * 64 + (t & 3) * 16 + ((t >> 2) & 1) * 8) =
              (v2u){pk(bitsf(rgz0.x), bitsf(rgz0.z)), pk(bitsf(rgz1.x), bitsf(rgz1.z))};
        wait_lgkm();
      }
    }
    TS(3 * st + 1);
    bar();
    TS(3 * st + 2);
    FENCE();
    constexpr int hi = sf >= HIWIN;
    const unsigned f0 = fb[hi][0], f1 = fb[hi][1], f2_ = fb[hi][2], f3 = fb[hi][3];
    constexpr unsigned rf = sf - hi * HIWIN;
    {
      v2u t0, t1;
      fwa[0] = trd2(L, f0 + rf, f0 + rf + 2048); FENCE();
      op_m0<st, 0>(c); FENCE();
      if constexpr (ch > 0) accF[0] = mfma32(fwb[0], hprev, accF[0]);
      FENCE();
      fwa[1] = trd2(L, f1 + rf, f1 + rf + 2048); FENCE();
      if constexpr (st == ST_PUBB) {
        if (threadIdx.x == 0) __builtin_amdgcn_raw_buffer_store_b32(c.tag, c.rs, WS_FLAG + (1 * 16 + c.rg) * 128 + (4 * c.q + c.jj) * 4, 0, AUX_SC1);
      }
      if constexpr (st == ST_PUBO) {
        if (c.isQ && threadIdx.x == 0) __builtin_amdgcn_raw_buffer_store_b32(c.tag, c.rs, WS_FLAG + (2 * 16 + c.rg) * 128 + c.hq * 4, 0, AUX_SC1);
      }
      op_go<st, 0>(c); FENCE();
      if constexpr (ch > 0) accF[1] = mfma32(fwb[1], hprev, accF[1]);
      FENCE();
      fwa[2] = trd2(L, f2_ + rf, f2_ + rf + 2048); FENCE();
      op_m0<st, 1>(c); FENCE();
      if constexpr (ch > 0) accF[2] = mfma32(fwb[2], hprev, accF[2]);
      FENCE();
      fwa[3] = trd2(L, f3 + rf, f3 + rf + 2048); FENCE();
      op_go<st, 1>(c); FENCE();
      if constexpr (ch > 0) accF[3] = mfma32(fwb[3], hprev, accF[3]);
      FENCE();
      fwb[0] = trd2(L, f0 + rf + 4096, f0 + rf + 4096 + 2048); FENCE();
      asm volatile("" : "+v"(fwa[0]), "+v"(fwa[1]), "+v"(fwa[2]), "+v"(fwa[3])); FENCE();
      accF[0] = mfma32(fwa[0], hf[ch][0], accF[0]); FENCE();
      fwb[1] = trd2(L, f1 + rf + 4096, f1 + rf + 4096 + 2048); FENCE();
      constexpr int hc = h_at(st);
      if constexpr (hc >= 0) { issue_h0<(hc >= 0 ? hc : 0)>(c, hf); FENCE(); }
      accF[1] = mfma32(fwa[1], hf[ch][0], accF[1]); FENCE();
      fwb[2] = trd2(L, f2_ + rf + 4096, f2_ + rf + 4096 + 2048); FENCE();
      accF[2] = mfma32(fwa[2], hf[ch][0], accF[2]); FENCE();
      fwb[3] = trd2(L, f3 + rf + 4096, f3 + rf + 4096 + 2048); FENCE();
      if constexpr (hc >= 0) { issue_h1<(hc >= 0 ? hc : 0)>(c, hf); FENCE(); }
      if constexpr (st == ST_POLLB_ISSUE) gld4_sc1(pollv, flagB, (lane & 31) * 4);
      if constexpr (st == ST_POLLO_ISSUE) gld4_sc1(pollv, flagO, (lane & 31) * 4);
      if constexpr (ch == 14) gld16(xres, x + (size_t)(64 * c.rt) * 1024 + c.ct * 128, (c.tid >> 3) * 1024 + (c.tid & 7) * 16);
      FENCE();
      accF[3] = mfma32(fwa[3], hf[ch][0], accF[3]);
      hprev = hf[ch][1];
      FENCE();
      (void)t0; (void)t1;
    }
    if constexpr (st == ST_O) {
      if (c.isQ) {
        v2u ob[2][2];
#pragma unroll
        for (int i = 0; i < 2; ++i) {
          const int hl = 2 * c.nh + i;
          const v4u Akv = ld128(L, L_SCR + hl * 1024 + lane * 16), Az = ld128(L, L_SCR + 4096 + hl * 64 + g * 16);
#pragma unroll
          for (int tt = 0; tt < 2; ++tt) {
            const v4u ph = {fp[2 * i + tt].x, fp[2 * i + tt].y, fn[2 * i + tt].x, fn[2 * i + tt].y};
            const f4 num = mfma32(Akv, ph, z4), den = mfma32(Az, ph, z4);
            const float inv = __builtin_amdgcn_rcpf(den[0] + 1e-6f);
            ob[i][tt] = (v2u){pk(num[0] * inv, num[1] * inv), pk(num[2] * inv, num[3] * inv)};
          }
        }
        const unsigned ks = 56 + 2 * c.hq + c.nh;
        __builtin_amdgcn_raw_buffer_store_b128((v4u){ob[0][0].x, ob[0][0].y, ob[1][0].x, ob[1][0].y}, c.rs, WS_H + (gt0 * 72 + ks) * 1024 + lane * 16, 0, AUX_SC1);
        __builtin_amdgcn_raw_buffer_store_b128((v4u){ob[0][1].x, ob[0][1].y, ob[1][1].x, ob[1][1].y}, c.rs, WS_H + (gt1 * 72 + ks) * 1024 + lane * 16, 0, AUX_SC1);
      }
    }
    FENCE();
  };
  phase2_step(IC<0>{});
  phase2_step(IC<1>{});
  phase2_step(IC<2>{});
  phase2_step(IC<3>{});
  phase2_step(IC<4>{});
  phase2_step(IC<5>{});
  STAMP(c, 7);
  phase2_step(IC<6>{});
  phase2_step(IC<7>{});
  phase2_step(IC<8>{});
  phase2_step(IC<9>{});
  phase2_step(IC<10>{});
  phase2_step(IC<11>{});
  STAMP(c, 8);
  phase2_step(IC<12>{});
  phase2_step(IC<13>{});
  phase2_step(IC<14>{});
  phase2_step(IC<15>{});
  phase2_step(IC<16>{});
  phase2_step(IC<17>{});
  STAMP(c, 9);

  TS(96);
#pragma unroll
  for (int t = 0; t < 4; ++t) accF[t] = mfma32(fwb[t], hprev, accF[t]);
  REFRESH();
#pragma unroll
  for (int t = 0; t < 4; ++t) *(LAS f4*)(L + L_SCR + c.nh * 17408 + (16 * c.m + l15) * 272 + (16 * t + 4 * g) * 4) = accF[t];
  asm volatile("s_waitcnt vmcnt(0) lgkmcnt(0)" : "+v"(xres)::"memory");
  TS(97);
  bar();
  {
    const int tok = c.tid >> 3, cc = c.tid & 7;
    const unsigned a = L_SCR + tok * 272 + cc * 32;
    f4 s0 = ldf4(L, a) + ldf4(L, a + 17408), s1 = ldf4(L, a + 16) + ldf4(L, a + 16 + 17408);
    const v4u bb = ld128(L, L_B2 + cc * 16);
    v4u o;
    o.x = pk(s0[0] + bflo(xres.x) + bflo(bb.x), s0[1] + bfhi(xres.x) + bfhi(bb.x));
    o.y = pk(s0[2] + bflo(xres.y) + bflo(bb.y), s0[3] + bfhi(xres.y) + bfhi(bb.y));
    o.z = pk(s1[0] + bflo(xres.z) + bflo(bb.z), s1[1] + bfhi(xres.z) + bfhi(bb.z));
    o.w = pk(s1[2] + bflo(xres.w) + bflo(bb.w), s1[3] + bfhi(xres.w) + bfhi(bb.w));
    *(v4u*)((char*)out + (size_t)(64 * c.rt + tok) * 1024 + c.ct * 128 + cc * 16) = o;
  }
  asm volatile(".Lhh_code_end:");
  if (blk == 0 && c.tid == 0) *(unsigned*)(ws + WS_EPOCH) = c.tag;
  STAMP(c, 10);
}

}

extern "C" void kernel_launch(void* const* d_in, const int* in_sizes, int n_in, void* d_out, int out_size, void* d_ws, size_t ws_size,
                              hipStream_t stream) {
  (void)in_sizes; (void)n_in; (void)out_size; (void)ws_size;
  (void)hipFuncSetAttribute((const void*)hh::fused_kernel, hipFuncAttributeMaxDynamicSharedMemorySize, hh::L_END);
  hh::fused_kernel<<<256, 512, hh::L_END, stream>>>((const char*)d_in[0], (const char*)d_in[1], (const char*)d_in[2], (const char*)d_in[3],
                                                    (const char*)d_in[5], (const char*)d_in[6], (const char*)d_in[7], (const char*)d_in[8],
                                                    (unsigned short*)d_out, (char*)d_ws);
}
```

```cpp
#include <hip/hip_runtime.h>
#include <stdint.h>
#include <type_traits>
#define OUT_NT

namespace hh {

#define LAS __attribute__((address_space(3)))
typedef unsigned v4u __attribute__((ext_vector_type(4)));
typedef unsigned v2u __attribute__((ext_vector_type(2)));
typedef float f4 __attribute__((ext_vector_type(4)));
typedef float f2 __attribute__((ext_vector_type(2)));
typedef short s4 __attribute__((ext_vector_type(4)));
typedef __bf16 b8 __attribute__((ext_vector_type(8)));
typedef __bf16 b2 __attribute__((ext_vector_type(2)));
template <int V> using IC = std::integral_constant<int, V>;

constexpr unsigned WS_EPOCH = 0;
constexpr unsigned WS_FLAG = 256;
constexpr unsigned WS_STATS = 8192;
constexpr unsigned WS_CS = WS_STATS + 2048 * 16;
constexpr unsigned PART_BYTES = 17408;
constexpr unsigned WS_P = WS_CS + 3328 * 32;
constexpr unsigned WS_R = WS_P + 8 * 16 * PART_BYTES;
constexpr unsigned WS_H = (WS_R + 8 * PART_BYTES + 4095u) & ~4095u;
constexpr unsigned WS_DBG = WS_H + 128 * 72 * 1024;
constexpr unsigned WS_END = WS_DBG;

constexpr int NSLOT = 7;
constexpr unsigned L_RING = 0;
constexpr unsigned L_SCR = NSLOT * 16384;
constexpr unsigned SCRW = 5248;
constexpr unsigned L_CS = L_SCR + 8 * SCRW;
constexpr unsigned L_ST = L_CS + 1024;
constexpr unsigned L_B1 = L_ST + 1024;
constexpr unsigned L_WPHI = L_B1 + 512;
constexpr unsigned L_B2 = L_WPHI + 2048;
constexpr unsigned L_DUMMY = L_B2 + 256;
constexpr unsigned L_END = L_DUMMY + 256;
static_assert(L_END <= 163840, "LDS");

constexpr int NITEM = 39, NSTEP = 32;
constexpr int ST_E1 = 8, ST_E2 = 13, ST_F0 = 14;
constexpr int IT_P0 = 16, IT_F0 = 20, IT_S = 21;
constexpr int item_of_F(int c) { return c == 0 ? IT_F0 : IT_S + c; }
#define HPRE_N 4
constexpr int HPRE = HPRE_N;
constexpr int DH = 3;
constexpr int ST_G1_ISSUE = 5, ST_G1_CHECK = 8;
#define PUBA_STEP 10
constexpr int ST_PUBA = PUBA_STEP;
constexpr int ST_POLLA_ISSUE = 12;
constexpr int ST_R_ISSUE = 11;
constexpr int ST_PUBB = 15;
constexpr int E2_OPS_AFTER_STORES = 4 + 2 * (HPRE - 4);
constexpr int ST_POLLB_ISSUE = 17, ST_POLLB_CHECK = 19;
constexpr int ST_RG_ISSUE = 13, ST_RG_CHECK = 16;
constexpr int ST_O = 17;
constexpr int ST_PUBO = 20;
constexpr int ST_POLLO_ISSUE = 23, ST_POLLO_CHECK = 25;
static_assert(ST_F0 + 8 - DH == ST_POLLB_CHECK && ST_F0 + 14 - DH == ST_POLLO_CHECK, "flag checks sit in front of the first dependent loads");

struct Sched {
  int slot[NITEM], issue[NITEM], first[NITEM], last[NITEM];
  int cum[NSTEP + 1];
  int pos_item[2][NITEM];
  int pos_h[2][18];
  int h_issue[18];
  int pos_g1[2];
};
#define NPRO_N 3
constexpr int NPRO = NPRO_N;
constexpr int PRO_REG_LAST = 6;
constexpr Sched make_sched() {
  Sched s{};
  for (int i = 0; i < NITEM; ++i) {
    if (i < 16) { s.first[i] = i >> 1; s.last[i] = i >> 1; }
    else if (i < IT_F0) { s.first[i] = 9 + (i - IT_P0); s.last[i] = s.first[i]; }
    else if (i == IT_F0) { s.first[i] = ST_F0; s.last[i] = ST_F0; }
    else if (i == IT_S) { s.first[i] = 9; s.last[i] = 12; }
    else { s.first[i] = ST_F0 + (i - IT_S); s.last[i] = s.first[i]; }
  }
  s.last[15] = ST_E1;
  for (int c = 0; c < 18; ++c) s.h_issue[c] = c < HPRE ? ST_E2 : c < 8 ? ST_F0 + c - HPRE : ST_F0 + c - DH;
  int n = 0;
  int next = 0;
  int owner[NSLOT] = {};
  for (int k = 0; k < NSLOT; ++k) owner[k] = -2;
  for (int k = 0; k < NPRO; ++k) {
    s.slot[next] = k; s.issue[next] = -1; owner[k] = next; n += 2; s.pos_item[0][next] = s.pos_item[1][next] = n - 1; ++next;
    if (k == 1) n += 4;
  }
  for (int st = 0; st < NSTEP; ++st) {
    s.cum[st] = n;
    const int first_new = next;
    for (int rep = 0; rep < NSLOT; ++rep) {
      int best = -1;
      for (int k = 0; k < NSLOT; ++k)
        if ((owner[k] == -2 || (owner[k] >= 0 && s.last[owner[k]] < st)) && (best < 0 || owner[k] < owner[best])) best = k;
      if (best < 0) break;
      if (next < NITEM) { s.slot[next] = best; s.issue[next] = st; owner[best] = next; ++next; }
      else owner[best] = -1;
    }
    int nreg = 0;
    for (int c = 0; c < 18; ++c) if (s.h_issue[c] == st) nreg += 2;
    if (st == ST_G1_ISSUE) nreg += 2;
    const int nring = 2 * (next - first_new);
    for (int o = 0; o < 2; ++o) {
      int p = n + (o == 0 ? 0 : nreg);
      for (int i = first_new; i < next; ++i) { p += 2; s.pos_item[o][i] = p - 1; }
      p = n + (o == 0 ? nring : 0);
      for (int c = 0; c < 18; ++c) if (s.h_issue[c] == st) { p += 2; s.pos_h[o][c] = p - 1; }
      if (st == ST_G1_ISSUE) { p += 2; s.pos_g1[o] = p - 1; }
    }
    n += nring + nreg;
  }
  s.cum[NSTEP] = n;
  return s;
}
constexpr Sched SC = make_sched();
constexpr int imin(int a, int b) { return a < b ? a : b; }
constexpr int imax(int a, int b) { return a > b ? a : b; }
constexpr int top_wait(int st) {
  int need = -1;
  for (int o = 0; o < 2; ++o) {
    for (int i = 0; i < NITEM; ++i) if (SC.first[i] == st) need = imax(need, SC.pos_item[o][i]);
    if (st >= ST_F0) need = imax(need, SC.pos_h[o][st - ST_F0]);
    if (st == ST_G1_CHECK) need = imax(need, SC.pos_g1[o]);
  }
  int n = need < 0 ? 63 : SC.cum[st] - 1 - need;
  if (st == 9) n += 2;
  if (st == ST_PUBA) n = imin(n, SC.cum[st] - SC.cum[ST_E1 + 1]);
  if (st == ST_PUBB) n = imin(n, SC.cum[st] - SC.cum[ST_E2 + 1] + E2_OPS_AFTER_STORES);
  if (st == ST_PUBO) n = imin(n, SC.cum[st] - SC.cum[ST_O + 1]);
  return n > 63 ? 63 : n;
}
constexpr int extra_wait(int a, int b) { int n = SC.cum[b] - SC.cum[a + 1]; return n > 63 ? 63 : n; }
constexpr int h_at(int st) {
  for (int c = 0; c < 18; ++c) if (SC.h_issue[c] == st) return c;
  return -1;
}
constexpr int nth_item(int st, int k) {
  for (int i = 0; i < NITEM; ++i) if (SC.issue[i] == st) { if (k == 0) return i; --k; }
  return -1;
}
static_assert(nth_item(0, 4) < 0 && nth_item(ST_E1, 0) == IT_S && nth_item(ST_E1, 1) < 0 && nth_item(ST_E2, 2) < 0 && nth_item(9, 1) < 0 && nth_item(10, 1) < 0 && nth_item(11, 1) < 0 && nth_item(12, 1) < 0, "schedule shape");

__host__ __device__ constexpr int unit_pos(int q, int off) { return off >= 2 && off <= 5 ? 4 * q + (off - 2) : 16 + 3 * q + (off == 6 ? 2 : off); }
__host__ __device__ constexpr int pos_unit(int pos) { return pos < 16 ? 7 * (pos >> 2) + 2 + (pos & 3) : 7 * ((pos - 16) / 3) + (((pos - 16) % 3) == 2 ? 6 : (pos - 16) % 3); }

#define DI __device__ __forceinline__
DI void glds16(const void* sbase, unsigned voff, unsigned lds) {
  asm volatile("s_mov_b32 m0, %2\n\ts_nop 0\n\tglobal_load_lds_dwordx4 %0, %1" ::"v"(voff), "s"(sbase), "s"(lds) : "memory");
}
DI void set_m0(unsigned lds) { asm volatile("s_mov_b32 m0, %0" ::"s"(lds) : "memory"); }
template <bool NT = false> DI void glds16_m0(const void* sbase, unsigned voff) {
  if constexpr (NT) asm volatile("global_load_lds_dwordx4 %0, %1 nt" ::"v"(voff), "s"(sbase) : "memory");
  else asm volatile("global_load_lds_dwordx4 %0, %1" ::"v"(voff), "s"(sbase) : "memory");
}
#define NT_X 0
#define NT_W 0
#define NT_F 0
DI void glds4(const void* sbase, unsigned voff, unsigned lds) {
  asm volatile("s_mov_b32 m0, %2\n\ts_nop 0\n\tglobal_load_lds_dword %0, %1" ::"v"(voff), "s"(sbase), "s"(lds) : "memory");
}
DI void glds4v(const void* vaddr, unsigned lds) {
  asm volatile("s_mov_b32 m0, %1\n\ts_nop 0\n\tglobal_load_lds_dword %0, off" ::"v"(vaddr), "s"(lds) : "memory");
}
DI void gld16(v4u& d, const void* sbase, unsigned voff) {
  asm volatile("global_load_dwordx4 %0, %1, %2" : "=v"(d) : "v"(voff), "s"(sbase) : "memory");
}
DI void gld16_sc1(v4u& d, const void* sbase, unsigned voff) {
  asm volatile("global_load_dwordx4 %0, %1, %2 sc1" : "=v"(d) : "v"(voff), "s"(sbase) : "memory");
}
template <int OFF> DI void gld16_sc1_off(v4u& d, const void* sbase, unsigned voff) {
  asm volatile("global_load_dwordx4 %0, %1, %2 offset:%3 sc1" : "=v"(d) : "v"(voff), "s"(sbase), "n"(OFF) : "memory");
}
DI void gld4_sc1(unsigned& d, const void* sbase, unsigned voff) {
  asm volatile("global_load_dword %0, %1, %2 sc1" : "=v"(d) : "v"(voff), "s"(sbase) : "memory");
}
template <int N> DI void wait_top() { asm volatile("s_waitcnt vmcnt(%0) lgkmcnt(0)" ::"n"(N) : "memory"); }
template <int N> DI void wait_vm() { asm volatile("s_waitcnt vmcnt(%0)" ::"n"(N) : "memory"); }
DI void wait_lgkm() { asm volatile("s_waitcnt lgkmcnt(0)" ::: "memory"); }
DI void bar() { asm volatile("" ::: "memory"); __builtin_amdgcn_s_barrier(); asm volatile("" ::: "memory"); }
#define FENCE() __builtin_amdgcn_sched_barrier(0)

DI unsigned pk(float a, float b) { b2 r = {(__bf16)a, (__bf16)b}; return __builtin_bit_cast(unsigned, r); }
DI float bflo(unsigned u) { return __builtin_bit_cast(float, u << 16); }
DI float bfhi(unsigned u) { return __builtin_bit_cast(float, u & 0xffff0000u); }
DI unsigned fbits(float f) { return __builtin_bit_cast(unsigned, f); }
DI float bitsf(unsigned u) { return __builtin_bit_cast(float, u); }
DI f4 mfma32(v4u a, v4u b, f4 c) { return __builtin_amdgcn_mfma_f32_16x16x32_bf16(__builtin_bit_cast(b8, a), __builtin_bit_cast(b8, b), c, 0, 0, 0); }
DI f4 mfma16(v2u a, v2u b, f4 c) { return __builtin_amdgcn_mfma_f32_16x16x16bf16_1k(__builtin_bit_cast(s4, a), __builtin_bit_cast(s4, b), c, 0, 0, 0); }
DI v2u trd(LAS char* L, unsigned a) { return __builtin_bit_cast(v2u, __builtin_amdgcn_ds_read_tr16_b64_v4i16((LAS s4*)(L + a))); }
DI v4u trd2(LAS char* L, unsigned a0, unsigned a1) { v2u x = trd(L, a0), y = trd(L, a1); return (v4u){x.x, x.y, y.x, y.y}; }
DI v4u ld128(LAS char* L, unsigned a) { return *(LAS v4u*)(L + a); }
DI f4 ldf4(LAS char* L, unsigned a) { return *(LAS f4*)(L + a); }
DI f2 ldf2(LAS char* L, unsigned a) { return *(LAS f2*)(L + a); }
DI v2u ld64(LAS char* L, unsigned a) { return *(LAS v2u*)(L + a); }

template <int CTRL> DI float dpp(float v) { return bitsf((unsigned)__builtin_amdgcn_update_dpp(0, (int)fbits(v), CTRL, 0xf, 0xf, true)); }
DI void sw32(float& a, float& b) { auto r = __builtin_amdgcn_permlane32_swap(fbits(a), fbits(b), false, false); a = bitsf((unsigned)r[0]); b = bitsf((unsigned)r[1]); }
DI void sw16(float& a, float& b) { auto r = __builtin_amdgcn_permlane16_swap(fbits(a), fbits(b), false, false); a = bitsf((unsigned)r[0]); b = bitsf((unsigned)r[1]); }
DI float xsum(float v) { float a = v, b = v; sw16(a, b); v = a + b; a = v; b = v; sw32(a, b); return a + b; }
DI float rowsum16(float v) { v += dpp<0x128>(v); v += dpp<0x124>(v); v += dpp<0x122>(v); v += dpp<0x121>(v); return v; }
DI float wavesum(float v) { return xsum(rowsum16(v)); }

constexpr float LOG2E = 1.4426950408889634f;
DI float vmax(float a, float b) { return __builtin_elementwise_maximum(a, b); }
DI float vmin(float a, float b) { return __builtin_elementwise_minimum(a, b); }
template <class OP> DI float fold4(float a0, float a1, float a2, float a3, OP op) {
  sw32(a0, a1); sw32(a2, a3);
  float m01 = op(a0, a1), m23 = op(a2, a3);
  sw16(m01, m23);
  return op(m01, m23);
}
DI void unfold4(float m, float (&o)[4]) {
  float a = m, b = m;
  sw16(a, b);
  float a2 = a, b2 = b;
  sw32(a, a2); sw32(b, b2);
  o[0] = a; o[1] = a2; o[2] = b; o[3] = b2;
}
struct Feat { float a[4], b[4]; f2 ep[4][2], en[4][2]; };
DI void tie8(f2 (&a)[4][2]) { asm volatile("" : "+v"(a[0][0]), "+v"(a[0][1]), "+v"(a[1][0]), "+v"(a[1][1]), "+v"(a[2][0]), "+v"(a[2][1]), "+v"(a[3][0]), "+v"(a[3][1])); }
DI void feat_max(const f4 (&p)[4], Feat& f) {
  float mx[4], mn[4];
#pragma unroll
  for (int t = 0; t < 4; ++t) {
    mx[t] = vmax(vmax(vmax(p[t][0], p[t][1]), p[t][2]), p[t][3]);
    mn[t] = vmin(vmin(vmin(p[t][0], p[t][1]), p[t][2]), p[t][3]);
  }
  const float MX = fold4(mx[0], mx[1], mx[2], mx[3], [](float a, float b) { return vmax(a, b); }) * -LOG2E;
  const float MN = fold4(mn[0], mn[1], mn[2], mn[3], [](float a, float b) { return vmin(a, b); }) * LOG2E;
  unfold4(MX, f.a);
  unfold4(MN, f.b);
}
DI void feat_exp(const f4 (&p)[4], Feat& f) {
#pragma unroll
  for (int t = 0; t < 4; ++t)
#pragma unroll
    for (int h = 0; h < 2; ++h) {
      const f2 v = {p[t][2 * h], p[t][2 * h + 1]};
      f.ep[t][h] = __builtin_elementwise_fma(v, (f2){LOG2E, LOG2E}, (f2){f.a[t], f.a[t]});
      f.en[t][h] = __builtin_elementwise_fma(v, (f2){-LOG2E, -LOG2E}, (f2){f.b[t], f.b[t]});
    }
  tie8(f.ep); tie8(f.en);
#pragma unroll
  for (int t = 0; t < 4; ++t)
#pragma unroll
    for (int h = 0; h < 2; ++h) {
      f.ep[t][h] = (f2){__builtin_amdgcn_exp2f(f.ep[t][h].x), __builtin_amdgcn_exp2f(f.ep[t][h].y)};
      f.en[t][h] = (f2){__builtin_amdgcn_exp2f(f.en[t][h].x), __builtin_amdgcn_exp2f(f.en[t][h].y)};
    }
  tie8(f.ep); tie8(f.en);
}
DI void feat_norm(const Feat& f, v2u (&pos)[4], v2u (&neg)[4]) {
  float sp[4], sn[4];
#pragma unroll
  for (int t = 0; t < 4; ++t) {
    const f2 a = f.ep[t][0] + f.ep[t][1], b = f.en[t][0] + f.en[t][1];
    sp[t] = a.x + a.y;
    sn[t] = b.x + b.y;
  }
  const float IP = __builtin_amdgcn_rcpf(fold4(sp[0], sp[1], sp[2], sp[3], [](float a_, float b_) { return a_ + b_; }));
  const float IN = __builtin_amdgcn_rcpf(fold4(sn[0], sn[1], sn[2], sn[3], [](float a_, float b_) { return a_ + b_; }));
  float ip[4], in[4];
  unfold4(IP, ip);
  unfold4(IN, in);
#pragma unroll
  for (int t = 0; t < 4; ++t) {
    const f2 a0 = f.ep[t][0] * ip[t], a1 = f.ep[t][1] * ip[t], b0 = f.en[t][0] * in[t], b1 = f.en[t][1] * in[t];
    pos[t] = (v2u){pk(a0.x, a0.y), pk(a1.x, a1.y)};
    neg[t] = (v2u){pk(b0.x, b0.y), pk(b1.x, b1.y)};
  }
}

template <unsigned OFF> DI unsigned win(unsigned base) {
  if constexpr (OFF >= 65536u - 16384u) { unsigned b = base + (OFF & ~16383u); asm volatile("" : "+v"(b)); return b; }
  else return base;
}
template <unsigned OFF> constexpr unsigned rem() { return OFF >= 65536u - 16384u ? (OFF & 16383u) : OFF; }
constexpr unsigned HIWIN = 49152;
#define PRIO(k) do { } while (0)
#define PRIO_OFF() do { } while (0)
#define AUX_SC1 16
constexpr unsigned SPIN_CAP = 1u << 16;

struct Ctx {
  LAS char* L;
  unsigned lbase;
  const char *x, *wo, *w2;
  char* ws;
  __amdgpu_buffer_rsrc_t rs;
  unsigned tag;
  int tid, lane, w, m, nh;
  int rg, q, jj, hq, isQ, rt, ct;
  const char *u0, *u1, *u2, *ush;
  unsigned ld1, ld2;
  unsigned vX0, vX1, vW0, vW1, vP0, vP1, vF0, vF1;
  const char *pX, *pW, *pP, *pF1, *pF5, *pWo;
  const char* pH;
  unsigned vH;
  unsigned ldsw;
};

#define TS(i) do { } while (0)
#define TSX(i) do { } while (0)
#define STAMP(c, i) do { } while (0)

template <int I, int U> DI void half_m0(const Ctx& c) { set_m0(c.ldsw + SC.slot[I] * 16384 + U * 1024); }
template <int I, int U> DI void half_go(const Ctx& c) {
  if constexpr (I < 16 && (I & 1) == 0) {
    glds16_m0<NT_X>(c.pX + (I >> 1) * 128, U ? c.vX1 : c.vX0);
  } else if constexpr (I < 16) {
    glds16_m0<NT_W>(c.pW + (size_t)(64 * (I >> 1)) * c.ld1, U ? c.vW1 : c.vW0);
  } else if constexpr (I == IT_S) {
    const unsigned rr = c.lane >> 1;
    glds16_m0<NT_W>(c.ush + (size_t)(64 * c.w + 32 * U) * 3584, (rr ^ ((rr & 8) >> 1)) * 3584 + (c.lane & 1) * 16);
  } else if constexpr (I < IT_F0) {
    glds16_m0<NT_W>(c.pP + (size_t)(128 * (I - IT_P0)) * c.ld2, U ? c.vP1 : c.vP0);
  } else {
    constexpr int C = I == IT_F0 ? 0 : I - IT_S;
    if constexpr (C < 14) {
      constexpr int d = pos_unit(2 * C + 1) - pos_unit(2 * C);
      static_assert(d == 1 || d == 5, "unit distance inside a chunk");
      glds16_m0<NT_F>((d == 1 ? c.pF1 : c.pF5) + pos_unit(2 * C) * 65536, U ? c.vF1 : c.vF0);
    } else {
      glds16_m0<NT_F>(c.pWo + (C - 14) * 131072, U ? c.vF1 : c.vF0);
    }
  }
}
template <int I, int U> DI void issue_half(const Ctx& c) { half_m0<I, U>(c); asm volatile("s_nop 0"); half_go<I, U>(c); }
template <int ST, int K> DI void op_m0(const Ctx& c) { constexpr int I = nth_item(ST, K >> 1); if constexpr (I >= 0) half_m0<I, (K & 1)>(c); }
template <int ST, int K> DI void op_go(const Ctx& c) { constexpr int I = nth_item(ST, K >> 1); if constexpr (I >= 0) half_go<I, (K & 1)>(c); }
template <int ST, int K> DI void issue_op(const Ctx& c) { constexpr int I = nth_item(ST, K >> 1); if constexpr (I >= 0) issue_half<I, (K & 1)>(c); }
template <int C> DI void issue_h0(const Ctx& c, v4u (&hf)[18][2]) { gld16_sc1_off<0>(hf[C][0], c.pH + C * 4096, c.vH); }
template <int C> DI void issue_h1(const Ctx& c, v4u (&hf)[18][2]) { gld16_sc1_off<1024>(hf[C][1], c.pH + C * 4096, c.vH); }
template <int C> DI void issue_h(const Ctx& c, v4u (&hf)[18][2]) { issue_h0<C>(c, hf); issue_h1<C>(c, hf); }

DI void spin_flags(const Ctx& c, const char* line, int n) {
#pragma unroll 1
  for (unsigned it = 0; it < SPIN_CAP; ++it) {
    unsigned v;
    gld4_sc1(v, line, (unsigned)(c.lane & 31) * 4);
    asm volatile("s_waitcnt vmcnt(0)" : "+v"(v)::"memory");
    if (__all(v == c.tag || (c.lane & 31) >= n)) return;
    __builtin_amdgcn_s_sleep(2);
  }
}
DI bool tags_ok(v4u a, unsigned tag) { return a.y == tag && a.w == tag; }

template <int N> DI void tie(f2 (&v)[N]) {
  if constexpr (N == 2) asm volatile("" : "+v"(v[0]), "+v"(v[1]));
  else if constexpr (N == 4) asm volatile("" : "+v"(v[0]), "+v"(v[1]), "+v"(v[2]), "+v"(v[3]));
  else static_assert(N == 2 || N == 4, "tie");
}
template <int N> DI void gelu_n(f2 (&u)[N]) {
  const float c0 = -2.0f * LOG2E * 0.7978845608028654f, c1 = c0 * 0.044715f;
  f2 e[N];
  tie(u);
#pragma unroll
  for (int r = 0; r < N; ++r) e[r] = u[r] * u[r];
#pragma unroll
  for (int r = 0; r < N; ++r) e[r] = __builtin_elementwise_fma(e[r], (f2){c1, c1}, (f2){c0, c0});
#pragma unroll
  for (int r = 0; r < N; ++r) e[r] = u[r] * e[r];
  tie(e);
#pragma unroll
  for (int r = 0; r < N; ++r) e[r] = (f2){__builtin_amdgcn_exp2f(e[r].x), __builtin_amdgcn_exp2f(e[r].y)};
  tie(e);
#pragma unroll
  for (int r = 0; r < N; ++r) e[r] = e[r] + (f2){1.0f, 1.0f};
  tie(e);
#pragma unroll
  for (int r = 0; r < N; ++r) e[r] = (f2){__builtin_amdgcn_rcpf(e[r].x), __builtin_amdgcn_rcpf(e[r].y)};
  tie(e);
#pragma unroll
  for (int r = 0; r < N; ++r) u[r] *= e[r];
}
DI void ln_bias(f2* u, f4 a, f4 cs, v2u bb, f2 st) {
  const f2 b0 = {bflo(bb.x), bfhi(bb.x)}, b1 = {bflo(bb.y), bfhi(bb.y)}, sx = {st.x, st.x}, sy = {-st.y, -st.y};
  u[0] = __builtin_elementwise_fma((f2){a[0], a[1]}, sx, __builtin_elementwise_fma(sy, (f2){cs[0], cs[1]}, b0));
  u[1] = __builtin_elementwise_fma((f2){a[2], a[3]}, sx, __builtin_elementwise_fma(sy, (f2){cs[2], cs[3]}, b1));
}
DI v2u ffn_tile(f4 a, f4 cs, v2u bb, f2 st) {
  f2 u[2];
  ln_bias(u, a, cs, bb, st);
  gelu_n<2>(u);
  return (v2u){pk(u[0].x, u[0].y), pk(u[1].x, u[1].y)};
}
DI v4u ffn_pair(f4 a0, f4 a1, f4 cs0, f4 cs1, v2u bb0, v2u bb1, f2 st) {
  f2 u[4];
  ln_bias(u, a0, cs0, bb0, st);
  ln_bias(u + 2, a1, cs1, bb1, st);
  gelu_n<4>(u);
  return (v4u){pk(u[0].x, u[0].y), pk(u[1].x, u[1].y), pk(u[2].x, u[2].y), pk(u[3].x, u[3].y)};
}
DI v2u ln_pack(f4 a, f4 cs, f2 st) {
  float v[4];
#pragma unroll
  for (int r = 0; r < 4; ++r) v[r] = __builtin_fmaf(a[r], st.x, -st.y * cs[r]);
  return (v2u){pk(v[0], v[1]), pk(v[2], v[3])};
}

__global__ __launch_bounds__(512, 2) void fused_kernel(const char* __restrict__ x, const char* __restrict__ wqkv, const char* __restrict__ wphi,
                                                       const char* __restrict__ wo, const char* __restrict__ w1, const char* __restrict__ b1,
                                                       const char* __restrict__ w2, const char* __restrict__ b2, unsigned short* __restrict__ out,
                                                       char* __restrict__ ws) {
  extern __shared__ __attribute__((aligned(16))) char smem[];
  Ctx c;
  c.L = (LAS char*)smem;
  c.lbase = (unsigned)(size_t)c.L;
  c.x = x; c.wo = wo; c.w2 = w2; c.ws = ws;
  c.tid = threadIdx.x; c.lane = c.tid & 63; c.w = __builtin_amdgcn_readfirstlane(c.tid >> 6); c.m = c.w & 3; c.nh = c.w >> 2;
  const int blk = blockIdx.x, xcd = blk & 7, idx = blk >> 3;
  c.rg = (xcd >> 2) * 8 + (idx >> 2); c.q = xcd & 3; c.jj = idx & 3; c.isQ = c.jj >> 1; c.hq = 2 * c.q + (c.jj & 1);
  c.rt = xcd * 4 + (idx >> 3); c.ct = idx & 7;
  c.rs = __builtin_amdgcn_make_buffer_rsrc((void*)ws, 0, WS_END, 0x00020000);
  int lane = c.lane, g = lane >> 4, l15 = lane & 15, q4 = l15 >> 2, p4 = lane & 3;
#define REFRESH() do { int t_ = threadIdx.x; asm volatile("" : "+v"(t_)); c.tid = t_; lane = t_ & 63; c.lane = lane; g = lane >> 4; l15 = lane & 15; q4 = l15 >> 2; p4 = lane & 3; } while (0)
  LAS char* L = c.L;
  STAMP(c, 0);
  asm volatile("" ::"s"(x), "s"(wqkv), "s"(wphi), "s"(wo), "s"(w1), "s"(b1), "s"(w2), "s"(b2), "s"(out), "s"(ws));
  const int f0u = 7 * c.q;
  const int fa = f0u + (c.isQ ? 2 + 2 * (c.jj & 1) : c.jj);
  c.u0 = c.isQ ? w1 + fa * 128 : wqkv + (8 + c.hq) * 128;
  c.u1 = c.u0 + (c.isQ ? 128 : 1024);
  c.u2 = c.isQ ? wqkv + c.hq * 128 : w1 + fa * 128;
  c.ld1 = c.isQ ? 3584 : 3072;
  c.ld2 = c.isQ ? 3072 : 3584;
  c.ush = w1 + (f0u + 6) * 128 + c.jj * 32;
  c.vX0 = (lane >> 3) * 1024 + (((lane & 7) ^ (lane >> 4)) * 16);
  c.vX1 = (c.vX0 ^ 64u) + 8 * 1024;
  c.vW0 = (lane >> 3) * c.ld1 + (((lane & 7) ^ ((lane >> 3) & 2)) * 16);
  c.vW1 = (c.vW0 ^ 64u) + 8 * c.ld1;
  c.pX = x + (size_t)(128 * c.rg + 16 * c.w) * 1024;
  c.pW = (c.nh ? c.u1 : c.u0) + (size_t)(16 * c.m) * c.ld1;
  c.ldsw = c.lbase + L_RING + c.w * 2048;

  issue_half<0, 0>(c); issue_half<0, 1>(c); issue_half<1, 0>(c); issue_half<1, 1>(c);
  c.tag = *(const __attribute__((address_space(4))) unsigned*)(ws + WS_EPOCH) + 1u;
  v4u xr, cw0, cw1;
  const int j16 = 4 * c.q + c.jj;
  gld16(xr, x + (size_t)(128 * c.rg + 8 * j16 + c.w) * 1024, lane * 16);
  const int job = (xcd >> 2) * 32 + idx;
  const int jobc = job < 52 ? job : 51;
  const int li = jobc >> 2, kq = jobc & 3;
  const int gunit = li < 6 ? (li >> 1) * 8 + 2 * c.q + (li & 1) : 24 + f0u + (li - 6);
  {
    const char* wb = li < 6 ? wqkv + gunit * 128 : w1 + (gunit - 24) * 128;
    const unsigned ld = li < 6 ? 3072 : 3584;
    const char* bb = wb + (size_t)(128 * kq + 16 * c.w) * ld;
    const unsigned v = (lane >> 3) * ld + (lane & 7) * 16;
    gld16(cw0, bb, v);
    gld16(cw1, bb + 8 * ld, v);
  }
  {
    const char* p = c.w == 0 ? b1 + fa * 128 : c.w == 1 ? b1 + (f0u + 6) * 128 + c.jj * 32 : c.w < 4 ? wphi + c.hq * 2048 + (c.w - 2) * 1024 : b2 + c.ct * 128;
    const unsigned d = c.lbase + (c.w == 0 ? L_B1 : c.w == 1 ? L_B1 + 256 : c.w < 4 ? L_WPHI + (c.w - 2) * 1024 : L_B2);
    if (c.w == 2 || c.w == 3) glds16(p, lane * 16, d);
    else glds4(p, (lane & (c.w == 0 ? 63 : c.w == 1 ? 7 : 31)) * 4, d);
  }
  issue_half<2, 0>(c); issue_half<2, 1>(c);
  if constexpr (NPRO > 3) { issue_half<3, 0>(c); issue_half<3, 1>(c); }
  if constexpr (NPRO > 4) { issue_half<4, 0>(c); issue_half<4, 1>(c); }
  if constexpr (NPRO > 5) { issue_half<5, 0>(c); issue_half<5, 1>(c); }
  if constexpr (NPRO > 6) { issue_half<6, 0>(c); issue_half<6, 1>(c); }
  {
    const int id = c.tid, half = id & 1, ln = id >> 1;
    const char* ax; const char* aw;
    { const int r = (ln * 205) >> 10, sg = ln - 5 * r; ax = x + (size_t)(128 * c.rg + 32 * c.jj + r) * 1024 + (3 + sg) * 128; }
    { const int lj = ln < 160 ? 0 : ln < 240 ? ln - 160 : 79, un = lj >= 40, r = lj - 40 * un; aw = (un ? c.u1 : c.u0) + (size_t)(192 + 40 * (c.rg & 7) + r) * c.ld1; }
    glds4v((c.w < 5 ? ax : aw) + half * 64, c.lbase + L_DUMMY);
  }
  {
    unsigned long long pc; unsigned len;
    asm volatile("s_getpc_b64 %0\n.Lhh_pc_here:\n\ts_mov_b32 %1, .Lhh_code_end-.Lhh_pc_here" : "=s"(pc), "=s"(len));
    if (c.w == 7) {
      unsigned o = (unsigned)(idx * 64 + lane) * 64;
      o = o < len ? o : 0;
      glds4v((const char*)pc + o, c.lbase + L_DUMMY);
    }
  }
  unsigned g1off;
  {
    const int t = c.tid;
    const int ci = t - 128 < 208 ? t - 128 : 207;
    const int un = ci >> 6;
    const int ufa = 24 + fa;
    const int ua = c.isQ ? ufa : 8 + c.hq, ub = c.isQ ? ufa + 1 : 16 + c.hq, uc = c.isQ ? c.hq : ufa;
    const int gu = un == 0 ? ua : un == 1 ? ub : un == 2 ? uc : 24 + f0u + 6;
    const int col = gu * 64 + (un == 3 ? 16 * c.jj + (ci & 15) : (ci & 63));
    g1off = t < 128 ? WS_STATS + (128 * c.rg + t) * 16 : WS_CS + col * 32;
    asm volatile("" : "+v"(g1off));
  }
  STAMP(c, 1);

  v4u xf[2][16];
  f4 acc[2][2][2];
  const f4 z4 = {0.f, 0.f, 0.f, 0.f};
#pragma unroll
  for (int a = 0; a < 2; ++a)
#pragma unroll
    for (int b = 0; b < 2; ++b)
#pragma unroll
      for (int t = 0; t < 2; ++t) acc[a][b][t] = z4;
  const unsigned xa0 = (unsigned)((32 * c.m + l15) * 128 + ((g ^ (l15 >> 1)) * 16));
  const unsigned xtt0 = c.nh * 2048, xtt1 = (1 - c.nh) * 2048;
  const unsigned wa0 = (unsigned)((8 * g + q4) * 128 + (((4 * c.nh + (p4 >> 1)) ^ ((((q4 >> 1) & 1) | ((g & 1) << 1)) << 1)) * 16) + (p4 & 1) * 8);
  unsigned xb[2][4] = {{xa0 + xtt0, xa0 + xtt1, (xa0 ^ 64) + xtt0, (xa0 ^ 64) + xtt1}, {}}, wa[2][2] = {{wa0, wa0 ^ 32}, {}};
#pragma unroll
  for (int i = 0; i < 4; ++i) { xb[1][i] = xb[0][i] + HIWIN; asm volatile("" : "+v"(xb[1][i])); }
#pragma unroll
  for (int i = 0; i < 2; ++i) { wa[1][i] = wa[0][i] + HIWIN; asm volatile("" : "+v"(wa[1][i])); }
  v4u g1a, g1b;
  v4u wfa[2][2], wfb[2][2];
  auto pass1_step = [&](auto KC) {
    constexpr int kc = decltype(KC)::value;
    constexpr unsigned sx = L_RING + SC.slot[2 * kc] * 16384, sw = L_RING + SC.slot[2 * kc + 1] * 16384;
    TS(3 * kc);
    if constexpr (kc > 0)
      asm volatile("" : "+v"(wfb[0][0]), "+v"(wfb[0][1]), "+v"(wfb[1][0]), "+v"(wfb[1][1]), "+v"(xf[0][kc > 0 ? 2 * kc - 1 : 0]), "+v"(xf[1][kc > 0 ? 2 * kc - 1 : 0]));
    if constexpr (kc == 0) {
      wait_top<top_wait(kc)>();
    } else {
      wait_vm<top_wait(kc)>();
    }
    TS(3 * kc + 1);
    bar();
    TS(3 * kc + 2);
    FENCE();
    if constexpr (kc == 5) {
      REFRESH();
      c.vP0 = (lane >> 3) * c.ld2 + (((lane & 7) ^ ((lane >> 3) & 2)) * 16);
      c.vP1 = (c.vP0 ^ 64u) + 8 * c.ld2;
      c.pP = c.u2 + (size_t)(16 * c.w) * c.ld2;
    }
    if constexpr (kc == 7) {
      REFRESH();
      c.vF0 = (lane >> 3) * 1024 + (((lane & 7) ^ ((lane >> 3) & 6)) * 16);
      c.vF1 = c.vF0 + 8 * 1024;
      const unsigned o = (16 * c.m) * 1024 + c.ct * 128;
      c.pF1 = w2 + o + c.nh * 65536; c.pF5 = w2 + o + c.nh * 5 * 65536; c.pWo = wo + o + c.nh * 65536;
    }
    constexpr int hx = sx >= HIWIN, hw = sw >= HIWIN;
    const unsigned x0 = xb[hx][0], x1 = xb[hx][1], x2 = xb[hx][2], x3 = xb[hx][3], w0 = wa[hw][0], w1_ = wa[hw][1];
    constexpr unsigned rx = sx - hx * HIWIN, rw = sw - hw * HIWIN;
#define P1_MMA(F, KS, UN, I, T) do { if constexpr (DOM_) acc[UN][I][T] = mfma32(F[UN][I], xf[T][KS], acc[UN][I][T]); FENCE(); } while (0)
#define P1_HALF(FR, FM, KSR, KSM, XA, XB, SOFF, G0, G1, DOM, TIE) {                                 \
    constexpr bool DOM_ = DOM;                                                                     \
    v2u t0, t1;                                                                                    \
    xf[0][KSR] = ld128(L, XA + rx); FENCE();                                                       \
    if constexpr (TIE) {                                                                           \
      xf[1][KSR] = ld128(L, XB + rx); FENCE();                                                     \
      t0 = trd(L, w0 + rw + SOFF); FENCE();                                                        \
      asm volatile("" : "+v"(FM[0][0]), "+v"(FM[0][1]), "+v"(FM[1][0]), "+v"(FM[1][1]), "+v"(xf[0][KSM]), "+v"(xf[1][KSM]));   \
      FENCE();                                                                                     \
      P1_MMA(FM, KSM, 0, 0, 0);                                                                    \
    } else {                                                                                       \
      P1_MMA(FM, KSM, 0, 0, 0);                                                                    \
      xf[1][KSR] = ld128(L, XB + rx); FENCE();                                                     \
      t0 = trd(L, w0 + rw + SOFF); FENCE();                                                        \
    }                                                                                              \
    P1_MMA(FM, KSM, 0, 0, 1);                                                                      \
    t1 = trd(L, w0 + rw + SOFF + 512); FR[0][0] = (v4u){t0.x, t0.y, t1.x, t1.y}; FENCE();          \
    op_m0<kc, G0>(c); FENCE();                                                                     \
    P1_MMA(FM, KSM, 0, 1, 0);                                                                      \
    FR[0][1] = trd2(L, w1_ + rw + SOFF, w1_ + rw + SOFF + 512); FENCE();                           \
    P1_MMA(FM, KSM, 0, 1, 1);                                                                      \
    t0 = trd(L, w0 + rw + SOFF + 8192); FENCE();                                                   \
    op_go<kc, G0>(c); FENCE();                                                                     \
    P1_MMA(FM, KSM, 1, 0, 0);                                                                      \
    t1 = trd(L, w0 + rw + SOFF + 8192 + 512); FR[1][0] = (v4u){t0.x, t0.y, t1.x, t1.y}; FENCE();   \
    op_m0<kc, G1>(c); FENCE();                                                                     \
    P1_MMA(FM, KSM, 1, 0, 1);                                                                      \
    FR[1][1] = trd2(L, w1_ + rw + SOFF + 8192, w1_ + rw + SOFF + 8192 + 512); FENCE();             \
    P1_MMA(FM, KSM, 1, 1, 0);                                                                      \
    op_go<kc, G1>(c); FENCE();                                                                     \
    P1_MMA(FM, KSM, 1, 1, 1);                                                                      \
  }
    P1_HALF(wfa, wfb, 2 * kc, (kc > 0 ? 2 * kc - 1 : 0), x0, x1, 0, 0, 1, (kc > 0), false)
    if constexpr (kc == 0) { issue_op<0, 2>(c); issue_op<0, 3>(c); issue_op<0, 4>(c); issue_op<0, 5>(c); FENCE(); }
    if constexpr (kc == 0) {
      asm volatile("s_waitcnt vmcnt(%3)" : "+v"(xr), "+v"(cw0), "+v"(cw1) : "n"(SC.cum[0] - 1 - PRO_REG_LAST + imin(SC.cum[1] - SC.cum[0], 6)) : "memory");
      const float x0v = bitsf(__builtin_amdgcn_readfirstlane(xr.x << 16));
      float d[8] = {bflo(xr.x) - x0v, bfhi(xr.x) - x0v, bflo(xr.y) - x0v, bfhi(xr.y) - x0v, bflo(xr.z) - x0v, bfhi(xr.z) - x0v, bflo(xr.w) - x0v, bfhi(xr.w) - x0v};
      float s = ((d[0] + d[1]) + (d[2] + d[3])) + ((d[4] + d[5]) + (d[6] + d[7]));
      float qq = d[0] * d[0];
#pragma unroll
      for (int e = 1; e < 8; ++e) qq = __builtin_fmaf(d[e], d[e], qq);
      s += dpp<0x128>(s); qq += dpp<0x128>(qq);
      s += dpp<0x124>(s); qq += dpp<0x124>(qq);
      s += dpp<0x122>(s); qq += dpp<0x122>(qq);
      s += dpp<0x121>(s); qq += dpp<0x121>(qq);
      sw32(s, qq);
      float sq = s + qq;
      { float a = sq, b = sq; sw16(a, b); sq = a + b; }
      const float sd = sq * (1.0f / 512.0f);
      const float m2 = bitsf(__builtin_amdgcn_readlane(fbits(sd), 32));
      if (lane == 0) {
        const float var = __builtin_fmaf(-sd, sd, m2);
        const float rstd = __builtin_amdgcn_rsqf(vmax(var, 0.f) + 1e-5f);
        __builtin_amdgcn_raw_buffer_store_b128((v4u){fbits(x0v + sd), c.tag, fbits(rstd), c.tag}, c.rs, WS_STATS + (128 * c.rg + 8 * j16 + c.w) * 16, 0, AUX_SC1);
      }
      FENCE();
    }
    if constexpr (kc == 1) {
      float cs[8] = {bflo(cw0.x) + bflo(cw1.x), bfhi(cw0.x) + bfhi(cw1.x), bflo(cw0.y) + bflo(cw1.y), bfhi(cw0.y) + bfhi(cw1.y),
                     bflo(cw0.z) + bflo(cw1.z), bfhi(cw0.z) + bfhi(cw1.z), bflo(cw0.w) + bflo(cw1.w), bfhi(cw0.w) + bfhi(cw1.w)};
#pragma unroll
      for (int e = 0; e < 4; ++e) { sw32(cs[e], cs[e + 4]); cs[e] += cs[e + 4]; }
#pragma unroll
      for (int e = 0; e < 2; ++e) { sw16(cs[e], cs[e + 2]); cs[e] += cs[e + 2]; }
      cs[0] += dpp<0x128>(cs[0]); cs[1] += dpp<0x128>(cs[1]);
      if ((lane & 8) == 0) *(LAS f2*)(L + L_SCR + c.w * 256 + ((lane & 7) * 8 + (lane >> 5) * 4 + ((lane >> 4) & 1) * 2) * 4) = (f2){cs[0], cs[1]};
      FENCE();
    }
    if constexpr (kc == 3) {
      if (c.w < 3) {
        const int id = threadIdx.x < 144 ? threadIdx.x : 143;
        const int ln = blk * 72 + (id >> 1);
        const char* a = ln < 14336 ? w2 + (size_t)ln * 128 : wo + (size_t)(ln - 14336) * 128;
        glds4v(a + (id & 1) * 64, c.lbase + L_DUMMY);
      }
      FENCE();
    }
    if constexpr (kc == 2) {
      if (c.w == 7 && job < 52) {
        float s = 0.f;
#pragma unroll
        for (int ww = 0; ww < 8; ++ww) s += *(LAS float*)(L + L_SCR + ww * 256 + lane * 4);
        __builtin_amdgcn_raw_buffer_store_b64((v2u){fbits(s), c.tag}, c.rs, WS_CS + (gunit * 64 + lane) * 32 + kq * 8, 0, AUX_SC1);
      }
      FENCE();
    }
    P1_HALF(wfb, wfa, 2 * kc + 1, 2 * kc, x2, x3, 4096, (kc == 0 ? 6 : 2), (kc == 0 ? 7 : 3), true, true)
    if constexpr (kc == ST_G1_ISSUE) {
      gld16_sc1(g1a, ws, g1off);
      gld16_sc1(g1b, ws, threadIdx.x < 128 ? g1off : g1off + 16);
      FENCE();
    }
  };
  pass1_step(IC<0>{});
  STAMP(c, 2);
  pass1_step(IC<1>{});
  pass1_step(IC<2>{});
  pass1_step(IC<3>{});
  pass1_step(IC<4>{});
  pass1_step(IC<5>{});
  pass1_step(IC<6>{});
  pass1_step(IC<7>{});
  STAMP(c, 3);

  TS(3 * ST_E1);
  asm volatile("" : "+v"(wfb[0][0]), "+v"(wfb[0][1]), "+v"(wfb[1][0]), "+v"(wfb[1][1]), "+v"(xf[0][15]), "+v"(xf[1][15]));
  {
      asm volatile("s_waitcnt vmcnt(%2) lgkmcnt(0)" : "+v"(g1a), "+v"(g1b) : "n"(top_wait(ST_E1)) : "memory");
      REFRESH();
      const bool isst = c.tid < 128;
      if (__builtin_expect(!__all(tags_ok(g1a, c.tag) && (isst || tags_ok(g1b, c.tag))), 0)) {
        const unsigned off = g1off;
#pragma unroll 1
        for (unsigned it = 0; it < SPIN_CAP; ++it) {
          __builtin_amdgcn_s_sleep(2);
          gld16_sc1(g1a, ws, off);
          gld16_sc1(g1b, ws, isst ? off : off + 16);
          asm volatile("s_waitcnt vmcnt(0)" : "+v"(g1a), "+v"(g1b)::"memory");
          if (__all(tags_ok(g1a, c.tag) && (isst || tags_ok(g1b, c.tag)))) break;
        }
      }
      if (isst) *(LAS f2*)(L + L_ST + c.tid * 8) = (f2){bitsf(g1a.z), bitsf(g1a.x) * bitsf(g1a.z)};
      else if (c.tid < 128 + 208) *(LAS float*)(L + L_CS + (c.tid - 128) * 4) = (bitsf(g1a.x) + bitsf(g1a.z)) + (bitsf(g1b.x) + bitsf(g1b.z));
      wait_lgkm();
  }
  TS(3 * ST_E1 + 1);
  bar();
  TS(3 * ST_E1 + 2);
  FENCE();
  REFRESH();
  const unsigned gt0 = 8 * c.rg + 2 * c.m + c.nh, gt1 = 8 * c.rg + 2 * c.m + 1 - c.nh;
  {
    f4 cs4[2][2];
#pragma unroll
    for (int un = 0; un < 2; ++un)
#pragma unroll
      for (int i = 0; i < 2; ++i) cs4[un][i] = ldf4(L, L_CS + (64 * un + 32 * c.nh + 16 * i + 4 * g) * 4);
    const f2 st0 = ldf2(L, L_ST + (16 * (2 * c.m + c.nh) + l15) * 8), st1 = ldf2(L, L_ST + (16 * (2 * c.m + 1 - c.nh) + l15) * 8);
    FENCE();
#pragma unroll
    for (int un = 0; un < 2; ++un)
#pragma unroll
      for (int i = 0; i < 2; ++i) {
        acc[un][i][0] = mfma32(wfb[un][i], xf[0][15], acc[un][i][0]);
        acc[un][i][1] = mfma32(wfb[un][i], xf[1][15], acc[un][i][1]);
      }
    FENCE();
    TSX(0);
    if (c.isQ) {
      v2u bb[2][2];
#pragma unroll
      for (int un = 0; un < 2; ++un)
#pragma unroll
        for (int i = 0; i < 2; ++i) bb[un][i] = ld64(L, L_B1 + un * 128 + c.nh * 64 + i * 32 + 8 * g);
      const unsigned ks0 = 2 * unit_pos(c.q, 2 + 2 * (c.jj & 1)) + c.nh;
      const unsigned ho0 = WS_H + (gt0 * 72 + ks0) * 1024 + lane * 16, ho1 = WS_H + (gt1 * 72 + ks0) * 1024 + lane * 16;
      PRIO(1);
      {
        __builtin_amdgcn_raw_buffer_store_b128(ffn_pair(acc[0][0][0], acc[0][1][0], cs4[0][0], cs4[0][1], bb[0][0], bb[0][1], st0), c.rs, ho0, 0, AUX_SC1);
      }
      FENCE(); PRIO(0); issue_op<ST_E1, 0>(c); FENCE();
      {
        __builtin_amdgcn_raw_buffer_store_b128(ffn_pair(acc[0][0][1], acc[0][1][1], cs4[0][0], cs4[0][1], bb[0][0], bb[0][1], st1), c.rs, ho1, 0, AUX_SC1);
      }
      FENCE(); PRIO(1); issue_op<ST_E1, 1>(c); FENCE();
      {
        __builtin_amdgcn_raw_buffer_store_b128(ffn_pair(acc[1][0][0], acc[1][1][0], cs4[1][0], cs4[1][1], bb[1][0], bb[1][1], st0), c.rs, ho0 + 2048, 0, AUX_SC1);
      }
      FENCE(); PRIO(0);
      {
        __builtin_amdgcn_raw_buffer_store_b128(ffn_pair(acc[1][0][1], acc[1][1][1], cs4[1][0], cs4[1][1], bb[1][0], bb[1][1], st1), c.rs, ho1 + 2048, 0, AUX_SC1);
      }
    } else {
      const v4u ones = {0x3f803f80u, 0x3f803f80u, 0x3f803f80u, 0x3f803f80u};
      const v2u wt0 = trd(L, L_WPHI + (2 * c.nh) * 512 + (4 * g + q4) * 32 + p4 * 8), wt1 = trd(L, L_WPHI + (2 * c.nh + 1) * 512 + (4 * g + q4) * 32 + p4 * 8);
      f4 pk4[4];
      v2u vb[4], fp[4], fn[4];
      PRIO(1);
#pragma unroll
      for (int i = 0; i < 2; ++i)
#pragma unroll
        for (int tt = 0; tt < 2; ++tt) pk4[2 * i + tt] = mfma16(i ? wt1 : wt0, ln_pack(acc[0][i][tt], cs4[0][i], tt ? st1 : st0), z4);
#pragma unroll
      for (int i = 0; i < 2; ++i)
#pragma unroll
        for (int tt = 0; tt < 2; ++tt) vb[2 * i + tt] = ln_pack(acc[1][i][tt], cs4[1][i], tt ? st1 : st0);
      TSX(1);
      FENCE(); PRIO(0); issue_op<ST_E1, 0>(c); FENCE();
      Feat ft;
      feat_max(pk4, ft);
      TSX(2);
      FENCE(); PRIO(1); issue_op<ST_E1, 1>(c); FENCE();
      feat_exp(pk4, ft);
      TSX(3);
      FENCE(); PRIO(0); FENCE();
      feat_norm(ft, fp, fn);
      FENCE();
      TSX(4);
      constexpr unsigned IMG_P = L_SCR, IMG_N = L_SCR + 16384, IMG_V = L_RING + SC.slot[15] * 16384;
      const unsigned wo_ = (2 * c.nh) * 4096 + (32 * c.m + l15) * 32 + ((g ^ q4) * 8);
#pragma unroll
      for (int i = 0; i < 2; ++i)
#pragma unroll
        for (int tt = 0; tt < 2; ++tt) {
          *(LAS v2u*)(L + IMG_P + wo_ + i * 4096 + tt * 512) = fp[2 * i + tt];
          *(LAS v2u*)(L + IMG_N + wo_ + i * 4096 + tt * 512) = fn[2 * i + tt];
          *(LAS v2u*)(L + IMG_V + wo_ + i * 4096 + tt * 512) = vb[2 * i + tt];
        }
      wait_lgkm();
      TSX(5);
      bar();
      TSX(6);
      const unsigned ro = (c.w >> 1) * 4096 + (4 * g + q4) * 32 + ((p4 ^ g) * 8);
      const unsigned ra = IMG_P + (c.w & 1) * 16384 + ro, rb = IMG_V + ro;
      v4u A[4], B[4];
#pragma unroll
      for (int j = 0; j < 4; ++j) { A[j] = trd2(L, ra + j * 1024, ra + j * 1024 + 512); B[j] = trd2(L, rb + j * 1024, rb + j * 1024 + 512); }
      f4 kv0 = mfma32(A[0], B[0], z4), kv1 = mfma32(A[1], B[1], z4), z0 = mfma32(A[0], ones, z4), z1 = mfma32(A[1], ones, z4);
      kv0 = mfma32(A[2], B[2], kv0); kv1 = mfma32(A[3], B[3], kv1); z0 = mfma32(A[2], ones, z0); z1 = mfma32(A[3], ones, z1);
      kv0 += kv1; z0 += z1;
      TSX(7);
      const unsigned pbase = WS_P + (c.hq * 16 + c.rg) * PART_BYTES;
      __builtin_amdgcn_raw_buffer_store_b128((v4u){fbits(kv0[0]), c.tag, fbits(kv0[1]), c.tag}, c.rs, pbase + (c.w * 128 + lane) * 16, 0, AUX_SC1);
      __builtin_amdgcn_raw_buffer_store_b128((v4u){fbits(kv0[2]), c.tag, fbits(kv0[3]), c.tag}, c.rs, pbase + (c.w * 128 + 64 + lane) * 16, 0, AUX_SC1);
      if (l15 == 0) {
        __builtin_amdgcn_raw_buffer_store_b128((v4u){fbits(z0[0]), c.tag, fbits(z0[1]), c.tag}, c.rs, pbase + (1024 + c.w * 8 + g * 2) * 16, 0, AUX_SC1);
        __builtin_amdgcn_raw_buffer_store_b128((v4u){fbits(z0[2]), c.tag, fbits(z0[3]), c.tag}, c.rs, pbase + (1024 + c.w * 8 + g * 2 + 1) * 16, 0, AUX_SC1);
      }
    }
  }
  PRIO_OFF();
  TSX(8);
  STAMP(c, 4);
  FENCE();

  f4 acc2[2][2], accS = z4;
#pragma unroll
  for (int b = 0; b < 2; ++b)
#pragma unroll
    for (int t = 0; t < 2; ++t) acc2[b][t] = z4;
  REFRESH();
  const unsigned wb0 = (unsigned)((8 * g + q4) * 128 + (((4 * c.nh + (p4 >> 1)) ^ ((((q4 >> 1) & 1) | ((g & 1) << 1)) << 1)) * 16) + (p4 & 1) * 8);
  const unsigned sa0 = (unsigned)((8 * g + ((g & 1) << 2) + q4) * 32 + p4 * 8);
  constexpr unsigned SS = L_RING + SC.slot[IT_S] * 16384;
  unsigned wbb[2][2] = {{wb0, wb0 ^ 32}, {}};
#pragma unroll
  for (int i = 0; i < 2; ++i) { wbb[1][i] = wbb[0][i] + HIWIN; asm volatile("" : "+v"(wbb[1][i])); }
  unsigned s0 = sa0 + (SS >= HIWIN ? HIWIN : 0), s1 = (sa0 ^ 128) + (SS >= HIWIN ? HIWIN : 0);
  asm volatile("" : "+v"(s0), "+v"(s1));
  unsigned pollv;
  v4u rk0, rk1, rz0, rz1;
  const char* flagA = ws + WS_FLAG + (0 * 16 + (c.rt >> 1)) * 128;
  const char* flagB = ws + WS_FLAG + (1 * 16 + (c.rt >> 1)) * 128;
  const char* flagO = ws + WS_FLAG + (2 * 16 + (c.rt >> 1)) * 128;
  const char* rsrc = ws + WS_P + (c.hq * 16 + 2 * c.w) * PART_BYTES;
  v4u pwa[2], psa, pwb[2], psb;

  auto pass2_step = [&](auto KC) {
    constexpr int kc = decltype(KC)::value, st = 9 + kc;
    constexpr unsigned sp = L_RING + SC.slot[IT_P0 + kc] * 16384, ss = L_RING + SC.slot[IT_S] * 16384;
    TS(3 * st);
    if constexpr (kc > 0) { asm volatile("" : "+v"(pwb[0]), "+v"(pwb[1]), "+v"(psb)); wait_vm<top_wait(st)>(); }
    else wait_top<top_wait(st)>();
    TS(3 * st + 1);
    bar();
    TS(3 * st + 2);
    FENCE();
    constexpr int hp = sp >= HIWIN;
    const unsigned w0 = wbb[hp][0], w1_ = wbb[hp][1];
    constexpr unsigned rp = sp - hp * HIWIN, rs_ = ss - (ss >= HIWIN ? HIWIN : 0);
#define P2_MMA(A, F, T, KM) do { if constexpr (DOM_) A = mfma32(F, xf[T][KM], A); FENCE(); } while (0)
#define P2_Q(FR, SR, KR, FM, SM, KM, DOM, TIE) {                                                    \
    constexpr bool DOM_ = DOM;                                                                     \
    v2u t0, t1;                                                                                    \
    t0 = trd(L, w0 + rp + ((KR) & 3) * 4096); FENCE();                                             \
    if constexpr (TIE) { asm volatile("" : "+v"(FM[0]), "+v"(FM[1]), "+v"(SM)); FENCE(); }         \
    P2_MMA(acc2[0][0], FM[0], 0, KM);                                                              \
    t1 = trd(L, w0 + rp + ((KR) & 3) * 4096 + 512); FR[0] = (v4u){t0.x, t0.y, t1.x, t1.y}; FENCE(); \
    P2_MMA(acc2[0][1], FM[0], 1, KM);                                                              \
    FR[1] = trd2(L, w1_ + rp + ((KR) & 3) * 4096, w1_ + rp + ((KR) & 3) * 4096 + 512); FENCE();    \
    P2_MMA(acc2[1][0], FM[1], 0, KM);                                                              \
    t0 = trd(L, s0 + rs_ + (KR) * 1024); FENCE();                                                  \
    P2_MMA(acc2[1][1], FM[1], 1, KM);                                                              \
    t1 = trd(L, s1 + rs_ + (KR) * 1024); SR = (v4u){t0.x, t0.y, t1.x, t1.y}; FENCE();              \
    P2_MMA(accS, SM, 0, KM);                                                                       \
  }
    op_m0<st, 0>(c);
    P2_Q(pwa, psa, 4 * kc, pwb, psb, (kc > 0 ? 4 * kc - 1 : 0), (kc > 0), false)
    op_go<st, 0>(c);
    if constexpr (st == ST_PUBA) {
      if (c.isQ && threadIdx.x == 0) __builtin_amdgcn_raw_buffer_store_b32(c.tag, c.rs, WS_FLAG + (0 * 16 + c.rg) * 128 + c.hq * 4, 0, AUX_SC1);
    }
    FENCE();
    op_m0<st, 1>(c);
    P2_Q(pwb, psb, 4 * kc + 1, pwa, psa, 4 * kc, true, true)
    op_go<st, 1>(c);
    FENCE();
    P2_Q(pwa, psa, 4 * kc + 2, pwb, psb, 4 * kc + 1, true, true)
    if constexpr (st == ST_POLLA_ISSUE) { REFRESH(); gld4_sc1(pollv, flagA, (lane & 31) * 4); }
    if constexpr (st == ST_R_ISSUE) {
      REFRESH();
      const unsigned vk = c.isQ ? (64 * c.rg + lane) * 16 : 0, vz = c.isQ ? (1024 + 4 * c.rg + (lane & 3)) * 16 : 0;
      gld16_sc1(rk0, rsrc, vk);
      gld16_sc1(rk1, rsrc + PART_BYTES, vk);
      gld16_sc1(rz0, rsrc, vz);
      gld16_sc1(rz1, rsrc + PART_BYTES, vz);
    }
    FENCE();
    P2_Q(pwb, psb, 4 * kc + 3, pwa, psa, 4 * kc + 2, true, true)
  };
  pass2_step(IC<0>{});
  pass2_step(IC<1>{});
  pass2_step(IC<2>{});
  pass2_step(IC<3>{});
  STAMP(c, 5);

  v4u hf[18][2];
  v4u rg0, rg1, rgz0, rgz1;
  TS(3 * ST_E2);
  wait_top<top_wait(ST_E2)>();
  asm volatile("s_waitcnt vmcnt(%5)" : "+v"(pollv), "+v"(rk0), "+v"(rk1), "+v"(rz0), "+v"(rz1)
               : "n"(imin(extra_wait(ST_POLLA_ISSUE, ST_E2), extra_wait(ST_R_ISSUE, ST_E2))) : "memory");
  REFRESH();
  if (c.w == 0) {
    if (__builtin_expect(!__all(pollv == c.tag || (lane & 31) >= 8), 0)) spin_flags(c, flagA, 8);
  }
  if (c.isQ) {
    if (__builtin_expect(!__all(tags_ok(rk0, c.tag) && tags_ok(rk1, c.tag) && tags_ok(rz0, c.tag) && tags_ok(rz1, c.tag)), 0)) {
      const unsigned vk = (64 * c.rg + lane) * 16, vz = (1024 + 4 * c.rg + (lane & 3)) * 16;
#pragma unroll 1
      for (unsigned it = 0; it < SPIN_CAP; ++it) {
        __builtin_amdgcn_s_sleep(2);
        gld16_sc1(rk0, rsrc, vk);
        gld16_sc1(rk1, rsrc + PART_BYTES, vk);
        gld16_sc1(rz0, rsrc, vz);
        gld16_sc1(rz1, rsrc + PART_BYTES, vz);
        asm volatile("s_waitcnt vmcnt(0)" : "+v"(rk0), "+v"(rk1), "+v"(rz0), "+v"(rz1)::"memory");
        if (__all(tags_ok(rk0, c.tag) && tags_ok(rk1, c.tag) && tags_ok(rz0, c.tag) && tags_ok(rz1, c.tag))) break;
      }
    }
    *(LAS f2*)(L + L_SCR + c.w * 1024 + lane * 8) = (f2){bitsf(rk0.x) + bitsf(rk1.x), bitsf(rk0.z) + bitsf(rk1.z)};
    if (lane < 4) *(LAS f2*)(L + L_SCR + c.w * 1024 + 512 + lane * 8) = (f2){bitsf(rz0.x) + bitsf(rz1.x), bitsf(rz0.z) + bitsf(rz1.z)};
    wait_lgkm();
  }
  TS(3 * ST_E2 + 1);
  bar();
  TS(3 * ST_E2 + 2);
  FENCE();
  c.pH = ws + WS_H + (size_t)((4 * c.rt + c.m) * 72 + 2 * c.nh) * 1024;
  c.vH = lane * 16;
  f2 st0, st1;
  f4 cs2[2];
  f4 pq4[4];
  Feat ftq;
  v2u fp[4], fn[4];
  {
#pragma unroll
    for (int i = 0; i < 2; ++i) {
      acc2[i][0] = mfma32(pwb[i], xf[0][15], acc2[i][0]);
      acc2[i][1] = mfma32(pwb[i], xf[1][15], acc2[i][1]);
    }
    accS = mfma32(psb, xf[0][15], accS);
    st0 = ldf2(L, L_ST + (16 * (2 * c.m + c.nh) + l15) * 8); st1 = ldf2(L, L_ST + (16 * (2 * c.m + 1 - c.nh) + l15) * 8);
    cs2[0] = ldf4(L, L_CS + (128 + 32 * c.nh + 4 * g) * 4); cs2[1] = ldf4(L, L_CS + (128 + 32 * c.nh + 16 + 4 * g) * 4);
    const f4 csS = ldf4(L, L_CS + (192 + 4 * g) * 4);
    const v2u bS = ld64(L, L_B1 + 256 + 8 * g);
    FENCE();
    issue_op<ST_E2, 0>(c);
    issue_h<0>(c, hf);
    FENCE();
    TSX(9);
    PRIO(1);
    const v2u bb0 = ld64(L, L_B1 + c.nh * 64 + 8 * g), bb1 = ld64(L, L_B1 + c.nh * 64 + 32 + 8 * g);
    const unsigned ksu = 2 * unit_pos(c.q, c.jj) + c.nh;
    if (c.isQ) {
      if (c.w == 0) {
        f2 s = {0.f, 0.f}, sz = {0.f, 0.f};
#pragma unroll
        for (int ww = 0; ww < 8; ++ww) { s += ldf2(L, L_SCR + ww * 1024 + lane * 8); sz += ldf2(L, L_SCR + ww * 1024 + 512 + (lane & 3) * 8); }
        const unsigned rb = WS_R + c.hq * PART_BYTES;
        __builtin_amdgcn_raw_buffer_store_b128((v4u){fbits(s.x), c.tag, fbits(s.y), c.tag}, c.rs, rb + (64 * c.rg + lane) * 16, 0, AUX_SC1);
        if (lane < 4) __builtin_amdgcn_raw_buffer_store_b128((v4u){fbits(sz.x), c.tag, fbits(sz.y), c.tag}, c.rs, rb + (1024 + 4 * c.rg + lane) * 16, 0, AUX_SC1);
      }
      const v2u wt0 = trd(L, L_WPHI + (2 * c.nh) * 512 + (4 * g + q4) * 32 + p4 * 8), wt1 = trd(L, L_WPHI + (2 * c.nh + 1) * 512 + (4 * g + q4) * 32 + p4 * 8);
#pragma unroll
      for (int i = 0; i < 2; ++i)
#pragma unroll
        for (int tt = 0; tt < 2; ++tt) pq4[2 * i + tt] = mfma16(i ? wt1 : wt0, ln_pack(acc2[i][tt], cs2[i], tt ? st1 : st0), z4);
      feat_max(pq4, ftq);
    } else {
      __builtin_amdgcn_raw_buffer_store_b128(ffn_pair(acc2[0][0], acc2[1][0], cs2[0], cs2[1], bb0, bb1, st0), c.rs, WS_H + (gt0 * 72 + ksu) * 1024 + lane * 16, 0, AUX_SC1);
    }
    FENCE();
    PRIO(0);
    issue_op<ST_E2, 1>(c);
    issue_h<1>(c, hf);
    FENCE();
    if (c.isQ) feat_exp(pq4, ftq);
    else __builtin_amdgcn_raw_buffer_store_b128(ffn_pair(acc2[0][1], acc2[1][1], cs2[0], cs2[1], bb0, bb1, st1), c.rs, WS_H + (gt1 * 72 + ksu) * 1024 + lane * 16, 0, AUX_SC1);
    FENCE();
    TSX(10);
    PRIO(1);
    issue_h<2>(c, hf);
    FENCE();
    {
      const v2u o = ffn_tile(accS, csS, bS, st0);
      const unsigned ks = 2 * unit_pos(c.q, 6) + (c.jj >> 1);
      __builtin_amdgcn_raw_buffer_store_b64(o, c.rs, WS_H + (gt0 * 72 + ks) * 1024 + lane * 16 + (c.jj & 1) * 8, 0, AUX_SC1);
    }
    FENCE();
    TSX(11);
    PRIO(0);
    issue_op<ST_E2, 2>(c);
    issue_h<3>(c, hf);
    issue_op<ST_E2, 3>(c);
    if constexpr (HPRE > 4) issue_h<4>(c, hf);
    FENCE();
    if (c.isQ) feat_norm(ftq, fp, fn);
    FENCE();
    TSX(12);
    if constexpr (HPRE > 5) issue_h<5>(c, hf);
    if constexpr (HPRE > 6) issue_h<6>(c, hf);
    if constexpr (HPRE > 7) issue_h<7>(c, hf);
    {
      const char* rres_ = ws + WS_R + c.hq * PART_BYTES;
      const unsigned vk = c.isQ ? (c.tid >> 6) * 2048 + (c.tid & 63) * 16 : 0, vz = c.isQ ? (c.tid & 31) * 32 : 0;
      gld16_sc1(rg0, rres_, vk);
      gld16_sc1(rg1, rres_ + 1024, vk);
      gld16_sc1(rgz0, rres_ + 16384, vz);
      gld16_sc1(rgz1, rres_ + 16384 + 16, vz);
    }
    PRIO_OFF();
    TSX(13);
    static_assert(HPRE >= 4 && HPRE <= 8 && ST_RG_ISSUE == ST_E2, "initial h loads");
  }
  STAMP(c, 6);
  FENCE();

  f4 accF[4];
#pragma unroll
  for (int t = 0; t < 4; ++t) accF[t] = z4;
  REFRESH();
  const unsigned fa0 = (unsigned)((64 * c.nh + 4 * g + q4) * 128 + (((p4 >> 1) ^ ((((g & 1) << 1) | (q4 >> 1)) << 1)) * 16) + (p4 & 1) * 8);
  unsigned fb[2][4] = {{fa0, fa0 ^ 32, fa0 ^ 64, fa0 ^ 96}, {}};
#pragma unroll
  for (int i = 0; i < 4; ++i) { fb[1][i] = fb[0][i] + HIWIN; asm volatile("" : "+v"(fb[1][i])); }
  v4u xres, fwa[4], fwb[4], hprev;
  const char* rres = ws + WS_R + c.hq * PART_BYTES;

  auto phase2_step = [&](auto CC) {
    constexpr int ch = decltype(CC)::value, st = ST_F0 + ch;
    constexpr unsigned sf = L_RING + SC.slot[item_of_F(ch)] * 16384;
    TS(3 * st);
    if constexpr (ch > 0) asm volatile("" : "+v"(fwb[0]), "+v"(fwb[1]), "+v"(fwb[2]), "+v"(fwb[3]));
    if constexpr (ch > 0) asm volatile("s_waitcnt vmcnt(%2)" : "+v"(hf[ch][0]), "+v"(hf[ch][1]) : "n"(top_wait(st)) : "memory");
    else asm volatile("s_waitcnt vmcnt(%2) lgkmcnt(0)" : "+v"(hf[ch][0]), "+v"(hf[ch][1]) : "n"(top_wait(st)) : "memory");
    if constexpr (st == ST_POLLB_CHECK) {
      asm volatile("s_waitcnt vmcnt(%1)" : "+v"(pollv) : "n"(extra_wait(ST_POLLB_ISSUE, st)) : "memory");
      if (c.w == 0) { if (__builtin_expect(!__all(pollv == c.tag || (lane & 31) >= 16), 0)) spin_flags(c, flagB, 16); }
    }
    if constexpr (st == ST_POLLO_CHECK) {
      asm volatile("s_waitcnt vmcnt(%1)" : "+v"(pollv) : "n"(extra_wait(ST_POLLO_ISSUE, st)) : "memory");
      if (c.w == 0) { if (__builtin_expect(!__all(pollv == c.tag || (lane & 31) >= 8), 0)) spin_flags(c, flagO, 8); }
    }
    if constexpr (st == ST_RG_CHECK) {
      asm volatile("s_waitcnt vmcnt(%4)" : "+v"(rg0), "+v"(rg1), "+v"(rgz0), "+v"(rgz1) : "n"(extra_wait(ST_RG_ISSUE, st)) : "memory");
      if (c.isQ) {
        const unsigned t = c.tid;
        if (__builtin_expect(!__all(tags_ok(rg0, c.tag) && tags_ok(rg1, c.tag) && tags_ok(rgz0, c.tag) && tags_ok(rgz1, c.tag)), 0)) {
#pragma unroll 1
          for (unsigned it = 0; it < SPIN_CAP; ++it) {
            __builtin_amdgcn_s_sleep(2);
            gld16_sc1(rg0, rres, (t >> 6) * 2048 + (t & 63) * 16);
            gld16_sc1(rg1, rres + 1024, (t >> 6) * 2048 + (t & 63) * 16);
            gld16_sc1(rgz0, rres + 16384, (t & 31) * 32);
            gld16_sc1(rgz1, rres + 16384 + 16, (t & 31) * 32);
            asm volatile("s_waitcnt vmcnt(0)" : "+v"(rg0), "+v"(rg1), "+v"(rgz0), "+v"(rgz1)::"memory");
            if (__all(tags_ok(rg0, c.tag) && tags_ok(rg1, c.tag) && tags_ok(rgz0, c.tag) && tags_ok(rgz1, c.tag))) break;
          }
        }
        *(LAS v2u*)(L + L_SCR + (t >> 7) * 1024 + (t & 63) * 16 + ((t >> 6) & 1) * 8) =
            (v2u){pk(bitsf(rg0.x), bitsf(rg0.z)), pk(bitsf(rg1.x), bitsf(rg1.z))};
        if (t < 32)
          *(LAS v2u*)(L + L_SCR + 4096 + (t >> 3) * 64 + (t & 3) * 16 + ((t >> 2) & 1) * 8) =
              (v2u){pk(bitsf(rgz0.x), bitsf(rgz0.z)), pk(bitsf(rgz1.x), bitsf(rgz1.z))};
        wait_lgkm();
      }
    }
    TS(3 * st + 1);
    bar();
    TS(3 * st + 2);
    FENCE();
    constexpr int hi = sf >= HIWIN;
    const unsigned f0 = fb[hi][0], f1 = fb[hi][1], f2_ = fb[hi][2], f3 = fb[hi][3];
    constexpr unsigned rf = sf - hi * HIWIN;
    {
      v2u t0, t1;
      fwa[0] = trd2(L, f0 + rf, f0 + rf + 2048); FENCE();
      op_m0<st, 0>(c); FENCE();
      if constexpr (ch > 0) accF[0] = mfma32(fwb[0], hprev, accF[0]);
      FENCE();
      fwa[1] = trd2(L, f1 + rf, f1 + rf + 2048); FENCE();
      if constexpr (st == ST_PUBB) {
        if (threadIdx.x == 0) __builtin_amdgcn_raw_buffer_store_b32(c.tag, c.rs, WS_FLAG + (1 * 16 + c.rg) * 128 + (4 * c.q + c.jj) * 4, 0, AUX_SC1);
      }
      if constexpr (st == ST_PUBO) {
        if (c.isQ && threadIdx.x == 0) __builtin_amdgcn_raw_buffer_store_b32(c.tag, c.rs, WS_FLAG + (2 * 16 + c.rg) * 128 + c.hq * 4, 0, AUX_SC1);
      }
      op_go<st, 0>(c); FENCE();
      if constexpr (ch > 0) accF[1] = mfma32(fwb[1], hprev, accF[1]);
      FENCE();
      fwa[2] = trd2(L, f2_ + rf, f2_ + rf + 2048); FENCE();
      op_m0<st, 1>(c); FENCE();
      if constexpr (ch > 0) accF[2] = mfma32(fwb[2], hprev, accF[2]);
      FENCE();
      fwa[3] = trd2(L, f3 + rf, f3 + rf + 2048); FENCE();
      op_go<st, 1>(c); FENCE();
      if constexpr (ch > 0) accF[3] = mfma32(fwb[3], hprev, accF[3]);
      FENCE();
      fwb[0] = trd2(L, f0 + rf + 4096, f0 + rf + 4096 + 2048); FENCE();
      asm volatile("" : "+v"(fwa[0]), "+v"(fwa[1]), "+v"(fwa[2]), "+v"(fwa[3])); FENCE();
      accF[0] = mfma32(fwa[0], hf[ch][0], accF[0]); FENCE();
      fwb[1] = trd2(L, f1 + rf + 4096, f1 + rf + 4096 + 2048); FENCE();
      constexpr int hc = h_at(st);
      if constexpr (hc >= 0) { issue_h0<(hc >= 0 ? hc : 0)>(c, hf); FENCE(); }
      accF[1] = mfma32(fwa[1], hf[ch][0], accF[1]); FENCE();
      fwb[2] = trd2(L, f2_ + rf + 4096, f2_ + rf + 4096 + 2048); FENCE();
      accF[2] = mfma32(fwa[2], hf[ch][0], accF[2]); FENCE();
      fwb[3] = trd2(L, f3 + rf + 4096, f3 + rf + 4096 + 2048); FENCE();
      if constexpr (hc >= 0) { issue_h1<(hc >= 0 ? hc : 0)>(c, hf); FENCE(); }
      if constexpr (st == ST_POLLB_ISSUE) gld4_sc1(pollv, flagB, (lane & 31) * 4);
      if constexpr (st == ST_POLLO_ISSUE) gld4_sc1(pollv, flagO, (lane & 31) * 4);
      if constexpr (ch == 14) gld16(xres, x + (size_t)(64 * c.rt) * 1024 + c.ct * 128, (c.tid >> 3) * 1024 + (c.tid & 7) * 16);
      FENCE();
      accF[3] = mfma32(fwa[3], hf[ch][0], accF[3]);
      hprev = hf[ch][1];
      FENCE();
      (void)t0; (void)t1;
    }
    if constexpr (st == ST_O) {
      if (c.isQ) {
        v2u ob[2][2];
#pragma unroll
        for (int i = 0; i < 2; ++i) {
          const int hl = 2 * c.nh + i;
          const v4u Akv = ld128(L, L_SCR + hl * 1024 + lane * 16), Az = ld128(L, L_SCR + 4096 + hl * 64 + g * 16);
#pragma unroll
          for (int tt = 0; tt < 2; ++tt) {
            const v4u ph = {fp[2 * i + tt].x, fp[2 * i + tt].y, fn[2 * i + tt].x, fn[2 * i + tt].y};
            const f4 num = mfma32(Akv, ph, z4), den = mfma32(Az, ph, z4);
            const float inv = __builtin_amdgcn_rcpf(den[0] + 1e-6f);
            ob[i][tt] = (v2u){pk(num[0] * inv, num[1] * inv), pk(num[2] * inv, num[3] * inv)};
          }
        }
        const unsigned ks = 56 + 2 * c.hq + c.nh;
        __builtin_amdgcn_raw_buffer_store_b128((v4u){ob[0][0].x, ob[0][0].y, ob[1][0].x, ob[1][0].y}, c.rs, WS_H + (gt0 * 72 + ks) * 1024 + lane * 16, 0, AUX_SC1);
        __builtin_amdgcn_raw_buffer_store_b128((v4u){ob[0][1].x, ob[0][1].y, ob[1][1].x, ob[1][1].y}, c.rs, WS_H + (gt1 * 72 + ks) * 1024 + lane * 16, 0, AUX_SC1);
      }
    }
    FENCE();
  };
  phase2_step(IC<0>{});
  phase2_step(IC<1>{});
  phase2_step(IC<2>{});
  phase2_step(IC<3>{});
  phase2_step(IC<4>{});
  phase2_step(IC<5>{});
  STAMP(c, 7);
  phase2_step(IC<6>{});
  phase2_step(IC<7>{});
  phase2_step(IC<8>{});
  phase2_step(IC<9>{});
  phase2_step(IC<10>{});
  phase2_step(IC<11>{});
  STAMP(c, 8);
  phase2_step(IC<12>{});
  phase2_step(IC<13>{});
  phase2_step(IC<14>{});
  phase2_step(IC<15>{});
  phase2_step(IC<16>{});
  phase2_step(IC<17>{});
  STAMP(c, 9);

  TS(96);
#pragma unroll
  for (int t = 0; t < 4; ++t) accF[t] = mfma32(fwb[t], hprev, accF[t]);
  REFRESH();
#pragma unroll
  for (int t = 0; t < 4; ++t) *(LAS f4*)(L + L_SCR + c.nh * 17408 + (16 * c.m + l15) * 272 + (16 * t + 4 * g) * 4) = accF[t];
  asm volatile("s_waitcnt vmcnt(0) lgkmcnt(0)" : "+v"(xres)::"memory");
  TS(97);
  bar();
  {
    const int tok = c.tid >> 3, cc = c.tid & 7;
    const unsigned a = L_SCR + tok * 272 + cc * 32;
    f4 s0 = ldf4(L, a) + ldf4(L, a + 17408), s1 = ldf4(L, a + 16) + ldf4(L, a + 16 + 17408);
    const v4u bb = ld128(L, L_B2 + cc * 16);
    v4u o;
    o.x = pk(s0[0] + bflo(xres.x) + bflo(bb.x), s0[1] + bfhi(xres.x) + bfhi(bb.x));
    o.y = pk(s0[2] + bflo(xres.y) + bflo(bb.y), s0[3] + bfhi(xres.y) + bfhi(bb.y));
    o.z = pk(s1[0] + bflo(xres.z) + bflo(bb.z), s1[1] + bfhi(xres.z) + bfhi(bb.z));
    o.w = pk(s1[2] + bflo(xres.w) + bflo(bb.w), s1[3] + bfhi(xres.w) + bfhi(bb.w));
    { const __amdgpu_buffer_rsrc_t ro = __builtin_amdgcn_make_buffer_rsrc((void*)out, 0, 2048 * 1024, 0x00020000);
      __builtin_amdgcn_raw_buffer_store_b128(o, ro, (64 * c.rt + tok) * 1024 + c.ct * 128 + cc * 16, 0, 2);
    }
  }
  asm volatile(".Lhh_code_end:");
  if (blk == 0 && c.tid == 0) *(unsigned*)(ws + WS_EPOCH) = c.tag;
  STAMP(c, 10);
}

}

extern "C" void kernel_launch(void* const* d_in, const int* in_sizes, int n_in, void* d_out, int out_size, void* d_ws, size_t ws_size,
                              hipStream_t stream) {
  (void)in_sizes; (void)n_in; (void)out_size; (void)ws_size;
  (void)hipFuncSetAttribute((const void*)hh::fused_kernel, hipFuncAttributeMaxDynamicSharedMemorySize, hh::L_END);
  hh::fused_kernel<<<256, 512, hh::L_END, stream>>>((const char*)d_in[0], (const char*)d_in[1], (const char*)d_in[2], (const char*)d_in[3],
                                                    (const char*)d_in[5], (const char*)d_in[6], (const char*)d_in[7], (const char*)d_in[8],
                                                    (unsigned short*)d_out, (char*)d_ws);
}
```

```cpp
#include <hip/hip_runtime.h>
#include <stdint.h>
#include <type_traits>

namespace hh {

#define LAS __attribute__((address_space(3)))
typedef unsigned v4u __attribute__((ext_vector_type(4)));
typedef unsigned v2u __attribute__((ext_vector_type(2)));
typedef float f4 __attribute__((ext_vector_type(4)));
typedef float f2 __attribute__((ext_vector_type(2)));
typedef short s4 __attribute__((ext_vector_type(4)));
typedef __bf16 b8 __attribute__((ext_vector_type(8)));
typedef __bf16 b2 __attribute__((ext_vector_type(2)));
template <int V> using IC = std::integral_constant<int, V>;

constexpr unsigned WS_EPOCH = 0;
constexpr unsigned WS_FLAG = 256;
constexpr unsigned WS_STATS = 8192;
constexpr unsigned WS_CS = WS_STATS + 2048 * 16;
constexpr unsigned PART_BYTES = 17408;
constexpr unsigned WS_P = WS_CS + 3328 * 32;
constexpr unsigned WS_R = WS_P + 8 * 16 * PART_BYTES;
constexpr unsigned WS_H = (WS_R + 8 * PART_BYTES + 4095u) & ~4095u;
constexpr unsigned WS_DBG = WS_H + 128 * 72 * 1024;
constexpr unsigned WS_END = WS_DBG;

constexpr int NSLOT = 7;
constexpr unsigned L_RING = 0;
constexpr unsigned L_SCR = NSLOT * 16384;
constexpr unsigned SCRW = 5248;
constexpr unsigned L_CS = L_SCR + 8 * SCRW;
constexpr unsigned L_ST = L_CS + 1024;
constexpr unsigned L_B1 = L_ST + 1024;
constexpr unsigned L_WPHI = L_B1 + 512;
constexpr unsigned L_B2 = L_WPHI + 2048;
constexpr unsigned L_DUMMY = L_B2 + 256;
constexpr unsigned L_END = L_DUMMY + 256;
static_assert(L_END <= 163840, "LDS");

constexpr int NITEM = 39, NSTEP = 32;
constexpr int ST_E1 = 8, ST_E2 = 13, ST_F0 = 14;
constexpr int IT_P0 = 16, IT_F0 = 20, IT_S = 21;
constexpr int item_of_F(int c) { return c == 0 ? IT_F0 : IT_S + c; }
#define HPRE_N 4
constexpr int HPRE = HPRE_N;
constexpr int DH = 3;
constexpr int ST_G1_ISSUE = 5, ST_G1_CHECK = 8;
#define PUBA_STEP 10
constexpr int ST_PUBA = PUBA_STEP;
constexpr int ST_POLLA_ISSUE = 12;
constexpr int ST_R_ISSUE = 11;
constexpr int ST_PUBB = 15;
constexpr int E2_OPS_AFTER_STORES = 4 + 2 * (HPRE - 4);
constexpr int ST_POLLB_ISSUE = 17, ST_POLLB_CHECK = 19;
constexpr int ST_RG_ISSUE = 13, ST_RG_CHECK = 16;
constexpr int ST_O = 17;
constexpr int ST_PUBO = 20;
constexpr int ST_POLLO_ISSUE = 23, ST_POLLO_CHECK = 25;
static_assert(ST_F0 + 8 - DH == ST_POLLB_CHECK && ST_F0 + 14 - DH == ST_POLLO_CHECK, "flag checks sit in front of the first dependent loads");

struct Sched {
  int slot[NITEM], issue[NITEM], first[NITEM], last[NITEM];
  int cum[NSTEP + 1];
  int pos_item[2][NITEM];
  int pos_h[2][18];
  int h_issue[18];
  int pos_g1[2];
};
#define NPRO_N 3
constexpr int NPRO = NPRO_N;
constexpr int PRO_REG_LAST = 6;
constexpr Sched make_sched() {
  Sched s{};
  for (int i = 0; i < NITEM; ++i) {
    if (i < 16) { s.first[i] = i >> 1; s.last[i] = i >> 1; }
    else if (i < IT_F0) { s.first[i] = 9 + (i - IT_P0); s.last[i] = s.first[i]; }
    else if (i == IT_F0) { s.first[i] = ST_F0; s.last[i] = ST_F0; }
    else if (i == IT_S) { s.first[i] = 9; s.last[i] = 12; }
    else { s.first[i] = ST_F0 + (i - IT_S); s.last[i] = s.first[i]; }
  }
  s.last[15] = ST_E1;
  for (int c = 0; c < 18; ++c) s.h_issue[c] = c < HPRE ? ST_E2 : c < 8 ? ST_F0 + c - HPRE : ST_F0 + c - DH;
  int n = 0;
  int next = 0;
  int owner[NSLOT] = {};
  for (int k = 0; k < NSLOT; ++k) owner[k] = -2;
  for (int k = 0; k < NPRO; ++k) {
    s.slot[next] = k; s.issue[next] = -1; owner[k] = next; n += 2; s.pos_item[0][next] = s.pos_item[1][next] = n - 1; ++next;
    if (k == 1) n += 4;
  }
  for (int st = 0; st < NSTEP; ++st) {
    s.cum[st] = n;
    const int first_new = next;
    for (int rep = 0; rep < NSLOT; ++rep) {
      int best = -1;
      for (int k = 0; k < NSLOT; ++k)
        if ((owner[k] == -2 || (owner[k] >= 0 && s.last[owner[k]] < st)) && (best < 0 || owner[k] < owner[best])) best = k;
      if (best < 0) break;
      if (next < NITEM) { s.slot[next] = best; s.issue[next] = st; owner[best] = next; ++next; }
      else owner[best] = -1;
    }
    int nreg = 0;
    for (int c = 0; c < 18; ++c) if (s.h_issue[c] == st) nreg += 2;
    if (st == ST_G1_ISSUE) nreg += 2;
    const int nring = 2 * (next - first_new);
    for (int o = 0; o < 2; ++o) {
      int p = n + (o == 0 ? 0 : nreg);
      for (int i = first_new; i < next; ++i) { p += 2; s.pos_item[o][i] = p - 1; }
      p = n + (o == 0 ? nring : 0);
      for (int c = 0; c < 18; ++c) if (s.h_issue[c] == st) { p += 2; s.pos_h[o][c] = p - 1; }
      if (st == ST_G1_ISSUE) { p += 2; s.pos_g1[o] = p - 1; }
    }
    n += nring + nreg;
  }
  s.cum[NSTEP] = n;
  return s;
}
constexpr Sched SC = make_sched();
constexpr int imin(int a, int b) { return a < b ? a : b; }
constexpr int imax(int a, int b) { return a > b ? a : b; }
constexpr int top_wait(int st) {
  int need = -1;
  for (int o = 0; o < 2; ++o) {
    for (int i = 0; i < NITEM; ++i) if (SC.first[i] == st) need = imax(need, SC.pos_item[o][i]);
    if (st >= ST_F0) need = imax(need, SC.pos_h[o][st - ST_F0]);
    if (st == ST_G1_CHECK) need = imax(need, SC.pos_g1[o]);
  }
  int n = need < 0 ? 63 : SC.cum[st] - 1 - need;
  if (st == 9) n += 2;
  if (st == ST_PUBA) n = imin(n, SC.cum[st] - SC.cum[ST_E1 + 1]);
  if (st == ST_PUBB) n = imin(n, SC.cum[st] - SC.cum[ST_E2 + 1] + E2_OPS_AFTER_STORES);
  if (st == ST_PUBO) n = imin(n, SC.cum[st] - SC.cum[ST_O + 1]);
  return n > 63 ? 63 : n;
}
constexpr int extra_wait(int a, int b) { int n = SC.cum[b] - SC.cum[a + 1]; return n > 63 ? 63 : n; }
constexpr int h_at(int st) {
  for (int c = 0; c < 18; ++c) if (SC.h_issue[c] == st) return c;
  return -1;
}
constexpr int nth_item(int st, int k) {
  for (int i = 0; i < NITEM; ++i) if (SC.issue[i] == st) { if (k == 0) return i; --k; }
  return -1;
}
static_assert(nth_item(0, 4) < 0 && nth_item(ST_E1, 0) == IT_S && nth_item(ST_E1, 1) < 0 && nth_item(ST_E2, 2) < 0 && nth_item(9, 1) < 0 && nth_item(10, 1) < 0 && nth_item(11, 1) < 0 && nth_item(12, 1) < 0, "schedule shape");

__host__ __device__ constexpr int unit_pos(int q, int off) { return off >= 2 && off <= 5 ? 4 * q + (off - 2) : 16 + 3 * q + (off == 6 ? 2 : off); }
__host__ __device__ constexpr int pos_unit(int pos) { return pos < 16 ? 7 * (pos >> 2) + 2 + (pos & 3) : 7 * ((pos - 16) / 3) + (((pos - 16) % 3) == 2 ? 6 : (pos - 16) % 3); }

#define DI __device__ __forceinline__
DI void glds16(const void* sbase, unsigned voff, unsigned lds) {
  asm volatile("s_mov_b32 m0, %2\n\ts_nop 0\n\tglobal_load_lds_dwordx4 %0, %1" ::"v"(voff), "s"(sbase), "s"(lds) : "memory");
}
DI void set_m0(unsigned lds) { asm volatile("s_mov_b32 m0, %0" ::"s"(lds) : "memory"); }
template <bool NT = false> DI void glds16_m0(const void* sbase, unsigned voff) {
  if constexpr (NT) asm volatile("global_load_lds_dwordx4 %0, %1 nt" ::"v"(voff), "s"(sbase) : "memory");
  else asm volatile("global_load_lds_dwordx4 %0, %1" ::"v"(voff), "s"(sbase) : "memory");
}
#define NT_X 0
#define NT_W 0
#define NT_F 0
DI void glds4(const void* sbase, unsigned voff, unsigned lds) {
  asm volatile("s_mov_b32 m0, %2\n\ts_nop 0\n\tglobal_load_lds_dword %0, %1" ::"v"(voff), "s"(sbase), "s"(lds) : "memory");
}
DI void glds4v(const void* vaddr, unsigned lds) {
  asm volatile("s_mov_b32 m0, %1\n\ts_nop 0\n\tglobal_load_lds_dword %0, off" ::"v"(vaddr), "s"(lds) : "memory");
}
DI void gld16(v4u& d, const void* sbase, unsigned voff) {
  asm volatile("global_load_dwordx4 %0, %1, %2" : "=v"(d) : "v"(voff), "s"(sbase) : "memory");
}
DI void gld16_sc1(v4u& d, const void* sbase, unsigned voff) {
  asm volatile("global_load_dwordx4 %0, %1, %2 sc1" : "=v"(d) : "v"(voff), "s"(sbase) : "memory");
}
template <int OFF> DI void gld16_sc1_off(v4u& d, const void* sbase, unsigned voff) {
  asm volatile("global_load_dwordx4 %0, %1, %2 offset:%3 sc1" : "=v"(d) : "v"(voff), "s"(sbase), "n"(OFF) : "memory");
}
DI void gld4_sc1(unsigned& d, const void* sbase, unsigned voff) {
  asm volatile("global_load_dword %0, %1, %2 sc1" : "=v"(d) : "v"(voff), "s"(sbase) : "memory");
}
template <int N> DI void wait_top() { asm volatile("s_waitcnt vmcnt(%0) lgkmcnt(0)" ::"n"(N) : "memory"); }
template <int N> DI void wait_vm() { asm volatile("s_waitcnt vmcnt(%0)" ::"n"(N) : "memory"); }
DI void wait_lgkm() { asm volatile("s_waitcnt lgkmcnt(0)" ::: "memory"); }
DI void bar() { asm volatile("" ::: "memory"); __builtin_amdgcn_s_barrier(); asm volatile("" ::: "memory"); }
#define FENCE() __builtin_amdgcn_sched_barrier(0)

DI unsigned pk(float a, float b) { b2 r = {(__bf16)a, (__bf16)b}; return __builtin_bit_cast(unsigned, r); }
DI float bflo(unsigned u) { return __builtin_bit_cast(float, u << 16); }
DI float bfhi(unsigned u) { return __builtin_bit_cast(float, u & 0xffff0000u); }
DI unsigned fbits(float f) { return __builtin_bit_cast(unsigned, f); }
DI float bitsf(unsigned u) { return __builtin_bit_cast(float, u); }
DI f4 mfma32(v4u a, v4u b, f4 c) { return __builtin_amdgcn_mfma_f32_16x16x32_bf16(__builtin_bit_cast(b8, a), __builtin_bit_cast(b8, b), c, 0, 0, 0); }
DI f4 mfma16(v2u a, v2u b, f4 c) { return __builtin_amdgcn_mfma_f32_16x16x16bf16_1k(__builtin_bit_cast(s4, a), __builtin_bit_cast(s4, b), c, 0, 0, 0); }
DI v2u trd(LAS char* L, unsigned a) { return __builtin_bit_cast(v2u, __builtin_amdgcn_ds_read_tr16_b64_v4i16((LAS s4*)(L + a))); }
DI v4u trd2(LAS char* L, unsigned a0, unsigned a1) { v2u x = trd(L, a0), y = trd(L, a1); return (v4u){x.x, x.y, y.x, y.y}; }
DI v4u ld128(LAS char* L, unsigned a) { return *(LAS v4u*)(L + a); }
DI f4 ldf4(LAS char* L, unsigned a) { return *(LAS f4*)(L + a); }
DI f2 ldf2(LAS char* L, unsigned a) { return *(LAS f2*)(L + a); }
DI v2u ld64(LAS char* L, unsigned a) { return *(LAS v2u*)(L + a); }

template <int CTRL> DI float dpp(float v) { return bitsf((unsigned)__builtin_amdgcn_update_dpp(0, (int)fbits(v), CTRL, 0xf, 0xf, true)); }
DI void sw32(float& a, float& b) { auto r = __builtin_amdgcn_permlane32_swap(fbits(a), fbits(b), false, false); a = bitsf((unsigned)r[0]); b = bitsf((unsigned)r[1]); }
DI void sw16(float& a, float& b) { auto r = __builtin_amdgcn_permlane16_swap(fbits(a), fbits(b), false, false); a = bitsf((unsigned)r[0]); b = bitsf((unsigned)r[1]); }
DI float xsum(float v) { float a = v, b = v; sw16(a, b); v = a + b; a = v; b = v; sw32(a, b); return a + b; }
DI float rowsum16(float v) { v += dpp<0x128>(v); v += dpp<0x124>(v); v += dpp<0x122>(v); v += dpp<0x121>(v); return v; }
DI float wavesum(float v) { return xsum(rowsum16(v)); }

constexpr float LOG2E = 1.4426950408889634f;
DI float vmax(float a, float b) { return __builtin_elementwise_maximum(a, b); }
DI float vmin(float a, float b) { return __builtin_elementwise_minimum(a, b); }
template <class OP> DI float fold4(float a0, float a1, float a2, float a3, OP op) {
  sw32(a0, a1); sw32(a2, a3);
  float m01 = op(a0, a1), m23 = op(a2, a3);
  sw16(m01, m23);
  return op(m01, m23);
}
DI void unfold4(float m, float (&o)[4]) {
  float a = m, b = m;
  sw16(a, b);
  float a2 = a, b2 = b;
  sw32(a, a2); sw32(b, b2);
  o[0] = a; o[1] = a2; o[2] = b; o[3] = b2;
}
struct Feat { float a[4], b[4]; f2 ep[4][2], en[4][2]; };
DI void tie8(f2 (&a)[4][2]) { asm volatile("" : "+v"(a[0][0]), "+v"(a[0][1]), "+v"(a[1][0]), "+v"(a[1][1]), "+v"(a[2][0]), "+v"(a[2][1]), "+v"(a[3][0]), "+v"(a[3][1])); }
DI void feat_max(const f4 (&p)[4], Feat& f) {
  float mx[4], mn[4];
#pragma unroll
  for (int t = 0; t < 4; ++t) {
    mx[t] = vmax(vmax(vmax(p[t][0], p[t][1]), p[t][2]), p[t][3]);
    mn[t] = vmin(vmin(vmin(p[t][0], p[t][1]), p[t][2]), p[t][3]);
  }
  const float MX = fold4(mx[0], mx[1], mx[2], mx[3], [](float a, float b) { return vmax(a, b); }) * -LOG2E;
  const float MN = fold4(mn[0], mn[1], mn[2], mn[3], [](float a, float b) { return vmin(a, b); }) * LOG2E;
  unfold4(MX, f.a);
  unfold4(MN, f.b);
}
DI void feat_exp(const f4 (&p)[4], Feat& f) {
#pragma unroll
  for (int t = 0; t < 4; ++t)
#pragma unroll
    for (int h = 0; h < 2; ++h) {
      const f2 v = {p[t][2 * h], p[t][2 * h + 1]};
      f.ep[t][h] = __builtin_elementwise_fma(v, (f2){LOG2E, LOG2E}, (f2){f.a[t], f.a[t]});
      f.en[t][h] = __builtin_elementwise_fma(v, (f2){-LOG2E, -LOG2E}, (f2){f.b[t], f.b[t]});
    }
  tie8(f.ep); tie8(f.en);
#pragma unroll
  for (int t = 0; t < 4; ++t)
#pragma unroll
    for (int h = 0; h < 2; ++h) {
      f.ep[t][h] = (f2){__builtin_amdgcn_exp2f(f.ep[t][h].x), __builtin_amdgcn_exp2f(f.ep[t][h].y)};
      f.en[t][h] = (f2){__builtin_amdgcn_exp2f(f.en[t][h].x), __builtin_amdgcn_exp2f(f.en[t][h].y)};
    }
  tie8(f.ep); tie8(f.en);
}
DI void feat_norm(const Feat& f, v2u (&pos)[4], v2u (&neg)[4]) {
  float sp[4], sn[4];
#pragma unroll
  for (int t = 0; t < 4; ++t) {
    const f2 a = f.ep[t][0] + f.ep[t][1], b = f.en[t][0] + f.en[t][1];
    sp[t] = a.x + a.y;
    sn[t] = b.x + b.y;
  }
  const float IP = __builtin_amdgcn_rcpf(fold4(sp[0], sp[1], sp[2], sp[3], [](float a_, float b_) { return a_ + b_; }));
  const float IN = __builtin_amdgcn_rcpf(fold4(sn[0], sn[1], sn[2], sn[3], [](float a_, float b_) { return a_ + b_; }));
  float ip[4], in[4];
  unfold4(IP, ip);
  unfold4(IN, in);
#pragma unroll
  for (int t = 0; t < 4; ++t) {
    const f2 a0 = f.ep[t][0] * ip[t], a1 = f.ep[t][1] * ip[t], b0 = f.en[t][0] * in[t], b1 = f.en[t][1] * in[t];
    pos[t] = (v2u){pk(a0.x, a0.y), pk(a1.x, a1.y)};
    neg[t] = (v2u){pk(b0.x, b0.y), pk(b1.x, b1.y)};
  }
}

template <unsigned OFF> DI unsigned win(unsigned base) {
  if constexpr (OFF >= 65536u - 16384u) { unsigned b = base + (OFF & ~16383u); asm volatile("" : "+v"(b)); return b; }
  else return base;
}
template <unsigned OFF> constexpr unsigned rem() { return OFF >= 65536u - 16384u ? (OFF & 16383u) : OFF; }
constexpr unsigned HIWIN = 49152;
#define PRIO(k) do { } while (0)
#define PRIO_OFF() do { } while (0)
#define AUX_SC1 16
constexpr unsigned SPIN_CAP = 1u << 16;

struct Ctx {
  LAS char* L;
  unsigned lbase;
  const char *x, *wo, *w2;
  char* ws;
  __amdgpu_buffer_rsrc_t rs;
  unsigned tag;
  int tid, lane, w, m, nh;
  int rg, q, jj, hq, isQ, rt, ct;
  const char *u0, *u1, *u2, *ush;
  unsigned ld1, ld2;
  unsigned vX0, vX1, vW0, vW1, vP0, vP1, vF0, vF1;
  const char *pX, *pW, *pP, *pF1, *pF5, *pWo;
  const char* pH;
  unsigned vH;
  unsigned ldsw;
};

#define TS(i) do { } while (0)
#define TSX(i) do { } while (0)
#define STAMP(c, i) do { } while (0)

template <int I, int U> DI void half_m0(const Ctx& c) { set_m0(c.ldsw + SC.slot[I] * 16384 + U * 1024); }
template <int I, int U> DI void half_go(const Ctx& c) {
  if constexpr (I < 16 && (I & 1) == 0) {
    glds16_m0<NT_X>(c.pX + (I >> 1) * 128, U ? c.vX1 : c.vX0);
  } else if constexpr (I < 16) {
    glds16_m0<NT_W>(c.pW + (size_t)(64 * (I >> 1)) * c.ld1, U ? c.vW1 : c.vW0);
  } else if constexpr (I == IT_S) {
    const unsigned rr = c.lane >> 1;
    glds16_m0<NT_W>(c.ush + (size_t)(64 * c.w + 32 * U) * 3584, (rr ^ ((rr & 8) >> 1)) * 3584 + (c.lane & 1) * 16);
  } else if constexpr (I < IT_F0) {
    glds16_m0<NT_W>(c.pP + (size_t)(128 * (I - IT_P0)) * c.ld2, U ? c.vP1 : c.vP0);
  } else {
    constexpr int C = I == IT_F0 ? 0 : I - IT_S;
    if constexpr (C < 14) {
      constexpr int d = pos_unit(2 * C + 1) - pos_unit(2 * C);
      static_assert(d == 1 || d == 5, "unit distance inside a chunk");
      glds16_m0<NT_F>((d == 1 ? c.pF1 : c.pF5) + pos_unit(2 * C) * 65536, U ? c.vF1 : c.vF0);
    } else {
      glds16_m0<NT_F>(c.pWo + (C - 14) * 131072, U ? c.vF1 : c.vF0);
    }
  }
}
template <int I, int U> DI void issue_half(const Ctx& c) { half_m0<I, U>(c); asm volatile("s_nop 0"); half_go<I, U>(c); }
template <int ST, int K> DI void op_m0(const Ctx& c) { constexpr int I = nth_item(ST, K >> 1); if constexpr (I >= 0) half_m0<I, (K & 1)>(c); }
template <int ST, int K> DI void op_go(const Ctx& c) { constexpr int I = nth_item(ST, K >> 1); if constexpr (I >= 0) half_go<I, (K & 1)>(c); }
template <int ST, int K> DI void issue_op(const Ctx& c) { constexpr int I = nth_item(ST, K >> 1); if constexpr (I >= 0) issue_half<I, (K & 1)>(c); }
template <int C> DI void issue_h0(const Ctx& c, v4u (&hf)[18][2]) { gld16_sc1_off<0>(hf[C][0], c.pH + C * 4096, c.vH); }
template <int C> DI void issue_h1(const Ctx& c, v4u (&hf)[18][2]) { gld16_sc1_off<1024>(hf[C][1], c.pH + C * 4096, c.vH); }
template <int C> DI void issue_h(const Ctx& c, v4u (&hf)[18][2]) { issue_h0<C>(c, hf); issue_h1<C>(c, hf); }

DI void spin_flags(const Ctx& c, const char* line, int n) {
#pragma unroll 1
  for (unsigned it = 0; it < SPIN_CAP; ++it) {
    unsigned v;
    gld4_sc1(v, line, (unsigned)(c.lane & 31) * 4);
    asm volatile("s_waitcnt vmcnt(0)" : "+v"(v)::"memory");
    if (__all(v == c.tag || (c.lane & 31) >= n)) return;
    __builtin_amdgcn_s_sleep(2);
  }
}
DI bool tags_ok(v4u a, unsigned tag) { return a.y == tag && a.w == tag; }

template <int N> DI void tie(f2 (&v)[N]) {
  if constexpr (N == 2) asm volatile("" : "+v"(v[0]), "+v"(v[1]));
  else if constexpr (N == 4) asm volatile("" : "+v"(v[0]), "+v"(v[1]), "+v"(v[2]), "+v"(v[3]));
  else static_assert(N == 2 || N == 4, "tie");
}
template <int N> DI void gelu_n(f2 (&u)[N]) {
  const float c0 = -2.0f * LOG2E * 0.7978845608028654f, c1 = c0 * 0.044715f;
  f2 e[N];
  tie(u);
#pragma unroll
  for (int r = 0; r < N; ++r) e[r] = u[r] * u[r];
#pragma unroll
  for (int r = 0; r < N; ++r) e[r] = __builtin_elementwise_fma(e[r], (f2){c1, c1}, (f2){c0, c0});
#pragma unroll
  for (int r = 0; r < N; ++r) e[r] = u[r] * e[r];
  tie(e);
#pragma unroll
  for (int r = 0; r < N; ++r) e[r] = (f2){__builtin_amdgcn_exp2f(e[r].x), __builtin_amdgcn_exp2f(e[r].y)};
  tie(e);
#pragma unroll
  for (int r = 0; r < N; ++r) e[r] = e[r] + (f2){1.0f, 1.0f};
  tie(e);
#pragma unroll
  for (int r = 0; r < N; ++r) e[r] = (f2){__builtin_amdgcn_rcpf(e[r].x), __builtin_amdgcn_rcpf(e[r].y)};
  tie(e);
#pragma unroll
  for (int r = 0; r < N; ++r) u[r] *= e[r];
}
DI void ln_bias(f2* u, f4 a, f4 cs, v2u bb, f2 st) {
  const f2 b0 = {bflo(bb.x), bfhi(bb.x)}, b1 = {bflo(bb.y), bfhi(bb.y)}, sx = {st.x, st.x}, sy = {-st.y, -st.y};
  u[0] = __builtin_elementwise_fma((f2){a[0], a[1]}, sx, __builtin_elementwise_fma(sy, (f2){cs[0], cs[1]}, b0));
  u[1] = __builtin_elementwise_fma((f2){a[2], a[3]}, sx, __builtin_elementwise_fma(sy, (f2){cs[2], cs[3]}, b1));
}
DI v2u ffn_tile(f4 a, f4 cs, v2u bb, f2 st) {
  f2 u[2];
  ln_bias(u, a, cs, bb, st);
  gelu_n<2>(u);
  return (v2u){pk(u[0].x, u[0].y), pk(u[1].x, u[1].y)};
}
DI v4u ffn_pair(f4 a0, f4 a1, f4 cs0, f4 cs1, v2u bb0, v2u bb1, f2 st) {
  f2 u[4];
  ln_bias(u, a0, cs0, bb0, st);
  ln_bias(u + 2, a1, cs1, bb1, st);
  gelu_n<4>(u);
  return (v4u){pk(u[0].x, u[0].y), pk(u[1].x, u[1].y), pk(u[2].x, u[2].y), pk(u[3].x, u[3].y)};
}
DI v2u ln_pack(f4 a, f4 cs, f2 st) {
  float v[4];
#pragma unroll
  for (int r = 0; r < 4; ++r) v[r] = __builtin_fmaf(a[r], st.x, -st.y * cs[r]);
  return (v2u){pk(v[0], v[1]), pk(v[2], v[3])};
}

__global__ __launch_bounds__(512, 2) void fused_kernel(const char* __restrict__ x, const char* __restrict__ wqkv, const char* __restrict__ wphi,
                                                       const char* __restrict__ wo, const char* __restrict__ w1, const char* __restrict__ b1,
                                                       const char* __restrict__ w2, const char* __restrict__ b2, unsigned short* __restrict__ out,
                                                       char* __restrict__ ws) {
  extern __shared__ __attribute__((aligned(16))) char smem[];
  Ctx c;
  c.L = (LAS char*)smem;
  c.lbase = (unsigned)(size_t)c.L;
  c.x = x; c.wo = wo; c.w2 = w2; c.ws = ws;
  c.tid = threadIdx.x; c.lane = c.tid & 63; c.w = __builtin_amdgcn_readfirstlane(c.tid >> 6); c.m = c.w & 3; c.nh = c.w >> 2;
  const int blk = blockIdx.x, xcd = blk & 7, idx = blk >> 3;
  c.rg = (xcd >> 2) * 8 + (idx >> 2); c.q = xcd & 3; c.jj = idx & 3; c.isQ = c.jj >> 1; c.hq = 2 * c.q + (c.jj & 1);
  c.rt = xcd * 4 + (idx >> 3); c.ct = idx & 7;
  c.rs = __builtin_amdgcn_make_buffer_rsrc((void*)ws, 0, WS_END, 0x00020000);
  int lane = c.lane, g = lane >> 4, l15 = lane & 15, q4 = l15 >> 2, p4 = lane & 3;
#define REFRESH() do { int t_ = threadIdx.x; asm volatile("" : "+v"(t_)); c.tid = t_; lane = t_ & 63; c.lane = lane; g = lane >> 4; l15 = lane & 15; q4 = l15 >> 2; p4 = lane & 3; } while (0)
  LAS char* L = c.L;
  STAMP(c, 0);
  asm volatile("" ::"s"(x), "s"(wqkv), "s"(wphi), "s"(wo), "s"(w1), "s"(b1), "s"(w2), "s"(b2), "s"(out), "s"(ws));
  const int f0u = 7 * c.q;
  const int fa = f0u + (c.isQ ? 2 + 2 * (c.jj & 1) : c.jj);
  c.u0 = c.isQ ? w1 + fa * 128 : wqkv + (8 + c.hq) * 128;
  c.u1 = c.u0 + (c.isQ ? 128 : 1024);
  c.u2 = c.isQ ? wqkv + c.hq * 128 : w1 + fa * 128;
  c.ld1 = c.isQ ? 3584 : 3072;
  c.ld2 = c.isQ ? 3072 : 3584;
  c.ush = w1 + (f0u + 6) * 128 + c.jj * 32;
  c.vX0 = (lane >> 3) * 1024 + (((lane & 7) ^ (lane >> 4)) * 16);
  c.vX1 = (c.vX0 ^ 64u) + 8 * 1024;
  c.vW0 = (lane >> 3) * c.ld1 + (((lane & 7) ^ ((lane >> 3) & 2)) * 16);
  c.vW1 = (c.vW0 ^ 64u) + 8 * c.ld1;
  c.pX = x + (size_t)(128 * c.rg + 16 * c.w) * 1024;
  c.pW = (c.nh ? c.u1 : c.u0) + (size_t)(16 * c.m) * c.ld1;
  c.ldsw = c.lbase + L_RING + c.w * 2048;

  issue_half<0, 0>(c); issue_half<0, 1>(c); issue_half<1, 0>(c); issue_half<1, 1>(c);
  c.tag = *(const __attribute__((address_space(4))) unsigned*)(ws + WS_EPOCH) + 1u;
  v4u xr, cw0, cw1;
  const int j16 = 4 * c.q + c.jj;
  gld16(xr, x + (size_t)(128 * c.rg + 8 * j16 + c.w) * 1024, lane * 16);
  const int job = (xcd >> 2) * 32 + idx;
  const int jobc = job < 52 ? job : 51;
  const int li = jobc >> 2, kq = jobc & 3;
  const int gunit = li < 6 ? (li >> 1) * 8 + 2 * c.q + (li & 1) : 24 + f0u + (li - 6);
  {
    const char* wb = li < 6 ? wqkv + gunit * 128 : w1 + (gunit - 24) * 128;
    const unsigned ld = li < 6 ? 3072 : 3584;
    const char* bb = wb + (size_t)(128 * kq + 16 * c.w) * ld;
    const unsigned v = (lane >> 3) * ld + (lane & 7) * 16;
    gld16(cw0, bb, v);
    gld16(cw1, bb + 8 * ld, v);
  }
  {
    const char* p = c.w == 0 ? b1 + fa * 128 : c.w == 1 ? b1 + (f0u + 6) * 128 + c.jj * 32 : c.w < 4 ? wphi + c.hq * 2048 + (c.w - 2) * 1024 : b2 + c.ct * 128;
    const unsigned d = c.lbase + (c.w == 0 ? L_B1 : c.w == 1 ? L_B1 + 256 : c.w < 4 ? L_WPHI + (c.w - 2) * 1024 : L_B2);
    if (c.w == 2 || c.w == 3) glds16(p, lane * 16, d);
    else glds4(p, (lane & (c.w == 0 ? 63 : c.w == 1 ? 7 : 31)) * 4, d);
  }
  issue_half<2, 0>(c); issue_half<2, 1>(c);
  if constexpr (NPRO > 3) { issue_half<3, 0>(c); issue_half<3, 1>(c); }
  if constexpr (NPRO > 4) { issue_half<4, 0>(c); issue_half<4, 1>(c); }
  if constexpr (NPRO > 5) { issue_half<5, 0>(c); issue_half<5, 1>(c); }
  if constexpr (NPRO > 6) { issue_half<6, 0>(c); issue_half<6, 1>(c); }
  {
    const int id = c.tid, half = id & 1, ln = id >> 1;
    const char* ax; const char* aw;
    { const int r = (ln * 205) >> 10, sg = ln - 5 * r; ax = x + (size_t)(128 * c.rg + 32 * c.jj + r) * 1024 + (3 + sg) * 128; }
    { const int lj = ln < 160 ? 0 : ln < 240 ? ln - 160 : 79, un = lj >= 40, r = lj - 40 * un; aw = (un ? c.u1 : c.u0) + (size_t)(192 + 40 * (c.rg & 7) + r) * c.ld1; }
    glds4v((c.w < 5 ? ax : aw) + half * 64, c.lbase + L_DUMMY);
  }
  {
    unsigned long long pc; unsigned len;
    asm volatile("s_getpc_b64 %0\n.Lhh_pc_here:\n\ts_mov_b32 %1, .Lhh_code_end-.Lhh_pc_here" : "=s"(pc), "=s"(len));
    if (c.w == 7) {
      unsigned o = (unsigned)(idx * 64 + lane) * 64;
      o = o < len ? o : 0;
      glds4v((const char*)pc + o, c.lbase + L_DUMMY);
    }
  }
  unsigned g1off;
  {
    const int t = c.tid;
    const int ci = t - 128 < 208 ? t - 128 : 207;
    const int un = ci >> 6;
    const int ufa = 24 + fa;
    const int ua = c.isQ ? ufa : 8 + c.hq, ub = c.isQ ? ufa + 1 : 16 + c.hq, uc = c.isQ ? c.hq : ufa;
    const int gu = un == 0 ? ua : un == 1 ? ub : un == 2 ? uc : 24 + f0u + 6;
    const int col = gu * 64 + (un == 3 ? 16 * c.jj + (ci & 15) : (ci & 63));
    g1off = t < 128 ? WS_STATS + (128 * c.rg + t) * 16 : WS_CS + col * 32;
    asm volatile("" : "+v"(g1off));
  }
  STAMP(c, 1);

  v4u xf[2][16];
  f4 acc[2][2][2];
  const f4 z4 = {0.f, 0.f, 0.f, 0.f};
#pragma unroll
  for (int a = 0; a < 2; ++a)
#pragma unroll
    for (int b = 0; b < 2; ++b)
#pragma unroll
      for (int t = 0; t < 2; ++t) acc[a][b][t] = z4;
  const unsigned xa0 = (unsigned)((32 * c.m + l15) * 128 + ((g ^ (l15 >> 1)) * 16));
  const unsigned xtt0 = c.nh * 2048, xtt1 = (1 - c.nh) * 2048;
  const unsigned wa0 = (unsigned)((8 * g + q4) * 128 + (((4 * c.nh + (p4 >> 1)) ^ ((((q4 >> 1) & 1) | ((g & 1) << 1)) << 1)) * 16) + (p4 & 1) * 8);
  unsigned xb[2][4] = {{xa0 + xtt0, xa0 + xtt1, (xa0 ^ 64) + xtt0, (xa0 ^ 64) + xtt1}, {}}, wa[2][2] = {{wa0, wa0 ^ 32}, {}};
#pragma unroll
  for (int i = 0; i < 4; ++i) { xb[1][i] = xb[0][i] + HIWIN; asm volatile("" : "+v"(xb[1][i])); }
#pragma unroll
  for (int i = 0; i < 2; ++i) { wa[1][i] = wa[0][i] + HIWIN; asm volatile("" : "+v"(wa[1][i])); }
  v4u g1a, g1b;
  v4u wfa[2][2], wfb[2][2];
  auto pass1_step = [&](auto KC) {
    constexpr int kc = decltype(KC)::value;
    constexpr unsigned sx = L_RING + SC.slot[2 * kc] * 16384, sw = L_RING + SC.slot[2 * kc + 1] * 16384;
    TS(3 * kc);
    if constexpr (kc > 0)
      asm volatile("" : "+v"(wfb[0][0]), "+v"(wfb[0][1]), "+v"(wfb[1][0]), "+v"(wfb[1][1]), "+v"(xf[0][kc > 0 ? 2 * kc - 1 : 0]), "+v"(xf[1][kc > 0 ? 2 * kc - 1 : 0]));
    if constexpr (kc == 0) {
      wait_top<top_wait(kc)>();
    } else {
      wait_vm<top_wait(kc)>();
    }
    TS(3 * kc + 1);
    bar();
    TS(3 * kc + 2);
    FENCE();
    if constexpr (kc == 5) {
      REFRESH();
      c.vP0 = (lane >> 3) * c.ld2 + (((lane & 7) ^ ((lane >> 3) & 2)) * 16);
      c.vP1 = (c.vP0 ^ 64u) + 8 * c.ld2;
      c.pP = c.u2 + (size_t)(16 * c.w) * c.ld2;
    }
    if constexpr (kc == 7) {
      REFRESH();
      c.vF0 = (lane >> 3) * 1024 + (((lane & 7) ^ ((lane >> 3) & 6)) * 16);
      c.vF1 = c.vF0 + 8 * 1024;
      const unsigned o = (16 * c.m) * 1024 + c.ct * 128;
      c.pF1 = w2 + o + c.nh * 65536; c.pF5 = w2 + o + c.nh * 5 * 65536; c.pWo = wo + o + c.nh * 65536;
    }
    constexpr int hx = sx >= HIWIN, hw = sw >= HIWIN;
    const unsigned x0 = xb[hx][0], x1 = xb[hx][1], x2 = xb[hx][2], x3 = xb[hx][3], w0 = wa[hw][0], w1_ = wa[hw][1];
    constexpr unsigned rx = sx - hx * HIWIN, rw = sw - hw * HIWIN;
#define P1_MMA(F, KS, UN, I, T) do { if constexpr (DOM_) acc[UN][I][T] = mfma32(F[UN][I], xf[T][KS], acc[UN][I][T]); FENCE(); } while (0)
#define P1_HALF(FR, FM, KSR, KSM, XA, XB, SOFF, G0, G1, DOM, TIE) {                                 \
    constexpr bool DOM_ = DOM;                                                                     \
    v2u t0, t1;                                                                                    \
    xf[0][KSR] = ld128(L, XA + rx); FENCE();                                                       \
    if constexpr (TIE) {                                                                           \
      xf[1][KSR] = ld128(L, XB + rx); FENCE();                                                     \
      t0 = trd(L, w0 + rw + SOFF); FENCE();                                                        \
      asm volatile("" : "+v"(FM[0][0]), "+v"(FM[0][1]), "+v"(FM[1][0]), "+v"(FM[1][1]), "+v"(xf[0][KSM]), "+v"(xf[1][KSM]));   \
      FENCE();                                                                                     \
      P1_MMA(FM, KSM, 0, 0, 0);                                                                    \
    } else {                                                                                       \
      P1_MMA(FM, KSM, 0, 0, 0);                                                                    \
      xf[1][KSR] = ld128(L, XB + rx); FENCE();                                                     \
      t0 = trd(L, w0 + rw + SOFF); FENCE();                                                        \
    }                                                                                              \
    P1_MMA(FM, KSM, 0, 0, 1);                                                                      \
    t1 = trd(L, w0 + rw + SOFF + 512); FR[0][0] = (v4u){t0.x, t0.y, t1.x, t1.y}; FENCE();          \
    op_m0<kc, G0>(c); FENCE();                                                                     \
    P1_MMA(FM, KSM, 0, 1, 0);                                                                      \
    FR[0][1] = trd2(L, w1_ + rw + SOFF, w1_ + rw + SOFF + 512); FENCE();                           \
    P1_MMA(FM, KSM, 0, 1, 1);                                                                      \
    t0 = trd(L, w0 + rw + SOFF + 8192); FENCE();                                                   \
    op_go<kc, G0>(c); FENCE();                                                                     \
    P1_MMA(FM, KSM, 1, 0, 0);                                                                      \
    t1 = trd(L, w0 + rw + SOFF + 8192 + 512); FR[1][0] = (v4u){t0.x, t0.y, t1.x, t1.y}; FENCE();   \
    op_m0<kc, G1>(c); FENCE();                                                                     \
    P1_MMA(FM, KSM, 1, 0, 1);                                                                      \
    FR[1][1] = trd2(L, w1_ + rw + SOFF + 8192, w1_ + rw + SOFF + 8192 + 512); FENCE();             \
    P1_MMA(FM, KSM, 1, 1, 0);                                                                      \
    op_go<kc, G1>(c); FENCE();                                                                     \
    P1_MMA(FM, KSM, 1, 1, 1);                                                                      \
  }
    P1_HALF(wfa, wfb, 2 * kc, (kc > 0 ? 2 * kc - 1 : 0), x0, x1, 0, 0, 1, (kc > 0), false)
    if constexpr (kc == 0) { issue_op<0, 2>(c); issue_op<0, 3>(c); issue_op<0, 4>(c); issue_op<0, 5>(c); FENCE(); }
    if constexpr (kc == 0) {
      asm volatile("s_waitcnt vmcnt(%3)" : "+v"(xr), "+v"(cw0), "+v"(cw1) : "n"(SC.cum[0] - 1 - PRO_REG_LAST + imin(SC.cum[1] - SC.cum[0], 6)) : "memory");
      const float x0v = bitsf(__builtin_amdgcn_readfirstlane(xr.x << 16));
      float d[8] = {bflo(xr.x) - x0v, bfhi(xr.x) - x0v, bflo(xr.y) - x0v, bfhi(xr.y) - x0v, bflo(xr.z) - x0v, bfhi(xr.z) - x0v, bflo(xr.w) - x0v, bfhi(xr.w) - x0v};
      float s = ((d[0] + d[1]) + (d[2] + d[3])) + ((d[4] + d[5]) + (d[6] + d[7]));
      float qq = d[0] * d[0];
#pragma unroll
      for (int e = 1; e < 8; ++e) qq = __builtin_fmaf(d[e], d[e], qq);
      s += dpp<0x128>(s); qq += dpp<0x128>(qq);
      s += dpp<0x124>(s); qq += dpp<0x124>(qq);
      s += dpp<0x122>(s); qq += dpp<0x122>(qq);
      s += dpp<0x121>(s); qq += dpp<0x121>(qq);
      sw32(s, qq);
      float sq = s + qq;
      { float a = sq, b = sq; sw16(a, b); sq = a + b; }
      const float sd = sq * (1.0f / 512.0f);
      const float m2 = bitsf(__builtin_amdgcn_readlane(fbits(sd), 32));
      if (lane == 0) {
        const float var = __builtin_fmaf(-sd, sd, m2);
        const float rstd = __builtin_amdgcn_rsqf(vmax(var, 0.f) + 1e-5f);
        __builtin_amdgcn_raw_buffer_store_b128((v4u){fbits(x0v + sd), c.tag, fbits(rstd), c.tag}, c.rs, WS_STATS + (128 * c.rg + 8 * j16 + c.w) * 16, 0, AUX_SC1);
      }
      FENCE();
    }
    if constexpr (kc == 1) {
      float cs[8] = {bflo(cw0.x) + bflo(cw1.x), bfhi(cw0.x) + bfhi(cw1.x), bflo(cw0.y) + bflo(cw1.y), bfhi(cw0.y) + bfhi(cw1.y),
                     bflo(cw0.z) + bflo(cw1.z), bfhi(cw0.z) + bfhi(cw1.z), bflo(cw0.w) + bflo(cw1.w), bfhi(cw0.w) + bfhi(cw1.w)};
#pragma unroll
      for (int e = 0; e < 4; ++e) { sw32(cs[e], cs[e + 4]); cs[e] += cs[e + 4]; }
#pragma unroll
      for (int e = 0; e < 2; ++e) { sw16(cs[e], cs[e + 2]); cs[e] += cs[e + 2]; }
      cs[0] += dpp<0x128>(cs[0]); cs[1] += dpp<0x128>(cs[1]);
      if ((lane & 8) == 0) *(LAS f2*)(L + L_SCR + c.w * 256 + ((lane & 7) * 8 + (lane >> 5) * 4 + ((lane >> 4) & 1) * 2) * 4) = (f2){cs[0], cs[1]};
      FENCE();
    }
#define BLAST2_STEP 3
    if constexpr (kc == BLAST2_STEP) {
      if (c.w < 3) {
        const int id = threadIdx.x < 144 ? threadIdx.x : 143;
        const int ln = blk * 72 + (id >> 1);
        const char* a = ln < 14336 ? w2 + (size_t)ln * 128 : wo + (size_t)(ln - 14336) * 128;
        glds4v(a + (id & 1) * 64, c.lbase + L_DUMMY);
      }
      FENCE();
    }
    if constexpr (kc == 2) {
      if (c.w == 7 && job < 52) {
        float s = 0.f;
#pragma unroll
        for (int ww = 0; ww < 8; ++ww) s += *(LAS float*)(L + L_SCR + ww * 256 + lane * 4);
        __builtin_amdgcn_raw_buffer_store_b64((v2u){fbits(s), c.tag}, c.rs, WS_CS + (gunit * 64 + lane) * 32 + kq * 8, 0, AUX_SC1);
      }
      FENCE();
    }
    P1_HALF(wfb, wfa, 2 * kc + 1, 2 * kc, x2, x3, 4096, (kc == 0 ? 6 : 2), (kc == 0 ? 7 : 3), true, true)
    if constexpr (kc == ST_G1_ISSUE) {
      gld16_sc1(g1a, ws, g1off);
      gld16_sc1(g1b, ws, threadIdx.x < 128 ? g1off : g1off + 16);
      FENCE();
    }
  };
  pass1_step(IC<0>{});
  STAMP(c, 2);
  pass1_step(IC<1>{});
  pass1_step(IC<2>{});
  pass1_step(IC<3>{});
  pass1_step(IC<4>{});
  pass1_step(IC<5>{});
  pass1_step(IC<6>{});
  pass1_step(IC<7>{});
  STAMP(c, 3);

  TS(3 * ST_E1);
  asm volatile("" : "+v"(wfb[0][0]), "+v"(wfb[0][1]), "+v"(wfb[1][0]), "+v"(wfb[1][1]), "+v"(xf[0][15]), "+v"(xf[1][15]));
  {
      asm volatile("s_waitcnt vmcnt(%2) lgkmcnt(0)" : "+v"(g1a), "+v"(g1b) : "n"(top_wait(ST_E1)) : "memory");
      REFRESH();
      const bool isst = c.tid < 128;
      if (__builtin_expect(!__all(tags_ok(g1a, c.tag) && (isst || tags_ok(g1b, c.tag))), 0)) {
        const unsigned off = g1off;
#pragma unroll 1
        for (unsigned it = 0; it < SPIN_CAP; ++it) {
          __builtin_amdgcn_s_sleep(2);
          gld16_sc1(g1a, ws, off);
          gld16_sc1(g1b, ws, isst ? off : off + 16);
          asm volatile("s_waitcnt vmcnt(0)" : "+v"(g1a), "+v"(g1b)::"memory");
          if (__all(tags_ok(g1a, c.tag) && (isst || tags_ok(g1b, c.tag)))) break;
        }
      }
      if (isst) *(LAS f2*)(L + L_ST + c.tid * 8) = (f2){bitsf(g1a.z), bitsf(g1a.x) * bitsf(g1a.z)};
      else if (c.tid < 128 + 208) *(LAS float*)(L + L_CS + (c.tid - 128) * 4) = (bitsf(g1a.x) + bitsf(g1a.z)) + (bitsf(g1b.x) + bitsf(g1b.z));
      wait_lgkm();
  }
  TS(3 * ST_E1 + 1);
  bar();
  TS(3 * ST_E1 + 2);
  FENCE();
  REFRESH();
  const unsigned gt0 = 8 * c.rg + 2 * c.m + c.nh, gt1 = 8 * c.rg + 2 * c.m + 1 - c.nh;
  {
    f4 cs4[2][2];
#pragma unroll
    for (int un = 0; un < 2; ++un)
#pragma unroll
      for (int i = 0; i < 2; ++i) cs4[un][i] = ldf4(L, L_CS + (64 * un + 32 * c.nh + 16 * i + 4 * g) * 4);
    const f2 st0 = ldf2(L, L_ST + (16 * (2 * c.m + c.nh) + l15) * 8), st1 = ldf2(L, L_ST + (16 * (2 * c.m + 1 - c.nh) + l15) * 8);
    FENCE();
#pragma unroll
    for (int un = 0; un < 2; ++un)
#pragma unroll
      for (int i = 0; i < 2; ++i) {
        acc[un][i][0] = mfma32(wfb[un][i], xf[0][15], acc[un][i][0]);
        acc[un][i][1] = mfma32(wfb[un][i], xf[1][15], acc[un][i][1]);
      }
    FENCE();
    TSX(0);
    if (c.isQ) {
      v2u bb[2][2];
#pragma unroll
      for (int un = 0; un < 2; ++un)
#pragma unroll
        for (int i = 0; i < 2; ++i) bb[un][i] = ld64(L, L_B1 + un * 128 + c.nh * 64 + i * 32 + 8 * g);
      const unsigned ks0 = 2 * unit_pos(c.q, 2 + 2 * (c.jj & 1)) + c.nh;
      const unsigned ho0 = WS_H + (gt0 * 72 + ks0) * 1024 + lane * 16, ho1 = WS_H + (gt1 * 72 + ks0) * 1024 + lane * 16;
      PRIO(1);
      {
        __builtin_amdgcn_raw_buffer_store_b128(ffn_pair(acc[0][0][0], acc[0][1][0], cs4[0][0], cs4[0][1], bb[0][0], bb[0][1], st0), c.rs, ho0, 0, AUX_SC1);
      }
      FENCE(); PRIO(0); issue_op<ST_E1, 0>(c); FENCE();
      {
        __builtin_amdgcn_raw_buffer_store_b128(ffn_pair(acc[0][0][1], acc[0][1][1], cs4[0][0], cs4[0][1], bb[0][0], bb[0][1], st1), c.rs, ho1, 0, AUX_SC1);
      }
      FENCE(); PRIO(1); issue_op<ST_E1, 1>(c); FENCE();
      {
        __builtin_amdgcn_raw_buffer_store_b128(ffn_pair(acc[1][0][0], acc[1][1][0], cs4[1][0], cs4[1][1], bb[1][0], bb[1][1], st0), c.rs, ho0 + 2048, 0, AUX_SC1);
      }
      FENCE(); PRIO(0);
      {
        __builtin_amdgcn_raw_buffer_store_b128(ffn_pair(acc[1][0][1], acc[1][1][1], cs4[1][0], cs4[1][1], bb[1][0], bb[1][1], st1), c.rs, ho1 + 2048, 0, AUX_SC1);
      }
    } else {
      const v4u ones = {0x3f803f80u, 0x3f803f80u, 0x3f803f80u, 0x3f803f80u};
      const v2u wt0 = trd(L, L_WPHI + (2 * c.nh) * 512 + (4 * g + q4) * 32 + p4 * 8), wt1 = trd(L, L_WPHI + (2 * c.nh + 1) * 512 + (4 * g + q4) * 32 + p4 * 8);
      f4 pk4[4];
      v2u vb[4], fp[4], fn[4];
      PRIO(1);
#pragma unroll
      for (int i = 0; i < 2; ++i)
#pragma unroll
        for (int tt = 0; tt < 2; ++tt) pk4[2 * i + tt] = mfma16(i ? wt1 : wt0, ln_pack(acc[0][i][tt], cs4[0][i], tt ? st1 : st0), z4);
#pragma unroll
      for (int i = 0; i < 2; ++i)
#pragma unroll
        for (int tt = 0; tt < 2; ++tt) vb[2 * i + tt] = ln_pack(acc[1][i][tt], cs4[1][i], tt ? st1 : st0);
      TSX(1);
      FENCE(); PRIO(0); issue_op<ST_E1, 0>(c); FENCE();
      Feat ft;
      feat_max(pk4, ft);
      TSX(2);
      FENCE(); PRIO(1); issue_op<ST_E1, 1>(c); FENCE();
      feat_exp(pk4, ft);
      TSX(3);
      FENCE(); PRIO(0); FENCE();
      feat_norm(ft, fp, fn);
      FENCE();
      TSX(4);
      constexpr unsigned IMG_P = L_SCR, IMG_N = L_SCR + 16384, IMG_V = L_RING + SC.slot[15] * 16384;
      const unsigned wo_ = (2 * c.nh) * 4096 + (32 * c.m + l15) * 32 + ((g ^ q4) * 8);
#pragma unroll
      for (int i = 0; i < 2; ++i)
#pragma unroll
        for (int tt = 0; tt < 2; ++tt) {
          *(LAS v2u*)(L + IMG_P + wo_ + i * 4096 + tt * 512) = fp[2 * i + tt];
          *(LAS v2u*)(L + IMG_N + wo_ + i * 4096 + tt * 512) = fn[2 * i + tt];
          *(LAS v2u*)(L + IMG_V + wo_ + i * 4096 + tt * 512) = vb[2 * i + tt];
        }
      wait_lgkm();
      TSX(5);
      bar();
      TSX(6);
      const unsigned ro = (c.w >> 1) * 4096 + (4 * g + q4) * 32 + ((p4 ^ g) * 8);
      const unsigned ra = IMG_P + (c.w & 1) * 16384 + ro, rb = IMG_V + ro;
      v4u A[4], B[4];
#pragma unroll
      for (int j = 0; j < 4; ++j) { A[j] = trd2(L, ra + j * 1024, ra + j * 1024 + 512); B[j] = trd2(L, rb + j * 1024, rb + j * 1024 + 512); }
      f4 kv0 = mfma32(A[0], B[0], z4), kv1 = mfma32(A[1], B[1], z4), z0 = mfma32(A[0], ones, z4), z1 = mfma32(A[1], ones, z4);
      kv0 = mfma32(A[2], B[2], kv0); kv1 = mfma32(A[3], B[3], kv1); z0 = mfma32(A[2], ones, z0); z1 = mfma32(A[3], ones, z1);
      kv0 += kv1; z0 += z1;
      TSX(7);
      const unsigned pbase = WS_P + (c.hq * 16 + c.rg) * PART_BYTES;
      __builtin_amdgcn_raw_buffer_store_b128((v4u){fbits(kv0[0]), c.tag, fbits(kv0[1]), c.tag}, c.rs, pbase + (c.w * 128 + lane) * 16, 0, AUX_SC1);
      __builtin_amdgcn_raw_buffer_store_b128((v4u){fbits(kv0[2]), c.tag, fbits(kv0[3]), c.tag}, c.rs, pbase + (c.w * 128 + 64 + lane) * 16, 0, AUX_SC1);
      if (l15 == 0) {
        __builtin_amdgcn_raw_buffer_store_b128((v4u){fbits(z0[0]), c.tag, fbits(z0[1]), c.tag}, c.rs, pbase + (1024 + c.w * 8 + g * 2) * 16, 0, AUX_SC1);
        __builtin_amdgcn_raw_buffer_store_b128((v4u){fbits(z0[2]), c.tag, fbits(z0[3]), c.tag}, c.rs, pbase + (1024 + c.w * 8 + g * 2 + 1) * 16, 0, AUX_SC1);
      }
    }
  }
  PRIO_OFF();
  TSX(8);
  STAMP(c, 4);
  FENCE();

  f4 acc2[2][2], accS = z4;
#pragma unroll
  for (int b = 0; b < 2; ++b)
#pragma unroll
    for (int t = 0; t < 2; ++t) acc2[b][t] = z4;
  REFRESH();
  const unsigned wb0 = (unsigned)((8 * g + q4) * 128 + (((4 * c.nh + (p4 >> 1)) ^ ((((q4 >> 1) & 1) | ((g & 1) << 1)) << 1)) * 16) + (p4 & 1) * 8);
  const unsigned sa0 = (unsigned)((8 * g + ((g & 1) << 2) + q4) * 32 + p4 * 8);
  constexpr unsigned SS = L_RING + SC.slot[IT_S] * 16384;
  unsigned wbb[2][2] = {{wb0, wb0 ^ 32}, {}};
#pragma unroll
  for (int i = 0; i < 2; ++i) { wbb[1][i] = wbb[0][i] + HIWIN; asm volatile("" : "+v"(wbb[1][i])); }
  unsigned s0 = sa0 + (SS >= HIWIN ? HIWIN : 0), s1 = (sa0 ^ 128) + (SS >= HIWIN ? HIWIN : 0);
  asm volatile("" : "+v"(s0), "+v"(s1));
  unsigned pollv;
  v4u rk0, rk1, rz0, rz1;
  const char* flagA = ws + WS_FLAG + (0 * 16 + (c.rt >> 1)) * 128;
  const char* flagB = ws + WS_FLAG + (1 * 16 + (c.rt >> 1)) * 128;
  const char* flagO = ws + WS_FLAG + (2 * 16 + (c.rt >> 1)) * 128;
  const char* rsrc = ws + WS_P + (c.hq * 16 + 2 * c.w) * PART_BYTES;
  v4u pwa[2], psa, pwb[2], psb;

  auto pass2_step = [&](auto KC) {
    constexpr int kc = decltype(KC)::value, st = 9 + kc;
    constexpr unsigned sp = L_RING + SC.slot[IT_P0 + kc] * 16384, ss = L_RING + SC.slot[IT_S] * 16384;
    TS(3 * st);
    if constexpr (kc > 0) { asm volatile("" : "+v"(pwb[0]), "+v"(pwb[1]), "+v"(psb)); wait_vm<top_wait(st)>(); }
    else wait_top<top_wait(st)>();
    TS(3 * st + 1);
    bar();
    TS(3 * st + 2);
    FENCE();
    constexpr int hp = sp >= HIWIN;
    const unsigned w0 = wbb[hp][0], w1_ = wbb[hp][1];
    constexpr unsigned rp = sp - hp * HIWIN, rs_ = ss - (ss >= HIWIN ? HIWIN : 0);
#define P2_MMA(A, F, T, KM) do { if constexpr (DOM_) A = mfma32(F, xf[T][KM], A); FENCE(); } while (0)
#define P2_Q(FR, SR, KR, FM, SM, KM, DOM, TIE) {                                                    \
    constexpr bool DOM_ = DOM;                                                                     \
    v2u t0, t1;                                                                                    \
    t0 = trd(L, w0 + rp + ((KR) & 3) * 4096); FENCE();                                             \
    if constexpr (TIE) { asm volatile("" : "+v"(FM[0]), "+v"(FM[1]), "+v"(SM)); FENCE(); }         \
    P2_MMA(acc2[0][0], FM[0], 0, KM);                                                              \
    t1 = trd(L, w0 + rp + ((KR) & 3) * 4096 + 512); FR[0] = (v4u){t0.x, t0.y, t1.x, t1.y}; FENCE(); \
    P2_MMA(acc2[0][1], FM[0], 1, KM);                                                              \
    FR[1] = trd2(L, w1_ + rp + ((KR) & 3) * 4096, w1_ + rp + ((KR) & 3) * 4096 + 512); FENCE();    \
    P2_MMA(acc2[1][0], FM[1], 0, KM);                                                              \
    t0 = trd(L, s0 + rs_ + (KR) * 1024); FENCE();                                                  \
    P2_MMA(acc2[1][1], FM[1], 1, KM);                                                              \
    t1 = trd(L, s1 + rs_ + (KR) * 1024); SR = (v4u){t0.x, t0.y, t1.x, t1.y}; FENCE();              \
    P2_MMA(accS, SM, 0, KM);                                                                       \
  }
    op_m0<st, 0>(c);
    P2_Q(pwa, psa, 4 * kc, pwb, psb, (kc > 0 ? 4 * kc - 1 : 0), (kc > 0), false)
    op_go<st, 0>(c);
    if constexpr (st == ST_PUBA) {
      if (c.isQ && threadIdx.x == 0) __builtin_amdgcn_raw_buffer_store_b32(c.tag, c.rs, WS_FLAG + (0 * 16 + c.rg) * 128 + c.hq * 4, 0, AUX_SC1);
    }
    FENCE();
    op_m0<st, 1>(c);
    P2_Q(pwb, psb, 4 * kc + 1, pwa, psa, 4 * kc, true, true)
    op_go<st, 1>(c);
    FENCE();
    P2_Q(pwa, psa, 4 * kc + 2, pwb, psb, 4 * kc + 1, true, true)
    if constexpr (st == ST_POLLA_ISSUE) { REFRESH(); gld4_sc1(pollv, flagA, (lane & 31) * 4); }
    if constexpr (st == ST_R_ISSUE) {
      REFRESH();
      const unsigned vk = c.isQ ? (64 * c.rg + lane) * 16 : 0, vz = c.isQ ? (1024 + 4 * c.rg + (lane & 3)) * 16 : 0;
      gld16_sc1(rk0, rsrc, vk);
      gld16_sc1(rk1, rsrc + PART_BYTES, vk);
      gld16_sc1(rz0, rsrc, vz);
      gld16_sc1(rz1, rsrc + PART_BYTES, vz);
    }
    FENCE();
    P2_Q(pwb, psb, 4 * kc + 3, pwa, psa, 4 * kc + 2, true, true)
  };
  pass2_step(IC<0>{});
  pass2_step(IC<1>{});
  pass2_step(IC<2>{});
  pass2_step(IC<3>{});
  STAMP(c, 5);

  v4u hf[18][2];
  v4u rg0, rg1, rgz0, rgz1;
  TS(3 * ST_E2);
  wait_top<top_wait(ST_E2)>();
  asm volatile("s_waitcnt vmcnt(%5)" : "+v"(pollv), "+v"(rk0), "+v"(rk1), "+v"(rz0), "+v"(rz1)
               : "n"(imin(extra_wait(ST_POLLA_ISSUE, ST_E2), extra_wait(ST_R_ISSUE, ST_E2))) : "memory");
  REFRESH();
  if (c.w == 0) {
    if (__builtin_expect(!__all(pollv == c.tag || (lane & 31) >= 8), 0)) spin_flags(c, flagA, 8);
  }
  if (c.isQ) {
    if (__builtin_expect(!__all(tags_ok(rk0, c.tag) && tags_ok(rk1, c.tag) && tags_ok(rz0, c.tag) && tags_ok(rz1, c.tag)), 0)) {
      const unsigned vk = (64 * c.rg + lane) * 16, vz = (1024 + 4 * c.rg + (lane & 3)) * 16;
#pragma unroll 1
      for (unsigned it = 0; it < SPIN_CAP; ++it) {
        __builtin_amdgcn_s_sleep(2);
        gld16_sc1(rk0, rsrc, vk);
        gld16_sc1(rk1, rsrc + PART_BYTES, vk);
        gld16_sc1(rz0, rsrc, vz);
        gld16_sc1(rz1, rsrc + PART_BYTES, vz);
        asm volatile("s_waitcnt vmcnt(0)" : "+v"(rk0), "+v"(rk1), "+v"(rz0), "+v"(rz1)::"memory");
        if (__all(tags_ok(rk0, c.tag) && tags_ok(rk1, c.tag) && tags_ok(rz0, c.tag) && tags_ok(rz1, c.tag))) break;
      }
    }
    *(LAS f2*)(L + L_SCR + c.w * 1024 + lane * 8) = (f2){bitsf(rk0.x) + bitsf(rk1.x), bitsf(rk0.z) + bitsf(rk1.z)};
    if (lane < 4) *(LAS f2*)(L + L_SCR + c.w * 1024 + 512 + lane * 8) = (f2){bitsf(rz0.x) + bitsf(rz1.x), bitsf(rz0.z) + bitsf(rz1.z)};
    wait_lgkm();
  }
  TS(3 * ST_E2 + 1);
  bar();
  TS(3 * ST_E2 + 2);
  FENCE();
  c.pH = ws + WS_H + (size_t)((4 * c.rt + c.m) * 72 + 2 * c.nh) * 1024;
  c.vH = lane * 16;
  f2 st0, st1;
  f4 cs2[2];
  f4 pq4[4];
  Feat ftq;
  v2u fp[4], fn[4];
  {
#pragma unroll
    for (int i = 0; i < 2; ++i) {
      acc2[i][0] = mfma32(pwb[i], xf[0][15], acc2[i][0]);
      acc2[i][1] = mfma32(pwb[i], xf[1][15], acc2[i][1]);
    }
    accS = mfma32(psb, xf[0][15], accS);
    st0 = ldf2(L, L_ST + (16 * (2 * c.m + c.nh) + l15) * 8); st1 = ldf2(L, L_ST + (16 * (2 * c.m + 1 - c.nh) + l15) * 8);
    cs2[0] = ldf4(L, L_CS + (128 + 32 * c.nh + 4 * g) * 4); cs2[1] = ldf4(L, L_CS + (128 + 32 * c.nh + 16 + 4 * g) * 4);
    const f4 csS = ldf4(L, L_CS + (192 + 4 * g) * 4);
    const v2u bS = ld64(L, L_B1 + 256 + 8 * g);
    FENCE();
    issue_op<ST_E2, 0>(c);
    issue_h<0>(c, hf);
    FENCE();
    TSX(9);
    PRIO(1);
    const v2u bb0 = ld64(L, L_B1 + c.nh * 64 + 8 * g), bb1 = ld64(L, L_B1 + c.nh * 64 + 32 + 8 * g);
    const unsigned ksu = 2 * unit_pos(c.q, c.jj) + c.nh;
    if (c.isQ) {
      if (c.w == 0) {
        f2 s = {0.f, 0.f}, sz = {0.f, 0.f};
#pragma unroll
        for (int ww = 0; ww < 8; ++ww) { s += ldf2(L, L_SCR + ww * 1024 + lane * 8); sz += ldf2(L, L_SCR + ww * 1024 + 512 + (lane & 3) * 8); }
        const unsigned rb = WS_R + c.hq * PART_BYTES;
        __builtin_amdgcn_raw_buffer_store_b128((v4u){fbits(s.x), c.tag, fbits(s.y), c.tag}, c.rs, rb + (64 * c.rg + lane) * 16, 0, AUX_SC1);
        if (lane < 4) __builtin_amdgcn_raw_buffer_store_b128((v4u){fbits(sz.x), c.tag, fbits(sz.y), c.tag}, c.rs, rb + (1024 + 4 * c.rg + lane) * 16, 0, AUX_SC1);
      }
      const v2u wt0 = trd(L, L_WPHI + (2 * c.nh) * 512 + (4 * g + q4) * 32 + p4 * 8), wt1 = trd(L, L_WPHI + (2 * c.nh + 1) * 512 + (4 * g + q4) * 32 + p4 * 8);
#pragma unroll
      for (int i = 0; i < 2; ++i)
#pragma unroll
        for (int tt = 0; tt < 2; ++tt) pq4[2 * i + tt] = mfma16(i ? wt1 : wt0, ln_pack(acc2[i][tt], cs2[i], tt ? st1 : st0), z4);
      feat_max(pq4, ftq);
    } else {
      __builtin_amdgcn_raw_buffer_store_b128(ffn_pair(acc2[0][0], acc2[1][0], cs2[0], cs2[1], bb0, bb1, st0), c.rs, WS_H + (gt0 * 72 + ksu) * 1024 + lane * 16, 0, AUX_SC1);
    }
    FENCE();
    PRIO(0);
    issue_op<ST_E2, 1>(c);
    issue_h<1>(c, hf);
    FENCE();
    if (c.isQ) feat_exp(pq4, ftq);
    else __builtin_amdgcn_raw_buffer_store_b128(ffn_pair(acc2[0][1], acc2[1][1], cs2[0], cs2[1], bb0, bb1, st1), c.rs, WS_H + (gt1 * 72 + ksu) * 1024 + lane * 16, 0, AUX_SC1);
    FENCE();
    TSX(10);
    PRIO(1);
    issue_h<2>(c, hf);
    FENCE();
    {
      const v2u o = ffn_tile(accS, csS, bS, st0);
      const unsigned ks = 2 * unit_pos(c.q, 6) + (c.jj >> 1);
      __builtin_amdgcn_raw_buffer_store_b64(o, c.rs, WS_H + (gt0 * 72 + ks) * 1024 + lane * 16 + (c.jj & 1) * 8, 0, AUX_SC1);
    }
    FENCE();
    TSX(11);
    PRIO(0);
    issue_op<ST_E2, 2>(c);
    issue_h<3>(c, hf);
    issue_op<ST_E2, 3>(c);
    if constexpr (HPRE > 4) issue_h<4>(c, hf);
    FENCE();
    if (c.isQ) feat_norm(ftq, fp, fn);
    FENCE();
    TSX(12);
    if constexpr (HPRE > 5) issue_h<5>(c, hf);
    if constexpr (HPRE > 6) issue_h<6>(c, hf);
    if constexpr (HPRE > 7) issue_h<7>(c, hf);
    {
      const char* rres_ = ws + WS_R + c.hq * PART_BYTES;
      const unsigned vk = c.isQ ? (c.tid >> 6) * 2048 + (c.tid & 63) * 16 : 0, vz = c.isQ ? (c.tid & 31) * 32 : 0;
      gld16_sc1(rg0, rres_, vk);
      gld16_sc1(rg1, rres_ + 1024, vk);
      gld16_sc1(rgz0, rres_ + 16384, vz);
      gld16_sc1(rgz1, rres_ + 16384 + 16, vz);
    }
    PRIO_OFF();
    TSX(13);
    static_assert(HPRE >= 4 && HPRE <= 8 && ST_RG_ISSUE == ST_E2, "initial h loads");
  }
  STAMP(c, 6);
  FENCE();

  f4 accF[4];
#pragma unroll
  for (int t = 0; t < 4; ++t) accF[t] = z4;
  REFRESH();
  const unsigned fa0 = (unsigned)((64 * c.nh + 4 * g + q4) * 128 + (((p4 >> 1) ^ ((((g & 1) << 1) | (q4 >> 1)) << 1)) * 16) + (p4 & 1) * 8);
  unsigned fb[2][4] = {{fa0, fa0 ^ 32, fa0 ^ 64, fa0 ^ 96}, {}};
#pragma unroll
  for (int i = 0; i < 4; ++i) { fb[1][i] = fb[0][i] + HIWIN; asm volatile("" : "+v"(fb[1][i])); }
  v4u xres, fwa[4], fwb[4], hprev;
  const char* rres = ws + WS_R + c.hq * PART_BYTES;

  auto phase2_step = [&](auto CC) {
    constexpr int ch = decltype(CC)::value, st = ST_F0 + ch;
    constexpr unsigned sf = L_RING + SC.slot[item_of_F(ch)] * 16384;
    TS(3 * st);
    if constexpr (ch > 0) asm volatile("" : "+v"(fwb[0]), "+v"(fwb[1]), "+v"(fwb[2]), "+v"(fwb[3]));
    if constexpr (ch > 0) asm volatile("s_waitcnt vmcnt(%2)" : "+v"(hf[ch][0]), "+v"(hf[ch][1]) : "n"(top_wait(st)) : "memory");
    else asm volatile("s_waitcnt vmcnt(%2) lgkmcnt(0)" : "+v"(hf[ch][0]), "+v"(hf[ch][1]) : "n"(top_wait(st)) : "memory");
    if constexpr (st == ST_POLLB_CHECK) {
      asm volatile("s_waitcnt vmcnt(%1)" : "+v"(pollv) : "n"(extra_wait(ST_POLLB_ISSUE, st)) : "memory");
      if (c.w == 0) { if (__builtin_expect(!__all(pollv == c.tag || (lane & 31) >= 16), 0)) spin_flags(c, flagB, 16); }
    }
    if constexpr (st == ST_POLLO_CHECK) {
      asm volatile("s_waitcnt vmcnt(%1)" : "+v"(pollv) : "n"(extra_wait(ST_POLLO_ISSUE, st)) : "memory");
      if (c.w == 0) { if (__builtin_expect(!__all(pollv == c.tag || (lane & 31) >= 8), 0)) spin_flags(c, flagO, 8); }
    }
    if constexpr (st == ST_RG_CHECK) {
      asm volatile("s_waitcnt vmcnt(%4)" : "+v"(rg0), "+v"(rg1), "+v"(rgz0), "+v"(rgz1) : "n"(extra_wait(ST_RG_ISSUE, st)) : "memory");
      if (c.isQ) {
        const unsigned t = c.tid;
        if (__builtin_expect(!__all(tags_ok(rg0, c.tag) && tags_ok(rg1, c.tag) && tags_ok(rgz0, c.tag) && tags_ok(rgz1, c.tag)), 0)) {
#pragma unroll 1
          for (unsigned it = 0; it < SPIN_CAP; ++it) {
            __builtin_amdgcn_s_sleep(2);
            gld16_sc1(rg0, rres, (t >> 6) * 2048 + (t & 63) * 16);
            gld16_sc1(rg1, rres + 1024, (t >> 6) * 2048 + (t & 63) * 16);
            gld16_sc1(rgz0, rres + 16384, (t & 31) * 32);
            gld16_sc1(rgz1, rres + 16384 + 16, (t & 31) * 32);
            asm volatile("s_waitcnt vmcnt(0)" : "+v"(rg0), "+v"(rg1), "+v"(rgz0), "+v"(rgz1)::"memory");
            if (__all(tags_ok(rg0, c.tag) && tags_ok(rg1, c.tag) && tags_ok(rgz0, c.tag) && tags_ok(rgz1, c.tag))) break;
          }
        }
        *(LAS v2u*)(L + L_SCR + (t >> 7) * 1024 + (t & 63) * 16 + ((t >> 6) & 1) * 8) =
            (v2u){pk(bitsf(rg0.x), bitsf(rg0.z)), pk(bitsf(rg1.x), bitsf(rg1.z))};
        if (t < 32)
          *(LAS v2u*)(L + L_SCR + 4096 + (t >> 3) * 64 + (t & 3) * 16 + ((t >> 2) & 1) * 8) =
              (v2u){pk(bitsf(rgz0.x), bitsf(rgz0.z)), pk(bitsf(rgz1.x), bitsf(rgz1.z))};
        wait_lgkm();
      }
    }
    TS(3 * st + 1);
    bar();
    TS(3 * st + 2);
    FENCE();
    constexpr int hi = sf >= HIWIN;
    const unsigned f0 = fb[hi][0], f1 = fb[hi][1], f2_ = fb[hi][2], f3 = fb[hi][3];
    constexpr unsigned rf = sf - hi * HIWIN;
    {
      v2u t0, t1;
      fwa[0] = trd2(L, f0 + rf, f0 + rf + 2048); FENCE();
      op_m0<st, 0>(c); FENCE();
      if constexpr (ch > 0) accF[0] = mfma32(fwb[0], hprev, accF[0]);
      FENCE();
      fwa[1] = trd2(L, f1 + rf, f1 + rf + 2048); FENCE();
      if constexpr (st == ST_PUBB) {
        if (threadIdx.x == 0) __builtin_amdgcn_raw_buffer_store_b32(c.tag, c.rs, WS_FLAG + (1 * 16 + c.rg) * 128 + (4 * c.q + c.jj) * 4, 0, AUX_SC1);
      }
      if constexpr (st == ST_PUBO) {
        if (c.isQ && threadIdx.x == 0) __builtin_amdgcn_raw_buffer_store_b32(c.tag, c.rs, WS_FLAG + (2 * 16 + c.rg) * 128 + c.hq * 4, 0, AUX_SC1);
      }
      op_go<st, 0>(c); FENCE();
      if constexpr (ch > 0) accF[1] = mfma32(fwb[1], hprev, accF[1]);
      FENCE();
      fwa[2] = trd2(L, f2_ + rf, f2_ + rf + 2048); FENCE();
      op_m0<st, 1>(c); FENCE();
      if constexpr (ch > 0) accF[2] = mfma32(fwb[2], hprev, accF[2]);
      FENCE();
      fwa[3] = trd2(L, f3 + rf, f3 + rf + 2048); FENCE();
      op_go<st, 1>(c); FENCE();
      if constexpr (ch > 0) accF[3] = mfma32(fwb[3], hprev, accF[3]);
      FENCE();
      fwb[0] = trd2(L, f0 + rf + 4096, f0 + rf + 4096 + 2048); FENCE();
      asm volatile("" : "+v"(fwa[0]), "+v"(fwa[1]), "+v"(fwa[2]), "+v"(fwa[3])); FENCE();
      accF[0] = mfma32(fwa[0], hf[ch][0], accF[0]); FENCE();
      fwb[1] = trd2(L, f1 + rf + 4096, f1 + rf + 4096 + 2048); FENCE();
      constexpr int hc = h_at(st);
      if constexpr (hc >= 0) { issue_h0<(hc >= 0 ? hc : 0)>(c, hf); FENCE(); }
      accF[1] = mfma32(fwa[1], hf[ch][0], accF[1]); FENCE();
      fwb[2] = trd2(L, f2_ + rf + 4096, f2_ + rf + 4096 + 2048); FENCE();
      accF[2] = mfma32(fwa[2], hf[ch][0], accF[2]); FENCE();
      fwb[3] = trd2(L, f3 + rf + 4096, f3 + rf + 4096 + 2048); FENCE();
      if constexpr (hc >= 0) { issue_h1<(hc >= 0 ? hc : 0)>(c, hf); FENCE(); }
      if constexpr (st == ST_POLLB_ISSUE) gld4_sc1(pollv, flagB, (lane & 31) * 4);
      if constexpr (st == ST_POLLO_ISSUE) gld4_sc1(pollv, flagO, (lane & 31) * 4);
      if constexpr (ch == 14) gld16(xres, x + (size_t)(64 * c.rt) * 1024 + c.ct * 128, (c.tid >> 3) * 1024 + (c.tid & 7) * 16);
      FENCE();
      accF[3] = mfma32(fwa[3], hf[ch][0], accF[3]);
      hprev = hf[ch][1];
      FENCE();
      (void)t0; (void)t1;
    }
    if constexpr (st == ST_O) {
      if (c.isQ) {
        v2u ob[2][2];
#pragma unroll
        for (int i = 0; i < 2; ++i) {
          const int hl = 2 * c.nh + i;
          const v4u Akv = ld128(L, L_SCR + hl * 1024 + lane * 16), Az = ld128(L, L_SCR + 4096 + hl * 64 + g * 16);
#pragma unroll
          for (int tt = 0; tt < 2; ++tt) {
            const v4u ph = {fp[2 * i + tt].x, fp[2 * i + tt].y, fn[2 * i + tt].x, fn[2 * i + tt].y};
            const f4 num = mfma32(Akv, ph, z4), den = mfma32(Az, ph, z4);
            const float inv = __builtin_amdgcn_rcpf(den[0] + 1e-6f);
            ob[i][tt] = (v2u){pk(num[0] * inv, num[1] * inv), pk(num[2] * inv, num[3] * inv)};
          }
        }
        const unsigned ks = 56 + 2 * c.hq + c.nh;
        __builtin_amdgcn_raw_buffer_store_b128((v4u){ob[0][0].x, ob[0][0].y, ob[1][0].x, ob[1][0].y}, c.rs, WS_H + (gt0 * 72 + ks) * 1024 + lane * 16, 0, AUX_SC1);
        __builtin_amdgcn_raw_buffer_store_b128((v4u){ob[0][1].x, ob[0][1].y, ob[1][1].x, ob[1][1].y}, c.rs, WS_H + (gt1 * 72 + ks) * 1024 + lane * 16, 0, AUX_SC1);
      }
    }
    FENCE();
  };
  phase2_step(IC<0>{});
  phase2_step(IC<1>{});
  phase2_step(IC<2>{});
  phase2_step(IC<3>{});
  phase2_step(IC<4>{});
  phase2_step(IC<5>{});
  STAMP(c, 7);
  phase2_step(IC<6>{});
  phase2_step(IC<7>{});
  phase2_step(IC<8>{});
  phase2_step(IC<9>{});
  phase2_step(IC<10>{});
  phase2_step(IC<11>{});
  STAMP(c, 8);
  phase2_step(IC<12>{});
  phase2_step(IC<13>{});
  phase2_step(IC<14>{});
  phase2_step(IC<15>{});
  phase2_step(IC<16>{});
  phase2_step(IC<17>{});
  STAMP(c, 9);

  TS(96);
#pragma unroll
  for (int t = 0; t < 4; ++t) accF[t] = mfma32(fwb[t], hprev, accF[t]);
  REFRESH();
#pragma unroll
  for (int t = 0; t < 4; ++t) *(LAS f4*)(L + L_SCR + c.nh * 17408 + (16 * c.m + l15) * 272 + (16 * t + 4 * g) * 4) = accF[t];
  asm volatile("s_waitcnt vmcnt(0) lgkmcnt(0)" : "+v"(xres)::"memory");
  TS(97);
  bar();
  {
    const int tok = c.tid >> 3, cc = c.tid & 7;
    const unsigned a = L_SCR + tok * 272 + cc * 32;
    f4 s0 = ldf4(L, a) + ldf4(L, a + 17408), s1 = ldf4(L, a + 16) + ldf4(L, a + 16 + 17408);
    const v4u bb = ld128(L, L_B2 + cc * 16);
    v4u o;
    o.x = pk(s0[0] + bflo(xres.x) + bflo(bb.x), s0[1] + bfhi(xres.x) + bfhi(bb.x));
    o.y = pk(s0[2] + bflo(xres.y) + bflo(bb.y), s0[3] + bfhi(xres.y) + bfhi(bb.y));
    o.z = pk(s1[0] + bflo(xres.z) + bflo(bb.z), s1[1] + bfhi(xres.z) + bfhi(bb.z));
    o.w = pk(s1[2] + bflo(xres.w) + bflo(bb.w), s1[3] + bfhi(xres.w) + bfhi(bb.w));
    { const __amdgpu_buffer_rsrc_t ro = __builtin_amdgcn_make_buffer_rsrc((void*)out, 0, 2048 * 1024, 0x00020000);
      __builtin_amdgcn_raw_buffer_store_b128(o, ro, (64 * c.rt + tok) * 1024 + c.ct * 128 + cc * 16, 0, 2);
    }
  }
  asm volatile(".Lhh_code_end:");
  if (blk == 0 && c.tid == 0) *(unsigned*)(ws + WS_EPOCH) = c.tag;
  STAMP(c, 10);
}

}

extern "C" void kernel_launch(void* const* d_in, const int* in_sizes, int n_in, void* d_out, int out_size, void* d_ws, size_t ws_size,
                              hipStream_t stream) {
  (void)in_sizes; (void)n_in; (void)out_size;
  const size_t WS_OFFSET = 0; (void)ws_size;
  (void)hipFuncSetAttribute((const void*)hh::fused_kernel, hipFuncAttributeMaxDynamicSharedMemorySize, hh::L_END);
  hh::fused_kernel<<<256, 512, hh::L_END, stream>>>((const char*)d_in[0], (const char*)d_in[1], (const char*)d_in[2], (const char*)d_in[3],
                                                    (const char*)d_in[5], (const char*)d_in[6], (const char*)d_in[7], (const char*)d_in[8],
                                                    (unsigned short*)d_out, (char*)d_ws + WS_OFFSET);
}
```

```cpp
#include <hip/hip_runtime.h>
#include <stdint.h>
#include <type_traits>

namespace hh {

#define LAS __attribute__((address_space(3)))
typedef unsigned v4u __attribute__((ext_vector_type(4)));
typedef unsigned v2u __attribute__((ext_vector_type(2)));
typedef float f4 __attribute__((ext_vector_type(4)));
typedef float f2 __attribute__((ext_vector_type(2)));
typedef short s4 __attribute__((ext_vector_type(4)));
typedef __bf16 b8 __attribute__((ext_vector_type(8)));
typedef __bf16 b2 __attribute__((ext_vector_type(2)));
template <int V> using IC = std::integral_constant<int, V>;

constexpr unsigned WS_EPOCH = 0;
constexpr unsigned WS_FLAG = 256;
constexpr unsigned WS_STATS = 8192;
constexpr unsigned WS_CS = WS_STATS + 2048 * 16;
constexpr unsigned PART_BYTES = 17408;
constexpr unsigned WS_P = WS_CS + 3328 * 32;
constexpr unsigned WS_R = WS_P + 8 * 16 * PART_BYTES;
constexpr unsigned WS_H = (WS_R + 8 * PART_BYTES + 4095u) & ~4095u;
constexpr unsigned WS_DBG = WS_H + 128 * 72 * 1024;
constexpr unsigned WS_END = WS_DBG;

constexpr int NSLOT = 7;
constexpr unsigned L_RING = 0;
constexpr unsigned L_SCR = NSLOT * 16384;
constexpr unsigned SCRW = 5248;
constexpr unsigned L_CS = L_SCR + 8 * SCRW;
constexpr unsigned L_ST = L_CS + 1024;
constexpr unsigned L_B1 = L_ST + 1024;
constexpr unsigned L_WPHI = L_B1 + 512;
constexpr unsigned L_B2 = L_WPHI + 2048;
constexpr unsigned L_DUMMY = L_B2 + 256;
constexpr unsigned L_END = L_DUMMY + 256;
static_assert(L_END <= 163840, "LDS");

constexpr int NITEM = 39, NSTEP = 32;
constexpr int ST_E1 = 8, ST_E2 = 13, ST_F0 = 14;
constexpr int IT_P0 = 16, IT_F0 = 20, IT_S = 21;
constexpr int item_of_F(int c) { return c == 0 ? IT_F0 : IT_S + c; }
#define HPRE_N 4
constexpr int HPRE = HPRE_N;
constexpr int DH = 2;
constexpr int ST_G1_ISSUE = 4, ST_G1_CHECK = 8;
#define PUBA_STEP 10
constexpr int ST_PUBA = PUBA_STEP;
constexpr int ST_POLLA_ISSUE = 12;
constexpr int ST_R_ISSUE = 11;
constexpr int ST_PUBB = 15;
constexpr int E2_OPS_AFTER_STORES = 4 + 2 * (HPRE - 4);
constexpr int ST_POLLB_ISSUE = 18, ST_POLLB_CHECK = 20;
constexpr int ST_RG_ISSUE = 13, ST_RG_CHECK = 16;
constexpr int ST_O = 17;
constexpr int ST_PUBO = 20;
constexpr int ST_POLLO_ISSUE = 24, ST_POLLO_CHECK = 26;
static_assert(ST_F0 + 8 - DH == ST_POLLB_CHECK && ST_F0 + 14 - DH == ST_POLLO_CHECK, "flag checks sit in front of the first dependent loads");

struct Sched {
  int slot[NITEM], issue[NITEM], first[NITEM], last[NITEM];
  int cum[NSTEP + 1];
  int pos_item[2][NITEM];
  int pos_h[2][18];
  int h_issue[18];
  int pos_g1[2];
};
constexpr int NPRO = 3;
constexpr int PRO_REG_LAST = 6;
constexpr Sched make_sched() {
  Sched s{};
  for (int i = 0; i < NITEM; ++i) {
    if (i < 16) { s.first[i] = i >> 1; s.last[i] = i >> 1; }
    else if (i < IT_F0) { s.first[i] = 9 + (i - IT_P0); s.last[i] = s.first[i]; }
    else if (i == IT_F0) { s.first[i] = ST_F0; s.last[i] = ST_F0; }
    else if (i == IT_S) { s.first[i] = 9; s.last[i] = 12; }
    else { s.first[i] = ST_F0 + (i - IT_S); s.last[i] = s.first[i]; }
  }
  s.last[15] = ST_E1;
  for (int c = 0; c < 18; ++c)
    s.h_issue[c] = c < HPRE ? ST_E2 : c < 8 ? ST_F0 + c - HPRE : c < 14 ? ST_POLLB_CHECK + (c - 8) / 2 : ST_POLLO_CHECK + (c - 14) / 2;
  int n = 0;
  int next = 0;
  int owner[NSLOT] = {};
  for (int k = 0; k < NSLOT; ++k) owner[k] = -2;
  for (int k = 0; k < NPRO; ++k) {
    s.slot[next] = k; s.issue[next] = -1; owner[k] = next; n += 2; s.pos_item[0][next] = s.pos_item[1][next] = n - 1; ++next;
    if (k == 1) n += 4;
  }
  for (int st = 0; st < NSTEP; ++st) {
    s.cum[st] = n;
    const int first_new = next;
    for (int rep = 0; rep < NSLOT; ++rep) {
      int best = -1;
      for (int k = 0; k < NSLOT; ++k)
        if ((owner[k] == -2 || (owner[k] >= 0 && s.last[owner[k]] < st)) && (best < 0 || owner[k] < owner[best])) best = k;
      if (best < 0) break;
      if (next < NITEM) { s.slot[next] = best; s.issue[next] = st; owner[best] = next; ++next; }
      else owner[best] = -1;
    }
    int nreg = 0;
    for (int c = 0; c < 18; ++c) if (s.h_issue[c] == st) nreg += 2;
    if (st == ST_G1_ISSUE) nreg += 2;
    const int nring = 2 * (next - first_new);
    for (int o = 0; o < 2; ++o) {
      int p = n + (o == 0 ? 0 : nreg);
      for (int i = first_new; i < next; ++i) { p += 2; s.pos_item[o][i] = p - 1; }
      p = n + (o == 0 ? nring : 0);
      for (int c = 0; c < 18; ++c) if (s.h_issue[c] == st) { p += 2; s.pos_h[o][c] = p - 1; }
      if (st == ST_G1_ISSUE) { p += 2; s.pos_g1[o] = p - 1; }
    }
    n += nring + nreg;
  }
  s.cum[NSTEP] = n;
  return s;
}
constexpr Sched SC = make_sched();
constexpr int imin(int a, int b) { return a < b ? a : b; }
constexpr int imax(int a, int b) { return a > b ? a : b; }
constexpr int top_wait(int st) {
  int need = -1;
  for (int o = 0; o < 2; ++o) {
    for (int i = 0; i < NITEM; ++i) if (SC.first[i] == st) need = imax(need, SC.pos_item[o][i]);
    if (st > ST_F0) need = imax(need, SC.pos_h[o][st - ST_F0 - 1]);
    if (st == ST_G1_CHECK) need = imax(need, SC.pos_g1[o]);
  }
  int n = need < 0 ? 63 : SC.cum[st] - 1 - need;
  if (st == 9) n += 2;
  if (st == ST_PUBA) n = imin(n, SC.cum[st] - SC.cum[ST_E1 + 1]);
  if (st == ST_PUBB) n = imin(n, SC.cum[st] - SC.cum[ST_E2 + 1] + E2_OPS_AFTER_STORES);
  if (st == ST_PUBO) n = imin(n, SC.cum[st] - SC.cum[ST_O + 1]);
  return n > 63 ? 63 : n;
}
constexpr int extra_wait(int a, int b) { int n = SC.cum[b] - SC.cum[a + 1]; return n > 63 ? 63 : n; }
constexpr int extra_wait_first(int a, int b) { int n = SC.cum[b] - SC.cum[a]; return n > 63 ? 63 : n; }
constexpr int h_at(int st, int k) {
  for (int c = 0; c < 18; ++c) if (SC.h_issue[c] == st) { if (k == 0) return c; --k; }
  return -1;
}
constexpr int nth_item(int st, int k) {
  for (int i = 0; i < NITEM; ++i) if (SC.issue[i] == st) { if (k == 0) return i; --k; }
  return -1;
}
static_assert(nth_item(0, 4) < 0 && nth_item(ST_E1, 0) == IT_S && nth_item(ST_E1, 1) < 0 && nth_item(ST_E2, 2) < 0 && nth_item(9, 1) < 0 && nth_item(10, 1) < 0 && nth_item(11, 1) < 0 && nth_item(12, 1) < 0, "schedule shape");

__host__ __device__ constexpr int unit_pos(int q, int off) { return off >= 2 && off <= 5 ? 4 * q + (off - 2) : 16 + 3 * q + (off == 6 ? 2 : off); }
__host__ __device__ constexpr int pos_unit(int pos) { return pos < 16 ? 7 * (pos >> 2) + 2 + (pos & 3) : 7 * ((pos - 16) / 3) + (((pos - 16) % 3) == 2 ? 6 : (pos - 16) % 3); }

#define DI __device__ __forceinline__
DI void glds16(const void* sbase, unsigned voff, unsigned lds) {
  asm volatile("s_mov_b32 m0, %2\n\ts_nop 0\n\tglobal_load_lds_dwordx4 %0, %1" ::"v"(voff), "s"(sbase), "s"(lds) : "memory");
}
DI void set_m0(unsigned lds) { asm volatile("s_mov_b32 m0, %0" ::"s"(lds) : "memory"); }
template <bool NT = false> DI void glds16_m0(const void* sbase, unsigned voff) {
  if constexpr (NT) asm volatile("global_load_lds_dwordx4 %0, %1 nt" ::"v"(voff), "s"(sbase) : "memory");
  else asm volatile("global_load_lds_dwordx4 %0, %1" ::"v"(voff), "s"(sbase) : "memory");
}
#define NT_X 0
#define NT_W 0
#define NT_F 0
DI void glds4(const void* sbase, unsigned voff, unsigned lds) {
  asm volatile("s_mov_b32 m0, %2\n\ts_nop 0\n\tglobal_load_lds_dword %0, %1" ::"v"(voff), "s"(sbase), "s"(lds) : "memory");
}
DI void glds4v(const void* vaddr, unsigned lds) {
  asm volatile("s_mov_b32 m0, %1\n\ts_nop 0\n\tglobal_load_lds_dword %0, off" ::"v"(vaddr), "s"(lds) : "memory");
}
DI void gld16(v4u& d, const void* sbase, unsigned voff) {
  asm volatile("global_load_dwordx4 %0, %1, %2" : "=v"(d) : "v"(voff), "s"(sbase) : "memory");
}
DI void gld16_sc1(v4u& d, const void* sbase, unsigned voff) {
  asm volatile("global_load_dwordx4 %0, %1, %2 sc1" : "=v"(d) : "v"(voff), "s"(sbase) : "memory");
}
template <int OFF> DI void gld16_sc1_off(v4u& d, const void* sbase, unsigned voff) {
  asm volatile("global_load_dwordx4 %0, %1, %2 offset:%3 sc1" : "=v"(d) : "v"(voff), "s"(sbase), "n"(OFF) : "memory");
}
DI void gld4_sc1(unsigned& d, const void* sbase, unsigned voff) {
  asm volatile("global_load_dword %0, %1, %2 sc1" : "=v"(d) : "v"(voff), "s"(sbase) : "memory");
}
template <int N> DI void wait_top() { asm volatile("s_waitcnt vmcnt(%0) lgkmcnt(0)" ::"n"(N) : "memory"); }
template <int N> DI void wait_vm() { asm volatile("s_waitcnt vmcnt(%0)" ::"n"(N) : "memory"); }
DI void wait_lgkm() { asm volatile("s_waitcnt lgkmcnt(0)" ::: "memory"); }
DI void bar() { asm volatile("" ::: "memory"); __builtin_amdgcn_s_barrier(); asm volatile("" ::: "memory"); }
#define FENCE() __builtin_amdgcn_sched_barrier(0)

DI unsigned pk(float a, float b) { b2 r = {(__bf16)a, (__bf16)b}; return __builtin_bit_cast(unsigned, r); }
DI float bflo(unsigned u) { return __builtin_bit_cast(float, u << 16); }
DI float bfhi(unsigned u) { return __builtin_bit_cast(float, u & 0xffff0000u); }
DI unsigned fbits(float f) { return __builtin_bit_cast(unsigned, f); }
DI float bitsf(unsigned u) { return __builtin_bit_cast(float, u); }
DI f4 mfma32(v4u a, v4u b, f4 c) { return __builtin_amdgcn_mfma_f32_16x16x32_bf16(__builtin_bit_cast(b8, a), __builtin_bit_cast(b8, b), c, 0, 0, 0); }
DI f4 mfma16(v2u a, v2u b, f4 c) { return __builtin_amdgcn_mfma_f32_16x16x16bf16_1k(__builtin_bit_cast(s4, a), __builtin_bit_cast(s4, b), c, 0, 0, 0); }
DI v2u trd(LAS char* L, unsigned a) { return __builtin_bit_cast(v2u, __builtin_amdgcn_ds_read_tr16_b64_v4i16((LAS s4*)(L + a))); }
DI v4u trd2(LAS char* L, unsigned a0, unsigned a1) { v2u x = trd(L, a0), y = trd(L, a1); return (v4u){x.x, x.y, y.x, y.y}; }
DI v4u ld128(LAS char* L, unsigned a) { return *(LAS v4u*)(L + a); }
DI f4 ldf4(LAS char* L, unsigned a) { return *(LAS f4*)(L + a); }
DI f2 ldf2(LAS char* L, unsigned a) { return *(LAS f2*)(L + a); }
DI v2u ld64(LAS char* L, unsigned a) { return *(LAS v2u*)(L + a); }

template <int CTRL> DI float dpp(float v) { return bitsf((unsigned)__builtin_amdgcn_update_dpp(0, (int)fbits(v), CTRL, 0xf, 0xf, true)); }
DI void sw32(float& a, float& b) { auto r = __builtin_amdgcn_permlane32_swap(fbits(a), fbits(b), false, false); a = bitsf((unsigned)r[0]); b = bitsf((unsigned)r[1]); }
DI void sw16(float& a, float& b) { auto r = __builtin_amdgcn_permlane16_swap(fbits(a), fbits(b), false, false); a = bitsf((unsigned)r[0]); b = bitsf((unsigned)r[1]); }
DI float xsum(float v) { float a = v, b = v; sw16(a, b); v = a + b; a = v; b = v; sw32(a, b); return a + b; }
DI float rowsum16(float v) { v += dpp<0x128>(v); v += dpp<0x124>(v); v += dpp<0x122>(v); v += dpp<0x121>(v); return v; }
DI float wavesum(float v) { return xsum(rowsum16(v)); }

constexpr float LOG2E = 1.4426950408889634f;
DI float vmax(float a, float b) { return __builtin_elementwise_maximum(a, b); }
DI float vmin(float a, float b) { return __builtin_elementwise_minimum(a, b); }
template <class OP> DI float fold4(float a0, float a1, float a2, float a3, OP op) {
  sw32(a0, a1); sw32(a2, a3);
  float m01 = op(a0, a1), m23 = op(a2, a3);
  sw16(m01, m23);
  return op(m01, m23);
}
DI void unfold4(float m, float (&o)[4]) {
  float a = m, b = m;
  sw16(a, b);
  float a2 = a, b2 = b;
  sw32(a, a2); sw32(b, b2);
  o[0] = a; o[1] = a2; o[2] = b; o[3] = b2;
}
struct Feat { float a[4], b[4]; f2 ep[4][2], en[4][2]; };
DI void tie8(f2 (&a)[4][2]) { asm volatile("" : "+v"(a[0][0]), "+v"(a[0][1]), "+v"(a[1][0]), "+v"(a[1][1]), "+v"(a[2][0]), "+v"(a[2][1]), "+v"(a[3][0]), "+v"(a[3][1])); }
DI void feat_max(const f4 (&p)[4], Feat& f) {
  float mx[4], mn[4];
#pragma unroll
  for (int t = 0; t < 4; ++t) {
    mx[t] = vmax(vmax(vmax(p[t][0], p[t][1]), p[t][2]), p[t][3]);
    mn[t] = vmin(vmin(vmin(p[t][0], p[t][1]), p[t][2]), p[t][3]);
  }
  const float MX = fold4(mx[0], mx[1], mx[2], mx[3], [](float a, float b) { return vmax(a, b); }) * -LOG2E;
  const float MN = fold4(mn[0], mn[1], mn[2], mn[3], [](float a, float b) { return vmin(a, b); }) * LOG2E;
  unfold4(MX, f.a);
  unfold4(MN, f.b);
}
DI void feat_exp(const f4 (&p)[4], Feat& f) {
#pragma unroll
  for (int t = 0; t < 4; ++t)
#pragma unroll
    for (int h = 0; h < 2; ++h) {
      const f2 v = {p[t][2 * h], p[t][2 * h + 1]};
      f.ep[t][h] = __builtin_elementwise_fma(v, (f2){LOG2E, LOG2E}, (f2){f.a[t], f.a[t]});
      f.en[t][h] = __builtin_elementwise_fma(v, (f2){-LOG2E, -LOG2E}, (f2){f.b[t], f.b[t]});
    }
  tie8(f.ep); tie8(f.en);
#pragma unroll
  for (int t = 0; t < 4; ++t)
#pragma unroll
    for (int h = 0; h < 2; ++h) {
      f.ep[t][h] = (f2){__builtin_amdgcn_exp2f(f.ep[t][h].x), __builtin_amdgcn_exp2f(f.ep[t][h].y)};
      f.en[t][h] = (f2){__builtin_amdgcn_exp2f(f.en[t][h].x), __builtin_amdgcn_exp2f(f.en[t][h].y)};
    }
  tie8(f.ep); tie8(f.en);
}
DI void feat_norm(const Feat& f, v2u (&pos)[4], v2u (&neg)[4]) {
  float sp[4], sn[4];
#pragma unroll
  for (int t = 0; t < 4; ++t) {
    const f2 a = f.ep[t][0] + f.ep[t][1], b = f.en[t][0] + f.en[t][1];
    sp[t] = a.x + a.y;
    sn[t] = b.x + b.y;
  }
  const float IP = __builtin_amdgcn_rcpf(fold4(sp[0], sp[1], sp[2], sp[3], [](float a_, float b_) { return a_ + b_; }));
  const float IN = __builtin_amdgcn_rcpf(fold4(sn[0], sn[1], sn[2], sn[3], [](float a_, float b_) { return a_ + b_; }));
  float ip[4], in[4];
  unfold4(IP, ip);
  unfold4(IN, in);
#pragma unroll
  for (int t = 0; t < 4; ++t) {
    const f2 a0 = f.ep[t][0] * ip[t], a1 = f.ep[t][1] * ip[t], b0 = f.en[t][0] * in[t], b1 = f.en[t][1] * in[t];
    pos[t] = (v2u){pk(a0.x, a0.y), pk(a1.x, a1.y)};
    neg[t] = (v2u){pk(b0.x, b0.y), pk(b1.x, b1.y)};
  }
}

template <unsigned OFF> DI unsigned win(unsigned base) {
  if constexpr (OFF >= 65536u - 16384u) { unsigned b = base + (OFF & ~16383u); asm volatile("" : "+v"(b)); return b; }
  else return base;
}
template <unsigned OFF> constexpr unsigned rem() { return OFF >= 65536u - 16384u ? (OFF & 16383u) : OFF; }
constexpr unsigned HIWIN = 49152;
#define PRIO(k) do { } while (0)
#define PRIO_OFF() do { } while (0)
#define AUX_SC1 16
constexpr unsigned SPIN_CAP = 1u << 16;

struct Ctx {
  LAS char* L;
  unsigned lbase;
  const char *x, *wo, *w2;
  char* ws;
  __amdgpu_buffer_rsrc_t rs;
  unsigned tag;
  int tid, lane, w, m, nh;
  int rg, q, jj, hq, isQ, rt, ct;
  const char *u0, *u1, *u2, *ush;
  unsigned ld1, ld2;
  unsigned vX0, vX1, vW0, vW1, vP0, vP1, vF0, vF1;
  const char *pX, *pW, *pP, *pF1, *pF5, *pWo;
  const char* pH;
  unsigned vH, vH1;
  unsigned ldsw;
};

#define TS(i) do { } while (0)
#define TSX(i) do { } while (0)
#define STAMP(c, i) do { } while (0)

template <int I, int U> DI void half_m0(const Ctx& c) { set_m0(c.ldsw + SC.slot[I] * 16384 + U * 1024); }
template <int I, int U> DI void half_go(const Ctx& c) {
  if constexpr (I < 16 && (I & 1) == 0) {
    glds16_m0<NT_X>(c.pX + (I >> 1) * 128, U ? c.vX1 : c.vX0);
  } else if constexpr (I < 16) {
    glds16_m0<NT_W>(c.pW + (size_t)(64 * (I >> 1)) * c.ld1, U ? c.vW1 : c.vW0);
  } else if constexpr (I == IT_S) {
    const unsigned rr = c.lane >> 1;
    glds16_m0<NT_W>(c.ush + (size_t)(64 * c.w + 32 * U) * 3584, (rr ^ ((rr & 8) >> 1)) * 3584 + (c.lane & 1) * 16);
  } else if constexpr (I < IT_F0) {
    glds16_m0<NT_W>(c.pP + (size_t)(128 * (I - IT_P0)) * c.ld2, U ? c.vP1 : c.vP0);
  } else {
    constexpr int C = I == IT_F0 ? 0 : I - IT_S;
    if constexpr (C < 14) {
      constexpr int d = pos_unit(2 * C + 1) - pos_unit(2 * C);
      static_assert(d == 1 || d == 5, "unit distance inside a chunk");
      glds16_m0<NT_F>((d == 1 ? c.pF1 : c.pF5) + pos_unit(2 * C) * 65536, U ? c.vF1 : c.vF0);
    } else {
      glds16_m0<NT_F>(c.pWo + (C - 14) * 131072, U ? c.vF1 : c.vF0);
    }
  }
}
template <int I, int U> DI void issue_half(const Ctx& c) { half_m0<I, U>(c); asm volatile("s_nop 0"); half_go<I, U>(c); }
template <int ST, int K> DI void op_m0(const Ctx& c) { constexpr int I = nth_item(ST, K >> 1); if constexpr (I >= 0) half_m0<I, (K & 1)>(c); }
template <int ST, int K> DI void op_go(const Ctx& c) { constexpr int I = nth_item(ST, K >> 1); if constexpr (I >= 0) half_go<I, (K & 1)>(c); }
template <int ST, int K> DI void issue_op(const Ctx& c) { constexpr int I = nth_item(ST, K >> 1); if constexpr (I >= 0) issue_half<I, (K & 1)>(c); }
template <int C> DI void issue_h0(const Ctx& c, v4u (&hf)[18][2]) { gld16_sc1(hf[C][0], c.pH + C * 4096, c.vH); }
template <int C> DI void issue_h1(const Ctx& c, v4u (&hf)[18][2]) { gld16_sc1(hf[C][1], c.pH + C * 4096, c.vH1); }
template <int C> DI void issue_h(const Ctx& c, v4u (&hf)[18][2]) { issue_h0<C>(c, hf); issue_h1<C>(c, hf); }

DI void spin_flags(const Ctx& c, const char* line, int n) {
#pragma unroll 1
  for (unsigned it = 0; it < SPIN_CAP; ++it) {
    unsigned v;
    gld4_sc1(v, line, (unsigned)(c.lane & 31) * 4);
    asm volatile("s_waitcnt vmcnt(0)" : "+v"(v)::"memory");
    if (__all(v == c.tag || (c.lane & 31) >= n)) return;
    __builtin_amdgcn_s_sleep(2);
  }
}
DI bool tags_ok(v4u a, unsigned tag) { return a.y == tag && a.w == tag; }

template <int N> DI void tie(f2 (&v)[N]) {
  if constexpr (N == 2) asm volatile("" : "+v"(v[0]), "+v"(v[1]));
  else if constexpr (N == 4) asm volatile("" : "+v"(v[0]), "+v"(v[1]), "+v"(v[2]), "+v"(v[3]));
  else static_assert(N == 2 || N == 4, "tie");
}
template <int N> DI void gelu_n(f2 (&u)[N]) {
  const float c0 = -2.0f * LOG2E * 0.7978845608028654f, c1 = c0 * 0.044715f;
  f2 e[N];
  tie(u);
#pragma unroll
  for (int r = 0; r < N; ++r) e[r] = u[r] * u[r];
#pragma unroll
  for (int r = 0; r < N; ++r) e[r] = __builtin_elementwise_fma(e[r], (f2){c1, c1}, (f2){c0, c0});
#pragma unroll
  for (int r = 0; r < N; ++r) e[r] = u[r] * e[r];
  tie(e);
#pragma unroll
  for (int r = 0; r < N; ++r) e[r] = (f2){__builtin_amdgcn_exp2f(e[r].x), __builtin_amdgcn_exp2f(e[r].y)};
  tie(e);
#pragma unroll
  for (int r = 0; r < N; ++r) e[r] = e[r] + (f2){1.0f, 1.0f};
  tie(e);
#pragma unroll
  for (int r = 0; r < N; ++r) e[r] = (f2){__builtin_amdgcn_rcpf(e[r].x), __builtin_amdgcn_rcpf(e[r].y)};
  tie(e);
#pragma unroll
  for (int r = 0; r < N; ++r) u[r] *= e[r];
}
DI void ln_bias(f2* u, f4 a, f4 cs, v2u bb, f2 st) {
  const f2 b0 = {bflo(bb.x), bfhi(bb.x)}, b1 = {bflo(bb.y), bfhi(bb.y)}, sx = {st.x, st.x}, sy = {-st.y, -st.y};
  u[0] = __builtin_elementwise_fma((f2){a[0], a[1]}, sx, __builtin_elementwise_fma(sy, (f2){cs[0], cs[1]}, b0));
  u[1] = __builtin_elementwise_fma((f2){a[2], a[3]}, sx, __builtin_elementwise_fma(sy, (f2){cs[2], cs[3]}, b1));
}
DI v2u ffn_tile(f4 a, f4 cs, v2u bb, f2 st) {
  f2 u[2];
  ln_bias(u, a, cs, bb, st);
  gelu_n<2>(u);
  return (v2u){pk(u[0].x, u[0].y), pk(u[1].x, u[1].y)};
}
DI v4u ffn_pair(f4 a0, f4 a1, f4 cs0, f4 cs1, v2u bb0, v2u bb1, f2 st) {
  f2 u[4];
  ln_bias(u, a0, cs0, bb0, st);
  ln_bias(u + 2, a1, cs1, bb1, st);
  gelu_n<4>(u);
  return (v4u){pk(u[0].x, u[0].y), pk(u[1].x, u[1].y), pk(u[2].x, u[2].y), pk(u[3].x, u[3].y)};
}
DI v2u ln_pack(f4 a, f4 cs, f2 st) {
  float v[4];
#pragma unroll
  for (int r = 0; r < 4; ++r) v[r] = __builtin_fmaf(a[r], st.x, -st.y * cs[r]);
  return (v2u){pk(v[0], v[1]), pk(v[2], v[3])};
}

__global__ __launch_bounds__(512, 2) void fused_kernel(const char* __restrict__ x, const char* __restrict__ wqkv, const char* __restrict__ wphi,
                                                       const char* __restrict__ wo, const char* __restrict__ w1, const char* __restrict__ b1,
                                                       const char* __restrict__ w2, const char* __restrict__ b2, unsigned short* __restrict__ out,
                                                       char* __restrict__ ws) {
  extern __shared__ __attribute__((aligned(16))) char smem[];
  Ctx c;
  c.L = (LAS char*)smem;
  c.lbase = (unsigned)(size_t)c.L;
  c.x = x; c.wo = wo; c.w2 = w2; c.ws = ws;
  c.tid = threadIdx.x; c.lane = c.tid & 63; c.w = __builtin_amdgcn_readfirstlane(c.tid >> 6); c.m = c.w & 3; c.nh = c.w >> 2;
  const int blk = blockIdx.x, xcd = blk & 7, idx = blk >> 3;
  c.rg = (xcd >> 2) * 8 + (idx >> 2); c.q = xcd & 3; c.jj = idx & 3; c.isQ = c.jj >> 1; c.hq = 2 * c.q + (c.jj & 1);
  c.rt = xcd * 4 + (idx >> 3); c.ct = idx & 7;
  c.rs = __builtin_amdgcn_make_buffer_rsrc((void*)ws, 0, WS_END, 0x00020000);
  int lane = c.lane, g = lane >> 4, l15 = lane & 15, q4 = l15 >> 2, p4 = lane & 3;
#define REFRESH() do { int t_ = threadIdx.x; asm volatile("" : "+v"(t_)); c.tid = t_; lane = t_ & 63; c.lane = lane; g = lane >> 4; l15 = lane & 15; q4 = l15 >> 2; p4 = lane & 3; } while (0)
  LAS char* L = c.L;
  STAMP(c, 0);
  asm volatile("" ::"s"(x), "s"(wqkv), "s"(wphi), "s"(wo), "s"(w1), "s"(b1), "s"(w2), "s"(b2), "s"(out), "s"(ws));
  c.ldsw = c.lbase + L_RING + c.w * 2048;
  c.vX0 = (lane >> 3) * 1024 + (((lane & 7) ^ (lane >> 4)) * 16);
  c.vX1 = (c.vX0 ^ 64u) + 8 * 1024;
  c.pX = x + (size_t)(128 * c.rg + 16 * c.w) * 1024;
  issue_half<0, 0>(c); issue_half<0, 1>(c);
  FENCE();
  const int f0u = 7 * c.q;
  const int fa = f0u + (c.isQ ? 2 + 2 * (c.jj & 1) : c.jj);
  c.u0 = c.isQ ? w1 + fa * 128 : wqkv + (8 + c.hq) * 128;
  c.u1 = c.u0 + (c.isQ ? 128 : 1024);
  c.ld1 = c.isQ ? 3584 : 3072;
  c.vW0 = (lane >> 3) * c.ld1 + (((lane & 7) ^ ((lane >> 3) & 2)) * 16);
  c.vW1 = (c.vW0 ^ 64u) + 8 * c.ld1;
  c.pW = (c.nh ? c.u1 : c.u0) + (size_t)(16 * c.m) * c.ld1;
  issue_half<1, 0>(c); issue_half<1, 1>(c);
  FENCE();
  c.tag = *(const __attribute__((address_space(4))) unsigned*)(ws + WS_EPOCH) + 1u;
  v4u xr, cw0, cw1;
  const int j16 = 4 * c.q + c.jj;
  gld16(xr, x + (size_t)(128 * c.rg + 8 * j16 + c.w) * 1024, lane * 16);
  const int job = (xcd >> 2) * 32 + idx;
  const int jobc = job < 52 ? job : 51;
  const int li = jobc >> 2, kq = jobc & 3;
  const int gunit = li < 6 ? (li >> 1) * 8 + 2 * c.q + (li & 1) : 24 + f0u + (li - 6);
  {
    const char* wb = li < 6 ? wqkv + gunit * 128 : w1 + (gunit - 24) * 128;
    const unsigned ld = li < 6 ? 3072 : 3584;
    const char* bb = wb + (size_t)(128 * kq + 16 * c.w) * ld;
    const unsigned v = (lane >> 3) * ld + (lane & 7) * 16;
    gld16(cw0, bb, v);
    gld16(cw1, bb + 8 * ld, v);
  }
  {
    const char* p = c.w == 0 ? b1 + fa * 128 : c.w == 1 ? b1 + (f0u + 6) * 128 + c.jj * 32 : c.w < 4 ? wphi + c.hq * 2048 + (c.w - 2) * 1024 : b2 + c.ct * 128;
    const unsigned d = c.lbase + (c.w == 0 ? L_B1 : c.w == 1 ? L_B1 + 256 : c.w < 4 ? L_WPHI + (c.w - 2) * 1024 : L_B2);
    if (c.w == 2 || c.w == 3) glds16(p, lane * 16, d);
    else glds4(p, (lane & (c.w == 0 ? 63 : c.w == 1 ? 7 : 31)) * 4, d);
  }
  issue_half<2, 0>(c); issue_half<2, 1>(c);
  FENCE();
  c.u2 = c.isQ ? wqkv + c.hq * 128 : w1 + fa * 128;
  c.ld2 = c.isQ ? 3072 : 3584;
  c.ush = w1 + (f0u + 6) * 128 + c.jj * 32;
  {
    const unsigned id = c.tid, half = (id & 1) * 64, ln = id >> 1;
    const unsigned r = (ln * 205) >> 10, ox = r * 1024 + (3 + ln - 5 * r) * 128;
    const unsigned lj = ln < 240 ? ln - 160 : 79, un = lj >= 40, ow = (lj - 40 * un) * c.ld1 + un * (c.isQ ? 128 : 1024);
    const char* bx = x + (size_t)(128 * c.rg + 32 * c.jj) * 1024;
    const char* bw = c.u0 + (size_t)(192 + 40 * (c.rg & 7)) * c.ld1;
    glds4(c.w < 5 ? bx : bw, (c.w < 5 ? ox : ow) + half, c.lbase + L_DUMMY);
    if (c.w < 3) {
      const unsigned o2 = c.w < 2 ? (id < 112 ? id : 111) * 64 : (lane & 31) * 64;
      glds4(c.w < 2 ? w2 + (size_t)blk * (56 * 128) : wo + (size_t)blk * (16 * 128), o2, c.lbase + L_DUMMY);
    }
  }
  unsigned g1off = 0;
  STAMP(c, 1);
  FENCE();

  v4u xf[2][16];
  f4 acc[2][2][2];
  const f4 z4 = {0.f, 0.f, 0.f, 0.f};
#pragma unroll
  for (int a = 0; a < 2; ++a)
#pragma unroll
    for (int b = 0; b < 2; ++b)
#pragma unroll
      for (int t = 0; t < 2; ++t) acc[a][b][t] = z4;
  const unsigned xa0 = (unsigned)((32 * c.m + l15) * 128 + ((g ^ (l15 >> 1)) * 16));
  const unsigned xtt0 = c.nh * 2048, xtt1 = (1 - c.nh) * 2048;
  const unsigned wa0 = (unsigned)((8 * g + q4) * 128 + (((4 * c.nh + (p4 >> 1)) ^ ((((q4 >> 1) & 1) | ((g & 1) << 1)) << 1)) * 16) + (p4 & 1) * 8);
  unsigned xb[2][4] = {{xa0 + xtt0, xa0 + xtt1, (xa0 ^ 64) + xtt0, (xa0 ^ 64) + xtt1}, {}}, wa[2][2] = {{wa0, wa0 ^ 32}, {}};
#pragma unroll
  for (int i = 0; i < 4; ++i) { xb[1][i] = xb[0][i] + HIWIN; asm volatile("" : "+v"(xb[1][i])); }
#pragma unroll
  for (int i = 0; i < 2; ++i) { wa[1][i] = wa[0][i] + HIWIN; asm volatile("" : "+v"(wa[1][i])); }
  v4u g1a, g1b;
  v4u wfa[2][2], wfb[2][2];
  auto pass1_step = [&](auto KC) {
    constexpr int kc = decltype(KC)::value;
    constexpr unsigned sx = L_RING + SC.slot[2 * kc] * 16384, sw = L_RING + SC.slot[2 * kc + 1] * 16384;
    TS(3 * kc);
    if constexpr (kc > 0)
      asm volatile("" : "+v"(wfb[0][0]), "+v"(wfb[0][1]), "+v"(wfb[1][0]), "+v"(wfb[1][1]), "+v"(xf[0][kc > 0 ? 2 * kc - 1 : 0]), "+v"(xf[1][kc > 0 ? 2 * kc - 1 : 0]));
    if constexpr (kc == 0) {
      wait_top<top_wait(kc)>();
    } else {
      wait_vm<top_wait(kc)>();
    }
    TS(3 * kc + 1);
    bar();
    TS(3 * kc + 2);
    FENCE();
    constexpr int hx = sx >= HIWIN, hw = sw >= HIWIN;
    const unsigned x0 = xb[hx][0], x1 = xb[hx][1], x2 = xb[hx][2], x3 = xb[hx][3], w0 = wa[hw][0], w1_ = wa[hw][1];
    constexpr unsigned rx = sx - hx * HIWIN, rw = sw - hw * HIWIN;
#define P1_MMA(F, KS, UN, I, T) do { if constexpr (DOM_) acc[UN][I][T] = mfma32(F[UN][I], xf[T][KS], acc[UN][I][T]); FENCE(); } while (0)
#define P1_HALF(FR, FM, KSR, KSM, XA, XB, SOFF, G0, G1, DOM, TIE) {                                 \
    constexpr bool DOM_ = DOM;                                                                     \
    v2u t0, t1;                                                                                    \
    xf[0][KSR] = ld128(L, XA + rx); FENCE();                                                       \
    if constexpr (TIE) {                                                                           \
      xf[1][KSR] = ld128(L, XB + rx); FENCE();                                                     \
      t0 = trd(L, w0 + rw + SOFF); FENCE();                                                        \
      asm volatile("" : "+v"(FM[0][0]), "+v"(FM[0][1]), "+v"(FM[1][0]), "+v"(FM[1][1]), "+v"(xf[0][KSM]), "+v"(xf[1][KSM]));   \
      FENCE();                                                                                     \
      P1_MMA(FM, KSM, 0, 0, 0);                                                                    \
    } else {                                                                                       \
      P1_MMA(FM, KSM, 0, 0, 0);                                                                    \
      xf[1][KSR] = ld128(L, XB + rx); FENCE();                                                     \
      t0 = trd(L, w0 + rw + SOFF); FENCE();                                                        \
    }                                                                                              \
    P1_MMA(FM, KSM, 0, 0, 1);                                                                      \
    t1 = trd(L, w0 + rw + SOFF + 512); FR[0][0] = (v4u){t0.x, t0.y, t1.x, t1.y}; FENCE();          \
    op_m0<kc, G0>(c); FENCE();                                                                     \
    P1_MMA(FM, KSM, 0, 1, 0);                                                                      \
    FR[0][1] = trd2(L, w1_ + rw + SOFF, w1_ + rw + SOFF + 512); FENCE();                           \
    P1_MMA(FM, KSM, 0, 1, 1);                                                                      \
    t0 = trd(L, w0 + rw + SOFF + 8192); FENCE();                                                   \
    op_go<kc, G0>(c); FENCE();                                                                     \
    P1_MMA(FM, KSM, 1, 0, 0);                                                                      \
    t1 = trd(L, w0 + rw + SOFF + 8192 + 512); FR[1][0] = (v4u){t0.x, t0.y, t1.x, t1.y}; FENCE();   \
    op_m0<kc, G1>(c); FENCE();                                                                     \
    P1_MMA(FM, KSM, 1, 0, 1);                                                                      \
    FR[1][1] = trd2(L, w1_ + rw + SOFF + 8192, w1_ + rw + SOFF + 8192 + 512); FENCE();             \
    P1_MMA(FM, KSM, 1, 1, 0);                                                                      \
    op_go<kc, G1>(c); FENCE();                                                                     \
    P1_MMA(FM, KSM, 1, 1, 1);                                                                      \
  }
    P1_HALF(wfa, wfb, 2 * kc, (kc > 0 ? 2 * kc - 1 : 0), x0, x1, 0, 0, 1, (kc > 0), false)
    if constexpr (kc == 0) { issue_op<0, 2>(c); issue_op<0, 3>(c); issue_op<0, 4>(c); issue_op<0, 5>(c); FENCE(); }
    if constexpr (kc == 0) {
    {
      asm volatile("s_waitcnt vmcnt(%3)" : "+v"(xr), "+v"(cw0), "+v"(cw1) : "n"(SC.cum[0] - 1 - PRO_REG_LAST + imin(SC.cum[1] - SC.cum[0], 6)) : "memory");
      const float x0v = bitsf(__builtin_amdgcn_readfirstlane(xr.x << 16));
      float d[8] = {bflo(xr.x) - x0v, bfhi(xr.x) - x0v, bflo(xr.y) - x0v, bfhi(xr.y) - x0v, bflo(xr.z) - x0v, bfhi(xr.z) - x0v, bflo(xr.w) - x0v, bfhi(xr.w) - x0v};
      float s = ((d[0] + d[1]) + (d[2] + d[3])) + ((d[4] + d[5]) + (d[6] + d[7]));
      float qq = d[0] * d[0];
  #pragma unroll
      for (int e = 1; e < 8; ++e) qq = __builtin_fmaf(d[e], d[e], qq);
      float cs[8] = {bflo(cw0.x) + bflo(cw1.x), bfhi(cw0.x) + bfhi(cw1.x), bflo(cw0.y) + bflo(cw1.y), bfhi(cw0.y) + bfhi(cw1.y),
                     bflo(cw0.z) + bflo(cw1.z), bfhi(cw0.z) + bfhi(cw1.z), bflo(cw0.w) + bflo(cw1.w), bfhi(cw0.w) + bfhi(cw1.w)};
      s += dpp<0x128>(s); qq += dpp<0x128>(qq);
  #pragma unroll
      for (int e = 0; e < 4; ++e) { sw32(cs[e], cs[e + 4]); cs[e] += cs[e + 4]; }
      s += dpp<0x124>(s); qq += dpp<0x124>(qq);
  #pragma unroll
      for (int e = 0; e < 2; ++e) { sw16(cs[e], cs[e + 2]); cs[e] += cs[e + 2]; }
      s += dpp<0x122>(s); qq += dpp<0x122>(qq);
      cs[0] += dpp<0x128>(cs[0]); cs[1] += dpp<0x128>(cs[1]);
      s += dpp<0x121>(s); qq += dpp<0x121>(qq);
      if ((lane & 8) == 0) *(LAS f2*)(L + L_SCR + c.w * 256 + ((lane & 7) * 8 + (lane >> 5) * 4 + ((lane >> 4) & 1) * 2) * 4) = (f2){cs[0], cs[1]};
      sw32(s, qq);
      float sq = s + qq;
      { float a = sq, b = sq; sw16(a, b); sq = a + b; }
      const float sd = sq * (1.0f / 512.0f);
      const float m2 = bitsf(__builtin_amdgcn_readlane(fbits(sd), 32));
      if (lane == 0) {
        const float var = __builtin_fmaf(-sd, sd, m2);
        const float rstd = __builtin_amdgcn_rsqf(vmax(var, 0.f) + 1e-5f);
        __builtin_amdgcn_raw_buffer_store_b128((v4u){fbits(x0v + sd), c.tag, fbits(rstd), c.tag}, c.rs, WS_STATS + (128 * c.rg + 8 * j16 + c.w) * 16, 0, AUX_SC1);
      }
    }
      FENCE();
    }
    if constexpr (kc == 2) {
      REFRESH();
      const int ufa = 24 + fa;
      const int ua = c.isQ ? ufa : 8 + c.hq, ub = c.isQ ? ufa + 1 : 16 + c.hq, uc = c.isQ ? c.hq : ufa;
      const unsigned base = c.w < 2 ? WS_STATS + (128 * c.rg + 64 * c.w) * 16
                          : WS_CS + (c.w == 2 ? ua * 64 : c.w == 3 ? ub * 64 : c.w == 4 ? uc * 64 : (24 + f0u + 6) * 64 + 16 * c.jj) * 32;
      g1off = base + (c.w < 2 ? lane * 16 : (c.w < 5 ? lane : (lane & 15)) * 32);
      asm volatile("" : "+v"(g1off));
    }
    if constexpr (kc == 4) {
      REFRESH();
      c.vP0 = (lane >> 3) * c.ld2 + (((lane & 7) ^ ((lane >> 3) & 2)) * 16);
      c.vP1 = (c.vP0 ^ 64u) + 8 * c.ld2;
      c.pP = c.u2 + (size_t)(16 * c.w) * c.ld2;
      asm volatile("" : "+v"(c.vP0), "+v"(c.vP1));
    }
    if constexpr (kc == 6) {
      REFRESH();
      c.vF0 = (lane >> 3) * 1024 + (((lane & 7) ^ ((lane >> 3) & 6)) * 16);
      c.vF1 = c.vF0 + 8 * 1024;
      const unsigned o = (16 * c.m) * 1024 + c.ct * 128;
      c.pF1 = w2 + o + c.nh * 65536; c.pF5 = w2 + o + c.nh * 5 * 65536; c.pWo = wo + o + c.nh * 65536;
      asm volatile("" : "+v"(c.vF0), "+v"(c.vF1));
    }
    if constexpr (kc == 1) {
      if (c.w == 7 && job < 52) {
        float s = 0.f;
#pragma unroll
        for (int ww = 0; ww < 8; ++ww) s += *(LAS float*)(L + L_SCR + ww * 256 + lane * 4);
        __builtin_amdgcn_raw_buffer_store_b64((v2u){fbits(s), c.tag}, c.rs, WS_CS + (gunit * 64 + lane) * 32 + kq * 8, 0, AUX_SC1);
      }
      FENCE();
    }
    P1_HALF(wfb, wfa, 2 * kc + 1, 2 * kc, x2, x3, 4096, (kc == 0 ? 6 : 2), (kc == 0 ? 7 : 3), true, true)
    if constexpr (kc == ST_G1_ISSUE) {
      gld16_sc1(g1a, ws, g1off);
      gld16_sc1(g1b, ws + (c.w < 2 ? 0 : 16), g1off);
      FENCE();
    }
  };
  pass1_step(IC<0>{});
  STAMP(c, 2);
  pass1_step(IC<1>{});
  pass1_step(IC<2>{});
  pass1_step(IC<3>{});
  pass1_step(IC<4>{});
  pass1_step(IC<5>{});
  pass1_step(IC<6>{});
  pass1_step(IC<7>{});
  STAMP(c, 3);

  TS(3 * ST_E1);
  asm volatile("" : "+v"(wfb[0][0]), "+v"(wfb[0][1]), "+v"(wfb[1][0]), "+v"(wfb[1][1]), "+v"(xf[0][15]), "+v"(xf[1][15]));
  {
      asm volatile("s_waitcnt vmcnt(%2) lgkmcnt(0)" : "+v"(g1a), "+v"(g1b) : "n"(top_wait(ST_E1)) : "memory");
      REFRESH();
      const bool isst = c.w < 2;
      if (__builtin_expect(!__all(tags_ok(g1a, c.tag) && (isst || tags_ok(g1b, c.tag))), 0)) {
        const unsigned off = g1off;
#pragma unroll 1
        for (unsigned it = 0; it < SPIN_CAP; ++it) {
          __builtin_amdgcn_s_sleep(2);
          gld16_sc1(g1a, ws, off);
          gld16_sc1(g1b, ws + (isst ? 0 : 16), off);
          asm volatile("s_waitcnt vmcnt(0)" : "+v"(g1a), "+v"(g1b)::"memory");
          if (__all(tags_ok(g1a, c.tag) && (isst || tags_ok(g1b, c.tag)))) break;
        }
      }
      if (isst) *(LAS f2*)(L + L_ST + c.tid * 8) = (f2){bitsf(g1a.z), bitsf(g1a.x) * bitsf(g1a.z)};
      else if (c.tid < 128 + 208) *(LAS float*)(L + L_CS + (c.tid - 128) * 4) = (bitsf(g1a.x) + bitsf(g1a.z)) + (bitsf(g1b.x) + bitsf(g1b.z));
      wait_lgkm();
  }
  TS(3 * ST_E1 + 1);
  bar();
  TS(3 * ST_E1 + 2);
  FENCE();
  REFRESH();
  const unsigned gt0 = 8 * c.rg + 2 * c.m + c.nh, gt1 = 8 * c.rg + 2 * c.m + 1 - c.nh;
  {
    f4 cs4[2][2];
#pragma unroll
    for (int un = 0; un < 2; ++un)
#pragma unroll
      for (int i = 0; i < 2; ++i) cs4[un][i] = ldf4(L, L_CS + (64 * un + 32 * c.nh + 16 * i + 4 * g) * 4);
    const f2 st0 = ldf2(L, L_ST + (16 * (2 * c.m + c.nh) + l15) * 8), st1 = ldf2(L, L_ST + (16 * (2 * c.m + 1 - c.nh) + l15) * 8);
    FENCE();
#pragma unroll
    for (int un = 0; un < 2; ++un)
#pragma unroll
      for (int i = 0; i < 2; ++i) {
        acc[un][i][0] = mfma32(wfb[un][i], xf[0][15], acc[un][i][0]);
        acc[un][i][1] = mfma32(wfb[un][i], xf[1][15], acc[un][i][1]);
      }
    FENCE();
    TSX(0);
    if (c.isQ) {
      v2u bb[2][2];
#pragma unroll
      for (int un = 0; un < 2; ++un)
#pragma unroll
        for (int i = 0; i < 2; ++i) bb[un][i] = ld64(L, L_B1 + un * 128 + c.nh * 64 + i * 32 + 8 * g);
      const unsigned ks0 = 2 * unit_pos(c.q, 2 + 2 * (c.jj & 1)) + c.nh;
      const unsigned ho0 = WS_H + (gt0 * 72 + ks0) * 1024 + lane * 16, ho1 = WS_H + (gt1 * 72 + ks0) * 1024 + lane * 16;
      PRIO(1);
      {
        __builtin_amdgcn_raw_buffer_store_b128(ffn_pair(acc[0][0][0], acc[0][1][0], cs4[0][0], cs4[0][1], bb[0][0], bb[0][1], st0), c.rs, ho0, 0, AUX_SC1);
      }
      FENCE(); PRIO(0); issue_op<ST_E1, 0>(c); FENCE();
      {
        __builtin_amdgcn_raw_buffer_store_b128(ffn_pair(acc[0][0][1], acc[0][1][1], cs4[0][0], cs4[0][1], bb[0][0], bb[0][1], st1), c.rs, ho1, 0, AUX_SC1);
      }
      FENCE(); PRIO(1); issue_op<ST_E1, 1>(c); FENCE();
      {
        __builtin_amdgcn_raw_buffer_store_b128(ffn_pair(acc[1][0][0], acc[1][1][0], cs4[1][0], cs4[1][1], bb[1][0], bb[1][1], st0), c.rs, ho0 + 2048, 0, AUX_SC1);
      }
      FENCE(); PRIO(0);
      {
        __builtin_amdgcn_raw_buffer_store_b128(ffn_pair(acc[1][0][1], acc[1][1][1], cs4[1][0], cs4[1][1], bb[1][0], bb[1][1], st1), c.rs, ho1 + 2048, 0, AUX_SC1);
      }
    } else {
      const v4u ones = {0x3f803f80u, 0x3f803f80u, 0x3f803f80u, 0x3f803f80u};
      const v2u wt0 = trd(L, L_WPHI + (2 * c.nh) * 512 + (4 * g + q4) * 32 + p4 * 8), wt1 = trd(L, L_WPHI + (2 * c.nh + 1) * 512 + (4 * g + q4) * 32 + p4 * 8);
      f4 pk4[4];
      v2u vb[4], fp[4], fn[4];
      PRIO(1);
#pragma unroll
      for (int i = 0; i < 2; ++i)
#pragma unroll
        for (int tt = 0; tt < 2; ++tt) pk4[2 * i + tt] = mfma16(i ? wt1 : wt0, ln_pack(acc[0][i][tt], cs4[0][i], tt ? st1 : st0), z4);
#pragma unroll
      for (int i = 0; i < 2; ++i)
#pragma unroll
        for (int tt = 0; tt < 2; ++tt) vb[2 * i + tt] = ln_pack(acc[1][i][tt], cs4[1][i], tt ? st1 : st0);
      TSX(1);
      FENCE(); PRIO(0); issue_op<ST_E1, 0>(c); FENCE();
      Feat ft;
      feat_max(pk4, ft);
      TSX(2);
      FENCE(); PRIO(1); issue_op<ST_E1, 1>(c); FENCE();
      feat_exp(pk4, ft);
      TSX(3);
      FENCE(); PRIO(0); FENCE();
      feat_norm(ft, fp, fn);
      FENCE();
      TSX(4);
      constexpr unsigned IMG_P = L_SCR, IMG_N = L_SCR + 16384, IMG_V = L_RING + SC.slot[15] * 16384;
      const unsigned wo_ = (2 * c.nh) * 4096 + (32 * c.m + l15) * 32 + ((g ^ q4) * 8);
#pragma unroll
      for (int i = 0; i < 2; ++i)
#pragma unroll
        for (int tt = 0; tt < 2; ++tt) {
          *(LAS v2u*)(L + IMG_P + wo_ + i * 4096 + tt * 512) = fp[2 * i + tt];
          *(LAS v2u*)(L + IMG_N + wo_ + i * 4096 + tt * 512) = fn[2 * i + tt];
          *(LAS v2u*)(L + IMG_V + wo_ + i * 4096 + tt * 512) = vb[2 * i + tt];
        }
      wait_lgkm();
      TSX(5);
      bar();
      TSX(6);
      const unsigned ro = (c.w >> 1) * 4096 + (4 * g + q4) * 32 + ((p4 ^ g) * 8);
      const unsigned ra = IMG_P + (c.w & 1) * 16384 + ro, rb = IMG_V + ro;
      v4u A[4], B[4];
#pragma unroll
      for (int j = 0; j < 4; ++j) { A[j] = trd2(L, ra + j * 1024, ra + j * 1024 + 512); B[j] = trd2(L, rb + j * 1024, rb + j * 1024 + 512); }
      f4 kv0 = mfma32(A[0], B[0], z4), kv1 = mfma32(A[1], B[1], z4), z0 = mfma32(A[0], ones, z4), z1 = mfma32(A[1], ones, z4);
      kv0 = mfma32(A[2], B[2], kv0); kv1 = mfma32(A[3], B[3], kv1); z0 = mfma32(A[2], ones, z0); z1 = mfma32(A[3], ones, z1);
      kv0 += kv1; z0 += z1;
      TSX(7);
      const unsigned pbase = WS_P + (c.hq * 16 + c.rg) * PART_BYTES;
      __builtin_amdgcn_raw_buffer_store_b128((v4u){fbits(kv0[0]), c.tag, fbits(kv0[1]), c.tag}, c.rs, pbase + (c.w * 128 + lane) * 16, 0, AUX_SC1);
      __builtin_amdgcn_raw_buffer_store_b128((v4u){fbits(kv0[2]), c.tag, fbits(kv0[3]), c.tag}, c.rs, pbase + (c.w * 128 + 64 + lane) * 16, 0, AUX_SC1);
      if (l15 == 0) {
        __builtin_amdgcn_raw_buffer_store_b128((v4u){fbits(z0[0]), c.tag, fbits(z0[1]), c.tag}, c.rs, pbase + (1024 + c.w * 8 + g * 2) * 16, 0, AUX_SC1);
        __builtin_amdgcn_raw_buffer_store_b128((v4u){fbits(z0[2]), c.tag, fbits(z0[3]), c.tag}, c.rs, pbase + (1024 + c.w * 8 + g * 2 + 1) * 16, 0, AUX_SC1);
      }
    }
  }
  PRIO_OFF();
  TSX(8);
  STAMP(c, 4);
  FENCE();

  f4 acc2[2][2], accS = z4;
#pragma unroll
  for (int b = 0; b < 2; ++b)
#pragma unroll
    for (int t = 0; t < 2; ++t) acc2[b][t] = z4;
  REFRESH();
  const unsigned wb0 = (unsigned)((8 * g + q4) * 128 + (((4 * c.nh + (p4 >> 1)) ^ ((((q4 >> 1) & 1) | ((g & 1) << 1)) << 1)) * 16) + (p4 & 1) * 8);
  const unsigned sa0 = (unsigned)((8 * g + ((g & 1) << 2) + q4) * 32 + p4 * 8);
  constexpr unsigned SS = L_RING + SC.slot[IT_S] * 16384;
  unsigned wbb[2][2] = {{wb0, wb0 ^ 32}, {}};
#pragma unroll
  for (int i = 0; i < 2; ++i) { wbb[1][i] = wbb[0][i] + HIWIN; asm volatile("" : "+v"(wbb[1][i])); }
  unsigned s0 = sa0 + (SS >= HIWIN ? HIWIN : 0), s1 = (sa0 ^ 128) + (SS >= HIWIN ? HIWIN : 0);
  asm volatile("" : "+v"(s0), "+v"(s1));
  unsigned pollv;
  v4u rk0, rk1, rz0, rz1;
  const char* flagA = ws + WS_FLAG + (0 * 16 + (c.rt >> 1)) * 128;
  const char* flagB = ws + WS_FLAG + (1 * 16 + (c.rt >> 1)) * 128;
  const char* flagO = ws + WS_FLAG + (2 * 16 + (c.rt >> 1)) * 128;
  const char* rsrc = ws + WS_P + (c.hq * 16 + 2 * c.w) * PART_BYTES;
  v4u pwa[2], psa, pwb[2], psb;

  auto pass2_step = [&](auto KC) {
    constexpr int kc = decltype(KC)::value, st = 9 + kc;
    constexpr unsigned sp = L_RING + SC.slot[IT_P0 + kc] * 16384, ss = L_RING + SC.slot[IT_S] * 16384;
    TS(3 * st);
    if constexpr (kc > 0) { asm volatile("" : "+v"(pwb[0]), "+v"(pwb[1]), "+v"(psb)); wait_vm<top_wait(st)>(); }
    else wait_top<top_wait(st)>();
    TS(3 * st + 1);
    bar();
    TS(3 * st + 2);
    FENCE();
    constexpr int hp = sp >= HIWIN;
    const unsigned w0 = wbb[hp][0], w1_ = wbb[hp][1];
    constexpr unsigned rp = sp - hp * HIWIN, rs_ = ss - (ss >= HIWIN ? HIWIN : 0);
#define P2_MMA(A, F, T, KM) do { if constexpr (DOM_) A = mfma32(F, xf[T][KM], A); FENCE(); } while (0)
#define P2_Q(FR, SR, KR, FM, SM, KM, DOM, TIE) {                                                    \
    constexpr bool DOM_ = DOM;                                                                     \
    v2u t0, t1;                                                                                    \
    t0 = trd(L, w0 + rp + ((KR) & 3) * 4096); FENCE();                                             \
    if constexpr (TIE) { asm volatile("" : "+v"(FM[0]), "+v"(FM[1]), "+v"(SM)); FENCE(); }         \
    P2_MMA(acc2[0][0], FM[0], 0, KM);                                                              \
    t1 = trd(L, w0 + rp + ((KR) & 3) * 4096 + 512); FR[0] = (v4u){t0.x, t0.y, t1.x, t1.y}; FENCE(); \
    P2_MMA(acc2[0][1], FM[0], 1, KM);                                                              \
    FR[1] = trd2(L, w1_ + rp + ((KR) & 3) * 4096, w1_ + rp + ((KR) & 3) * 4096 + 512); FENCE();    \
    P2_MMA(acc2[1][0], FM[1], 0, KM);                                                              \
    t0 = trd(L, s0 + rs_ + (KR) * 1024); FENCE();                                                  \
    P2_MMA(acc2[1][1], FM[1], 1, KM);                                                              \
    t1 = trd(L, s1 + rs_ + (KR) * 1024); SR = (v4u){t0.x, t0.y, t1.x, t1.y}; FENCE();              \
    P2_MMA(accS, SM, 0, KM);                                                                       \
  }
    if constexpr (st == ST_POLLA_ISSUE) { REFRESH(); gld4_sc1(pollv, flagA, (lane & 31) * 4); }
    if constexpr (st == ST_R_ISSUE) {
      if (c.isQ) {
        REFRESH();
        const unsigned vk = (64 * c.rg + lane) * 16, vz = (1024 + 4 * c.rg + (lane & 3)) * 16;
        gld16_sc1(rk0, rsrc, vk);
        gld16_sc1(rk1, rsrc + PART_BYTES, vk);
        gld16_sc1(rz0, rsrc, vz);
        gld16_sc1(rz1, rsrc + PART_BYTES, vz);
      }
    }
    FENCE();
    op_m0<st, 0>(c);
    P2_Q(pwa, psa, 4 * kc, pwb, psb, (kc > 0 ? 4 * kc - 1 : 0), (kc > 0), false)
    op_go<st, 0>(c);
    if constexpr (st == ST_PUBA) {
      if (c.isQ && threadIdx.x == 0) __builtin_amdgcn_raw_buffer_store_b32(c.tag, c.rs, WS_FLAG + (0 * 16 + c.rg) * 128 + c.hq * 4, 0, AUX_SC1);
    }
    FENCE();
    op_m0<st, 1>(c);
    P2_Q(pwb, psb, 4 * kc + 1, pwa, psa, 4 * kc, true, true)
    op_go<st, 1>(c);
    FENCE();
    P2_Q(pwa, psa, 4 * kc + 2, pwb, psb, 4 * kc + 1, true, true)

    FENCE();
    P2_Q(pwb, psb, 4 * kc + 3, pwa, psa, 4 * kc + 2, true, true)
  };
  pass2_step(IC<0>{});
  pass2_step(IC<1>{});
  pass2_step(IC<2>{});
  pass2_step(IC<3>{});
  STAMP(c, 5);

  v4u hf[18][2];
  v4u rg0, rg1, rgz0, rgz1;
  TS(3 * ST_E2);
  wait_top<top_wait(ST_E2)>();
  asm volatile("s_waitcnt vmcnt(%1)" : "+v"(pollv) : "n"(extra_wait_first(ST_POLLA_ISSUE, ST_E2)) : "memory");
  REFRESH();
  if (c.w == 0) {
    if (__builtin_expect(!__all(pollv == c.tag || (lane & 31) >= 8), 0)) spin_flags(c, flagA, 8);
  }
  if (c.isQ) {
    asm volatile("s_waitcnt vmcnt(%4)" : "+v"(rk0), "+v"(rk1), "+v"(rz0), "+v"(rz1) : "n"(extra_wait_first(ST_R_ISSUE, ST_E2)) : "memory");
    if (__builtin_expect(!__all(tags_ok(rk0, c.tag) && tags_ok(rk1, c.tag) && tags_ok(rz0, c.tag) && tags_ok(rz1, c.tag)), 0)) {
      const unsigned vk = (64 * c.rg + lane) * 16, vz = (1024 + 4 * c.rg + (lane & 3)) * 16;
#pragma unroll 1
      for (unsigned it = 0; it < SPIN_CAP; ++it) {
        __builtin_amdgcn_s_sleep(2);
        gld16_sc1(rk0, rsrc, vk);
        gld16_sc1(rk1, rsrc + PART_BYTES, vk);
        gld16_sc1(rz0, rsrc, vz);
        gld16_sc1(rz1, rsrc + PART_BYTES, vz);
        asm volatile("s_waitcnt vmcnt(0)" : "+v"(rk0), "+v"(rk1), "+v"(rz0), "+v"(rz1)::"memory");
        if (__all(tags_ok(rk0, c.tag) && tags_ok(rk1, c.tag) && tags_ok(rz0, c.tag) && tags_ok(rz1, c.tag))) break;
      }
    }
    *(LAS f2*)(L + L_SCR + c.w * 1024 + lane * 8) = (f2){bitsf(rk0.x) + bitsf(rk1.x), bitsf(rk0.z) + bitsf(rk1.z)};
    if (lane < 4) *(LAS f2*)(L + L_SCR + c.w * 1024 + 512 + lane * 8) = (f2){bitsf(rz0.x) + bitsf(rz1.x), bitsf(rz0.z) + bitsf(rz1.z)};
    wait_lgkm();
  }
  TS(3 * ST_E2 + 1);
  bar();
  TS(3 * ST_E2 + 2);
  FENCE();
  c.pH = ws + WS_H + (size_t)((4 * c.rt + 2 * (c.w & 1)) * 72 + (c.w >> 1)) * 1024;
  c.vH = lane * 16;
  c.vH1 = lane * 16 + 72 * 1024;
  f2 st0, st1;
  f4 cs2[2];
  f4 pq4[4];
  Feat ftq;
  v2u fp[4], fn[4];
  {
#pragma unroll
    for (int i = 0; i < 2; ++i) {
      acc2[i][0] = mfma32(pwb[i], xf[0][15], acc2[i][0]);
      acc2[i][1] = mfma32(pwb[i], xf[1][15], acc2[i][1]);
    }
    accS = mfma32(psb, xf[0][15], accS);
    st0 = ldf2(L, L_ST + (16 * (2 * c.m + c.nh) + l15) * 8); st1 = ldf2(L, L_ST + (16 * (2 * c.m + 1 - c.nh) + l15) * 8);
    cs2[0] = ldf4(L, L_CS + (128 + 32 * c.nh + 4 * g) * 4); cs2[1] = ldf4(L, L_CS + (128 + 32 * c.nh + 16 + 4 * g) * 4);
    const f4 csS = ldf4(L, L_CS + (192 + 4 * g) * 4);
    const v2u bS = ld64(L, L_B1 + 256 + 8 * g);
    FENCE();
    issue_op<ST_E2, 0>(c);
    issue_h<0>(c, hf);
    FENCE();
    TSX(9);
    PRIO(1);
    const v2u bb0 = ld64(L, L_B1 + c.nh * 64 + 8 * g), bb1 = ld64(L, L_B1 + c.nh * 64 + 32 + 8 * g);
    const unsigned ksu = 2 * unit_pos(c.q, c.jj) + c.nh;
    if (c.isQ) {
      if (c.w == 0) {
        f2 s = {0.f, 0.f}, sz = {0.f, 0.f};
#pragma unroll
        for (int ww = 0; ww < 8; ++ww) { s += ldf2(L, L_SCR + ww * 1024 + lane * 8); sz += ldf2(L, L_SCR + ww * 1024 + 512 + (lane & 3) * 8); }
        const unsigned rb = WS_R + c.hq * PART_BYTES;
        __builtin_amdgcn_raw_buffer_store_b128((v4u){fbits(s.x), c.tag, fbits(s.y), c.tag}, c.rs, rb + (64 * c.rg + lane) * 16, 0, AUX_SC1);
        if (lane < 4) __builtin_amdgcn_raw_buffer_store_b128((v4u){fbits(sz.x), c.tag, fbits(sz.y), c.tag}, c.rs, rb + (1024 + 4 * c.rg + lane) * 16, 0, AUX_SC1);
      }
      const v2u wt0 = trd(L, L_WPHI + (2 * c.nh) * 512 + (4 * g + q4) * 32 + p4 * 8), wt1 = trd(L, L_WPHI + (2 * c.nh + 1) * 512 + (4 * g + q4) * 32 + p4 * 8);
#pragma unroll
      for (int i = 0; i < 2; ++i)
#pragma unroll
        for (int tt = 0; tt < 2; ++tt) pq4[2 * i + tt] = mfma16(i ? wt1 : wt0, ln_pack(acc2[i][tt], cs2[i], tt ? st1 : st0), z4);
      feat_max(pq4, ftq);
    } else {
      __builtin_amdgcn_raw_buffer_store_b128(ffn_pair(acc2[0][0], acc2[1][0], cs2[0], cs2[1], bb0, bb1, st0), c.rs, WS_H + (gt0 * 72 + ksu) * 1024 + lane * 16, 0, AUX_SC1);
    }
    FENCE();
    PRIO(0);
    issue_op<ST_E2, 1>(c);
    issue_h<1>(c, hf);
    FENCE();
    if (c.isQ) feat_exp(pq4, ftq);
    else __builtin_amdgcn_raw_buffer_store_b128(ffn_pair(acc2[0][1], acc2[1][1], cs2[0], cs2[1], bb0, bb1, st1), c.rs, WS_H + (gt1 * 72 + ksu) * 1024 + lane * 16, 0, AUX_SC1);
    FENCE();
    TSX(10);
    PRIO(1);
    issue_h<2>(c, hf);
    FENCE();
    {
      const v2u o = ffn_tile(accS, csS, bS, st0);
      const unsigned ks = 2 * unit_pos(c.q, 6) + (c.jj >> 1);
      __builtin_amdgcn_raw_buffer_store_b64(o, c.rs, WS_H + (gt0 * 72 + ks) * 1024 + lane * 16 + (c.jj & 1) * 8, 0, AUX_SC1);
    }
    FENCE();
    TSX(11);
    PRIO(0);
    issue_op<ST_E2, 2>(c);
    issue_h<3>(c, hf);
    issue_op<ST_E2, 3>(c);
    if constexpr (HPRE > 4) issue_h<4>(c, hf);
    FENCE();
    if (c.isQ) feat_norm(ftq, fp, fn);
    FENCE();
    TSX(12);
    if constexpr (HPRE > 5) issue_h<5>(c, hf);
    if constexpr (HPRE > 6) issue_h<6>(c, hf);
    if constexpr (HPRE > 7) issue_h<7>(c, hf);
    if (c.isQ) {
      const char* rres_ = ws + WS_R + c.hq * PART_BYTES;
      const unsigned vk = (c.tid >> 6) * 2048 + (c.tid & 63) * 16, vz = (c.tid & 31) * 32;
      gld16_sc1(rg0, rres_, vk);
      gld16_sc1(rg1, rres_ + 1024, vk);
      gld16_sc1(rgz0, rres_ + 16384, vz);
      gld16_sc1(rgz1, rres_ + 16384 + 16, vz);
    }
    PRIO_OFF();
    TSX(13);
    static_assert(HPRE >= 4 && HPRE <= 8 && ST_RG_ISSUE == ST_E2, "initial h loads");
  }
  STAMP(c, 6);
  FENCE();

  f4 accF[2][4];
#pragma unroll
  for (int j = 0; j < 2; ++j)
#pragma unroll
    for (int t = 0; t < 4; ++t) accF[j][t] = z4;
  REFRESH();
  const unsigned fa0 = (unsigned)((32 * (c.w >> 1) + 4 * g + q4) * 128 + (((p4 >> 1) ^ ((((g & 1) << 1) | (q4 >> 1)) << 1)) * 16) + (p4 & 1) * 8);
  unsigned fb[2][4] = {{fa0, fa0 ^ 32, fa0 ^ 64, fa0 ^ 96}, {}};
#pragma unroll
  for (int i = 0; i < 4; ++i) { fb[1][i] = fb[0][i] + HIWIN; asm volatile("" : "+v"(fb[1][i])); }
  v4u xres, fw[2][4];
  const char* rres = ws + WS_R + c.hq * PART_BYTES;

  auto phase2_step = [&](auto CC) {
    constexpr int ch = decltype(CC)::value, st = ST_F0 + ch;
    constexpr unsigned sf = L_RING + SC.slot[item_of_F(ch)] * 16384;
    TS(3 * st);
    constexpr int pc = ch > 0 ? ch - 1 : 0;
    if constexpr (ch > 0) {
      asm volatile("" : "+v"(fw[pc & 1][0]), "+v"(fw[pc & 1][1]), "+v"(fw[pc & 1][2]), "+v"(fw[pc & 1][3]));
      asm volatile("s_waitcnt vmcnt(%2)" : "+v"(hf[pc][0]), "+v"(hf[pc][1]) : "n"(top_wait(st)) : "memory");
    } else {
      wait_top<top_wait(st)>();
    }
    if constexpr (st == ST_POLLB_CHECK) {
      asm volatile("s_waitcnt vmcnt(%1)" : "+v"(pollv) : "n"(extra_wait_first(ST_POLLB_ISSUE, st)) : "memory");
      if (c.w == 0) { if (__builtin_expect(!__all(pollv == c.tag || (lane & 31) >= 16), 0)) spin_flags(c, flagB, 16); }
    }
    if constexpr (st == ST_POLLO_CHECK) {
      asm volatile("s_waitcnt vmcnt(%1)" : "+v"(pollv) : "n"(extra_wait_first(ST_POLLO_ISSUE, st)) : "memory");
      if (c.w == 0) { if (__builtin_expect(!__all(pollv == c.tag || (lane & 31) >= 8), 0)) spin_flags(c, flagO, 8); }
    }
    if constexpr (st == ST_RG_CHECK) {
      if (c.isQ) {
        asm volatile("s_waitcnt vmcnt(%4)" : "+v"(rg0), "+v"(rg1), "+v"(rgz0), "+v"(rgz1) : "n"(extra_wait(ST_RG_ISSUE, st)) : "memory");
        const unsigned t = c.tid;
        if (__builtin_expect(!__all(tags_ok(rg0, c.tag) && tags_ok(rg1, c.tag) && tags_ok(rgz0, c.tag) && tags_ok(rgz1, c.tag)), 0)) {
#pragma unroll 1
          for (unsigned it = 0; it < SPIN_CAP; ++it) {
            __builtin_amdgcn_s_sleep(2);
            gld16_sc1(rg0, rres, (t >> 6) * 2048 + (t & 63) * 16);
            gld16_sc1(rg1, rres + 1024, (t >> 6) * 2048 + (t & 63) * 16);
            gld16_sc1(rgz0, rres + 16384, (t & 31) * 32);
            gld16_sc1(rgz1, rres + 16384 + 16, (t & 31) * 32);
            asm volatile("s_waitcnt vmcnt(0)" : "+v"(rg0), "+v"(rg1), "+v"(rgz0), "+v"(rgz1)::"memory");
            if (__all(tags_ok(rg0, c.tag) && tags_ok(rg1, c.tag) && tags_ok(rgz0, c.tag) && tags_ok(rgz1, c.tag))) break;
          }
        }
        *(LAS v2u*)(L + L_SCR + (t >> 7) * 1024 + (t & 63) * 16 + ((t >> 6) & 1) * 8) =
            (v2u){pk(bitsf(rg0.x), bitsf(rg0.z)), pk(bitsf(rg1.x), bitsf(rg1.z))};
        if (t < 32)
          *(LAS v2u*)(L + L_SCR + 4096 + (t >> 3) * 64 + (t & 3) * 16 + ((t >> 2) & 1) * 8) =
              (v2u){pk(bitsf(rgz0.x), bitsf(rgz0.z)), pk(bitsf(rgz1.x), bitsf(rgz1.z))};
        wait_lgkm();
      }
    }
    TS(3 * st + 1);
    bar();
    TS(3 * st + 2);
    FENCE();
    constexpr int hi = sf >= HIWIN;
    const unsigned f0 = fb[hi][0], f1 = fb[hi][1], f2_ = fb[hi][2], f3 = fb[hi][3];
    constexpr unsigned rf = sf - hi * HIWIN;
    {
      constexpr int hc = h_at(st, 0), hd = h_at(st, 1);
      static_assert(h_at(st, 2) < 0, "at most two h chunks per step");
#define F_MMA(J, T) do { if constexpr (ch > 0) accF[J][T] = mfma32(fw[pc & 1][T], hf[pc][J], accF[J][T]); FENCE(); } while (0)
      v2u t0, t1;
      t0 = trd(L, f0 + rf); FENCE();
      if constexpr (st == ST_POLLB_ISSUE) gld4_sc1(pollv, flagB, (lane & 31) * 4);
      if constexpr (st == ST_POLLO_ISSUE) gld4_sc1(pollv, flagO, (lane & 31) * 4);
      op_m0<st, 0>(c); FENCE();
      F_MMA(0, 0);
      t1 = trd(L, f0 + rf + 2048); fw[ch & 1][0] = (v4u){t0.x, t0.y, t1.x, t1.y}; FENCE();
      if constexpr (st == ST_PUBB) {
        if (threadIdx.x == 0) __builtin_amdgcn_raw_buffer_store_b32(c.tag, c.rs, WS_FLAG + (1 * 16 + c.rg) * 128 + (4 * c.q + c.jj) * 4, 0, AUX_SC1);
      }
      if constexpr (st == ST_PUBO) {
        if (c.isQ && threadIdx.x == 0) __builtin_amdgcn_raw_buffer_store_b32(c.tag, c.rs, WS_FLAG + (2 * 16 + c.rg) * 128 + c.hq * 4, 0, AUX_SC1);
      }
      op_go<st, 0>(c); FENCE();
      F_MMA(1, 0);
      t0 = trd(L, f1 + rf); FENCE();
      op_m0<st, 1>(c); FENCE();
      F_MMA(0, 1);
      t1 = trd(L, f1 + rf + 2048); fw[ch & 1][1] = (v4u){t0.x, t0.y, t1.x, t1.y}; FENCE();
      op_go<st, 1>(c); FENCE();
      F_MMA(1, 1);
      t0 = trd(L, f2_ + rf); FENCE();
      F_MMA(0, 2);
      t1 = trd(L, f2_ + rf + 2048); fw[ch & 1][2] = (v4u){t0.x, t0.y, t1.x, t1.y}; FENCE();
      if constexpr (hc >= 0) { issue_h0<(hc >= 0 ? hc : 0)>(c, hf); FENCE(); }
      F_MMA(1, 2);
      t0 = trd(L, f3 + rf); FENCE();
      F_MMA(0, 3);
      t1 = trd(L, f3 + rf + 2048); fw[ch & 1][3] = (v4u){t0.x, t0.y, t1.x, t1.y}; FENCE();
      if constexpr (hc >= 0) { issue_h1<(hc >= 0 ? hc : 0)>(c, hf); FENCE(); }
      if constexpr (hd >= 0) { issue_h<(hd >= 0 ? hd : 0)>(c, hf); FENCE(); }
      if constexpr (ch == 14) gld16(xres, x + (size_t)(64 * c.rt) * 1024 + c.ct * 128, (c.tid >> 3) * 1024 + (c.tid & 7) * 16);
      FENCE();
      F_MMA(1, 3);
    }
    if constexpr (st == ST_O) {
      if (c.isQ) {
        v2u ob[2][2];
#pragma unroll
        for (int i = 0; i < 2; ++i) {
          const int hl = 2 * c.nh + i;
          const v4u Akv = ld128(L, L_SCR + hl * 1024 + lane * 16), Az = ld128(L, L_SCR + 4096 + hl * 64 + g * 16);
#pragma unroll
          for (int tt = 0; tt < 2; ++tt) {
            const v4u ph = {fp[2 * i + tt].x, fp[2 * i + tt].y, fn[2 * i + tt].x, fn[2 * i + tt].y};
            const f4 num = mfma32(Akv, ph, z4), den = mfma32(Az, ph, z4);
            const float inv = __builtin_amdgcn_rcpf(den[0] + 1e-6f);
            ob[i][tt] = (v2u){pk(num[0] * inv, num[1] * inv), pk(num[2] * inv, num[3] * inv)};
          }
        }
        const unsigned ks = 56 + 2 * c.hq + c.nh;
        __builtin_amdgcn_raw_buffer_store_b128((v4u){ob[0][0].x, ob[0][0].y, ob[1][0].x, ob[1][0].y}, c.rs, WS_H + (gt0 * 72 + ks) * 1024 + lane * 16, 0, AUX_SC1);
        __builtin_amdgcn_raw_buffer_store_b128((v4u){ob[0][1].x, ob[0][1].y, ob[1][1].x, ob[1][1].y}, c.rs, WS_H + (gt1 * 72 + ks) * 1024 + lane * 16, 0, AUX_SC1);
      }
    }
    FENCE();
  };
  phase2_step(IC<0>{});
  phase2_step(IC<1>{});
  phase2_step(IC<2>{});
  phase2_step(IC<3>{});
  phase2_step(IC<4>{});
  phase2_step(IC<5>{});
  STAMP(c, 7);
  phase2_step(IC<6>{});
  phase2_step(IC<7>{});
  phase2_step(IC<8>{});
  phase2_step(IC<9>{});
  phase2_step(IC<10>{});
  phase2_step(IC<11>{});
  STAMP(c, 8);
  phase2_step(IC<12>{});
  phase2_step(IC<13>{});
  phase2_step(IC<14>{});
  phase2_step(IC<15>{});
  phase2_step(IC<16>{});
  phase2_step(IC<17>{});
  STAMP(c, 9);

  TS(3 * NSTEP);
  asm volatile("" : "+v"(fw[1][0]), "+v"(fw[1][1]), "+v"(fw[1][2]), "+v"(fw[1][3]));
  asm volatile("s_waitcnt vmcnt(0) lgkmcnt(0)" : "+v"(xres), "+v"(hf[17][0]), "+v"(hf[17][1])::"memory");
  TS(3 * NSTEP + 1);
#pragma unroll
  for (int t = 0; t < 4; ++t) { accF[0][t] = mfma32(fw[1][t], hf[17][0], accF[0][t]); accF[1][t] = mfma32(fw[1][t], hf[17][1], accF[1][t]); }
  REFRESH();
  constexpr unsigned FIN = L_RING + 4 * 16384;
  static_assert(SC.slot[NITEM - 1] < 4 && FIN + 4 * 17408 <= L_CS, "final partial sums");
#pragma unroll
  for (int j = 0; j < 2; ++j)
#pragma unroll
    for (int t = 0; t < 4; ++t) *(LAS f4*)(L + FIN + (c.w >> 1) * 17408 + (16 * (2 * (c.w & 1) + j) + l15) * 272 + (16 * t + 4 * g) * 4) = accF[j][t];
  wait_lgkm();
  bar();
  {
    const int tok = c.tid >> 3, cc = c.tid & 7;
    const unsigned a = FIN + tok * 272 + cc * 32;
    f4 s0 = (ldf4(L, a) + ldf4(L, a + 17408)) + (ldf4(L, a + 2 * 17408) + ldf4(L, a + 3 * 17408));
    f4 s1 = (ldf4(L, a + 16) + ldf4(L, a + 16 + 17408)) + (ldf4(L, a + 16 + 2 * 17408) + ldf4(L, a + 16 + 3 * 17408));
    const v4u bb = ld128(L, L_B2 + cc * 16);
    v4u o;
    o.x = pk(s0[0] + bflo(xres.x) + bflo(bb.x), s0[1] + bfhi(xres.x) + bfhi(bb.x));
    o.y = pk(s0[2] + bflo(xres.y) + bflo(bb.y), s0[3] + bfhi(xres.y) + bfhi(bb.y));
    o.z = pk(s1[0] + bflo(xres.z) + bflo(bb.z), s1[1] + bfhi(xres.z) + bfhi(bb.z));
    o.w = pk(s1[2] + bflo(xres.w) + bflo(bb.w), s1[3] + bfhi(xres.w) + bfhi(bb.w));
    { const __amdgpu_buffer_rsrc_t ro = __builtin_amdgcn_make_buffer_rsrc((void*)out, 0, 2048 * 1024, 0x00020000);
      __builtin_amdgcn_raw_buffer_store_b128(o, ro, (64 * c.rt + tok) * 1024 + c.ct * 128 + cc * 16, 0, 2);
    }
  }
  asm volatile(".Lhh_code_end:");
  if (blk == 0 && c.tid == 0) *(unsigned*)(ws + WS_EPOCH) = c.tag;
  STAMP(c, 10);
}

}

extern "C" void kernel_launch(void* const* d_in, const int* in_sizes, int n_in, void* d_out, int out_size, void* d_ws, size_t ws_size,
                              hipStream_t stream) {
  (void)in_sizes; (void)n_in; (void)out_size;
  const size_t WS_OFFSET = 0; (void)ws_size;
  (void)hipFuncSetAttribute((const void*)hh::fused_kernel, hipFuncAttributeMaxDynamicSharedMemorySize, hh::L_END);
  hh::fused_kernel<<<256, 512, hh::L_END, stream>>>((const char*)d_in[0], (const char*)d_in[1], (const char*)d_in[2], (const char*)d_in[3],
                                                    (const char*)d_in[5], (const char*)d_in[6], (const char*)d_in[7], (const char*)d_in[8],
                                                    (unsigned short*)d_out, (char*)d_ws + WS_OFFSET);
}
```

```cpp
#include <hip/hip_runtime.h>
#include <stdint.h>
#include <type_traits>
#define PAGE_TOUCH
#define PT_STRIDE 65536
#define PT_WAVES 1

namespace hh {

#define LAS __attribute__((address_space(3)))
typedef unsigned v4u __attribute__((ext_vector_type(4)));
typedef unsigned v2u __attribute__((ext_vector_type(2)));
typedef float f4 __attribute__((ext_vector_type(4)));
typedef float f2 __attribute__((ext_vector_type(2)));
typedef short s4 __attribute__((ext_vector_type(4)));
typedef __bf16 b8 __attribute__((ext_vector_type(8)));
typedef __bf16 b2 __attribute__((ext_vector_type(2)));
template <int V> using IC = std::integral_constant<int, V>;

constexpr unsigned WS_EPOCH = 0;
constexpr unsigned WS_FLAG = 256;
constexpr unsigned WS_STATS = 8192;
constexpr unsigned WS_CS = WS_STATS + 2048 * 16;
constexpr unsigned PART_BYTES = 17408;
constexpr unsigned WS_P = WS_CS + 3328 * 32;
constexpr unsigned WS_R = WS_P + 8 * 16 * PART_BYTES;
constexpr unsigned WS_H = (WS_R + 8 * PART_BYTES + 4095u) & ~4095u;
constexpr unsigned WS_DBG = WS_H + 128 * 72 * 1024;
constexpr unsigned WS_END = WS_DBG;

constexpr int NSLOT = 7;
constexpr unsigned L_RING = 0;
constexpr unsigned L_SCR = NSLOT * 16384;
constexpr unsigned SCRW = 5248;
constexpr unsigned L_CS = L_SCR + 8 * SCRW;
constexpr unsigned L_ST = L_CS + 1024;
constexpr unsigned L_B1 = L_ST + 1024;
constexpr unsigned L_WPHI = L_B1 + 512;
constexpr unsigned L_B2 = L_WPHI + 2048;
constexpr unsigned L_DUMMY = L_B2 + 256;
constexpr unsigned L_END = L_DUMMY + 256;
static_assert(L_END <= 163840, "LDS");

constexpr int NITEM = 39, NSTEP = 32;
constexpr int ST_E1 = 8, ST_E2 = 13, ST_F0 = 14;
constexpr int IT_P0 = 16, IT_F0 = 20, IT_S = 21;
constexpr int item_of_F(int c) { return c == 0 ? IT_F0 : IT_S + c; }
#define HPRE_N 4
constexpr int HPRE = HPRE_N;
constexpr int DH = 2;
constexpr int ST_G1_ISSUE = 4, ST_G1_CHECK = 8;
#define PUBA_STEP 10
constexpr int ST_PUBA = PUBA_STEP;
constexpr int ST_POLLA_ISSUE = 12;
constexpr int ST_R_ISSUE = 11;
constexpr int ST_PUBB = 15;
constexpr int E2_OPS_AFTER_STORES = 4 + 2 * (HPRE - 4);
constexpr int ST_POLLB_ISSUE = 18, ST_POLLB_CHECK = 20;
constexpr int ST_RG_ISSUE = 13, ST_RG_CHECK = 16;
constexpr int ST_O = 17;
constexpr int ST_PUBO = 20;
constexpr int ST_POLLO_ISSUE = 24, ST_POLLO_CHECK = 26;
static_assert(ST_F0 + 8 - DH == ST_POLLB_CHECK && ST_F0 + 14 - DH == ST_POLLO_CHECK, "flag checks sit in front of the first dependent loads");

struct Sched {
  int slot[NITEM], issue[NITEM], first[NITEM], last[NITEM];
  int cum[NSTEP + 1];
  int pos_item[2][NITEM];
  int pos_h[2][18];
  int h_issue[18];
  int pos_g1[2];
};
constexpr int NPRO = 3;
constexpr int PRO_REG_LAST = 6;
constexpr Sched make_sched() {
  Sched s{};
  for (int i = 0; i < NITEM; ++i) {
    if (i < 16) { s.first[i] = i >> 1; s.last[i] = i >> 1; }
    else if (i < IT_F0) { s.first[i] = 9 + (i - IT_P0); s.last[i] = s.first[i]; }
    else if (i == IT_F0) { s.first[i] = ST_F0; s.last[i] = ST_F0; }
    else if (i == IT_S) { s.first[i] = 9; s.last[i] = 12; }
    else { s.first[i] = ST_F0 + (i - IT_S); s.last[i] = s.first[i]; }
  }
  s.last[15] = ST_E1;
  for (int c = 0; c < 18; ++c)
    s.h_issue[c] = c < HPRE ? ST_E2 : c < 8 ? ST_F0 + c - HPRE : c < 14 ? ST_POLLB_CHECK + (c - 8) / 2 : ST_POLLO_CHECK + (c - 14) / 2;
  int n = 0;
  int next = 0;
  int owner[NSLOT] = {};
  for (int k = 0; k < NSLOT; ++k) owner[k] = -2;
  for (int k = 0; k < NPRO; ++k) {
    s.slot[next] = k; s.issue[next] = -1; owner[k] = next; n += 2; s.pos_item[0][next] = s.pos_item[1][next] = n - 1; ++next;
    if (k == 1) n += 4;
  }
  for (int st = 0; st < NSTEP; ++st) {
    s.cum[st] = n;
    const int first_new = next;
    for (int rep = 0; rep < NSLOT; ++rep) {
      int best = -1;
      for (int k = 0; k < NSLOT; ++k)
        if ((owner[k] == -2 || (owner[k] >= 0 && s.last[owner[k]] < st)) && (best < 0 || owner[k] < owner[best])) best = k;
      if (best < 0) break;
      if (next < NITEM) { s.slot[next] = best; s.issue[next] = st; owner[best] = next; ++next; }
      else owner[best] = -1;
    }
    int nreg = 0;
    for (int c = 0; c < 18; ++c) if (s.h_issue[c] == st) nreg += 2;
    if (st == ST_G1_ISSUE) nreg += 2;
    const int nring = 2 * (next - first_new);
    for (int o = 0; o < 2; ++o) {
      int p = n + (o == 0 ? 0 : nreg);
      for (int i = first_new; i < next; ++i) { p += 2; s.pos_item[o][i] = p - 1; }
      p = n + (o == 0 ? nring : 0);
      for (int c = 0; c < 18; ++c) if (s.h_issue[c] == st) { p += 2; s.pos_h[o][c] = p - 1; }
      if (st == ST_G1_ISSUE) { p += 2; s.pos_g1[o] = p - 1; }
    }
    n += nring + nreg;
  }
  s.cum[NSTEP] = n;
  return s;
}
constexpr Sched SC = make_sched();
constexpr int imin(int a, int b) { return a < b ? a : b; }
constexpr int imax(int a, int b) { return a > b ? a : b; }
constexpr int top_wait(int st) {
  int need = -1;
  for (int o = 0; o < 2; ++o) {
    for (int i = 0; i < NITEM; ++i) if (SC.first[i] == st) need = imax(need, SC.pos_item[o][i]);
    if (st > ST_F0) need = imax(need, SC.pos_h[o][st - ST_F0 - 1]);
    if (st == ST_G1_CHECK) need = imax(need, SC.pos_g1[o]);
  }
  int n = need < 0 ? 63 : SC.cum[st] - 1 - need;
  if (st == 9) n += 2;
  if (st == ST_PUBA) n = imin(n, SC.cum[st] - SC.cum[ST_E1 + 1]);
  if (st == ST_PUBB) n = imin(n, SC.cum[st] - SC.cum[ST_E2 + 1] + E2_OPS_AFTER_STORES);
  if (st == ST_PUBO) n = imin(n, SC.cum[st] - SC.cum[ST_O + 1]);
  return n > 63 ? 63 : n;
}
constexpr int extra_wait(int a, int b) { int n = SC.cum[b] - SC.cum[a + 1]; return n > 63 ? 63 : n; }
constexpr int extra_wait_first(int a, int b) { int n = SC.cum[b] - SC.cum[a]; return n > 63 ? 63 : n; }
constexpr int h_at(int st, int k) {
  for (int c = 0; c < 18; ++c) if (SC.h_issue[c] == st) { if (k == 0) return c; --k; }
  return -1;
}
constexpr int nth_item(int st, int k) {
  for (int i = 0; i < NITEM; ++i) if (SC.issue[i] == st) { if (k == 0) return i; --k; }
  return -1;
}
static_assert(nth_item(0, 4) < 0 && nth_item(ST_E1, 0) == IT_S && nth_item(ST_E1, 1) < 0 && nth_item(ST_E2, 2) < 0 && nth_item(9, 1) < 0 && nth_item(10, 1) < 0 && nth_item(11, 1) < 0 && nth_item(12, 1) < 0, "schedule shape");

__host__ __device__ constexpr int unit_pos(int q, int off) { return off >= 2 && off <= 5 ? 4 * q + (off - 2) : 16 + 3 * q + (off == 6 ? 2 : off); }
__host__ __device__ constexpr int pos_unit(int pos) { return pos < 16 ? 7 * (pos >> 2) + 2 + (pos & 3) : 7 * ((pos - 16) / 3) + (((pos - 16) % 3) == 2 ? 6 : (pos - 16) % 3); }

#define DI __device__ __forceinline__
DI void glds16(const void* sbase, unsigned voff, unsigned lds) {
  asm volatile("s_mov_b32 m0, %2\n\ts_nop 0\n\tglobal_load_lds_dwordx4 %0, %1" ::"v"(voff), "s"(sbase), "s"(lds) : "memory");
}
DI void set_m0(unsigned lds) { asm volatile("s_mov_b32 m0, %0" ::"s"(lds) : "memory"); }
template <bool NT = false> DI void glds16_m0(const void* sbase, unsigned voff) {
  if constexpr (NT) asm volatile("global_load_lds_dwordx4 %0, %1 nt" ::"v"(voff), "s"(sbase) : "memory");
  else asm volatile("global_load_lds_dwordx4 %0, %1" ::"v"(voff), "s"(sbase) : "memory");
}
#define NT_X 0
#define NT_W 0
#define NT_F 0
DI void glds4(const void* sbase, unsigned voff, unsigned lds) {
  asm volatile("s_mov_b32 m0, %2\n\ts_nop 0\n\tglobal_load_lds_dword %0, %1" ::"v"(voff), "s"(sbase), "s"(lds) : "memory");
}
DI void glds4v(const void* vaddr, unsigned lds) {
  asm volatile("s_mov_b32 m0, %1\n\ts_nop 0\n\tglobal_load_lds_dword %0, off" ::"v"(vaddr), "s"(lds) : "memory");
}
DI void gld16(v4u& d, const void* sbase, unsigned voff) {
  asm volatile("global_load_dwordx4 %0, %1, %2" : "=v"(d) : "v"(voff), "s"(sbase) : "memory");
}
DI void gld16_sc1(v4u& d, const void* sbase, unsigned voff) {
  asm volatile("global_load_dwordx4 %0, %1, %2 sc1" : "=v"(d) : "v"(voff), "s"(sbase) : "memory");
}
template <int OFF> DI void gld16_sc1_off(v4u& d, const void* sbase, unsigned voff) {
  asm volatile("global_load_dwordx4 %0, %1, %2 offset:%3 sc1" : "=v"(d) : "v"(voff), "s"(sbase), "n"(OFF) : "memory");
}
DI void gld4_sc1(unsigned& d, const void* sbase, unsigned voff) {
  asm volatile("global_load_dword %0, %1, %2 sc1" : "=v"(d) : "v"(voff), "s"(sbase) : "memory");
}
template <int N> DI void wait_top() { asm volatile("s_waitcnt vmcnt(%0) lgkmcnt(0)" ::"n"(N) : "memory"); }
template <int N> DI void wait_vm() { asm volatile("s_waitcnt vmcnt(%0)" ::"n"(N) : "memory"); }
DI void wait_lgkm() { asm volatile("s_waitcnt lgkmcnt(0)" ::: "memory"); }
DI void bar() { asm volatile("" ::: "memory"); __builtin_amdgcn_s_barrier(); asm volatile("" ::: "memory"); }
#define FENCE() __builtin_amdgcn_sched_barrier(0)

DI unsigned pk(float a, float b) { b2 r = {(__bf16)a, (__bf16)b}; return __builtin_bit_cast(unsigned, r); }
DI float bflo(unsigned u) { return __builtin_bit_cast(float, u << 16); }
DI float bfhi(unsigned u) { return __builtin_bit_cast(float, u & 0xffff0000u); }
DI unsigned fbits(float f) { return __builtin_bit_cast(unsigned, f); }
DI float bitsf(unsigned u) { return __builtin_bit_cast(float, u); }
DI f4 mfma32(v4u a, v4u b, f4 c) { return __builtin_amdgcn_mfma_f32_16x16x32_bf16(__builtin_bit_cast(b8, a), __builtin_bit_cast(b8, b), c, 0, 0, 0); }
DI f4 mfma16(v2u a, v2u b, f4 c) { return __builtin_amdgcn_mfma_f32_16x16x16bf16_1k(__builtin_bit_cast(s4, a), __builtin_bit_cast(s4, b), c, 0, 0, 0); }
DI v2u trd(LAS char* L, unsigned a) { return __builtin_bit_cast(v2u, __builtin_amdgcn_ds_read_tr16_b64_v4i16((LAS s4*)(L + a))); }
DI v4u trd2(LAS char* L, unsigned a0, unsigned a1) { v2u x = trd(L, a0), y = trd(L, a1); return (v4u){x.x, x.y, y.x, y.y}; }
DI v4u ld128(LAS char* L, unsigned a) { return *(LAS v4u*)(L + a); }
DI f4 ldf4(LAS char* L, unsigned a) { return *(LAS f4*)(L + a); }
DI f2 ldf2(LAS char* L, unsigned a) { return *(LAS f2*)(L + a); }
DI v2u ld64(LAS char* L, unsigned a) { return *(LAS v2u*)(L + a); }

template <int CTRL> DI float dpp(float v) { return bitsf((unsigned)__builtin_amdgcn_update_dpp(0, (int)fbits(v), CTRL, 0xf, 0xf, true)); }
DI void sw32(float& a, float& b) { auto r = __builtin_amdgcn_permlane32_swap(fbits(a), fbits(b), false, false); a = bitsf((unsigned)r[0]); b = bitsf((unsigned)r[1]); }
DI void sw16(float& a, float& b) { auto r = __builtin_amdgcn_permlane16_swap(fbits(a), fbits(b), false, false); a = bitsf((unsigned)r[0]); b = bitsf((unsigned)r[1]); }
DI float xsum(float v) { float a = v, b = v; sw16(a, b); v = a + b; a = v; b = v; sw32(a, b); return a + b; }
DI float rowsum16(float v) { v += dpp<0x128>(v); v += dpp<0x124>(v); v += dpp<0x122>(v); v += dpp<0x121>(v); return v; }
DI float wavesum(float v) { return xsum(rowsum16(v)); }

constexpr float LOG2E = 1.4426950408889634f;
DI float vmax(float a, float b) { return __builtin_elementwise_maximum(a, b); }
DI float vmin(float a, float b) { return __builtin_elementwise_minimum(a, b); }
template <class OP> DI float fold4(float a0, float a1, float a2, float a3, OP op) {
  sw32(a0, a1); sw32(a2, a3);
  float m01 = op(a0, a1), m23 = op(a2, a3);
  sw16(m01, m23);
  return op(m01, m23);
}
DI void unfold4(float m, float (&o)[4]) {
  float a = m, b = m;
  sw16(a, b);
  float a2 = a, b2 = b;
  sw32(a, a2); sw32(b, b2);
  o[0] = a; o[1] = a2; o[2] = b; o[3] = b2;
}
struct Feat { float a[4], b[4]; f2 ep[4][2], en[4][2]; };
DI void tie8(f2 (&a)[4][2]) { asm volatile("" : "+v"(a[0][0]), "+v"(a[0][1]), "+v"(a[1][0]), "+v"(a[1][1]), "+v"(a[2][0]), "+v"(a[2][1]), "+v"(a[3][0]), "+v"(a[3][1])); }
DI void feat_max(const f4 (&p)[4], Feat& f) {
  float mx[4], mn[4];
#pragma unroll
  for (int t = 0; t < 4; ++t) {
    mx[t] = vmax(vmax(vmax(p[t][0], p[t][1]), p[t][2]), p[t][3]);
    mn[t] = vmin(vmin(vmin(p[t][0], p[t][1]), p[t][2]), p[t][3]);
  }
  const float MX = fold4(mx[0], mx[1], mx[2], mx[3], [](float a, float b) { return vmax(a, b); }) * -LOG2E;
  const float MN = fold4(mn[0], mn[1], mn[2], mn[3], [](float a, float b) { return vmin(a, b); }) * LOG2E;
  unfold4(MX, f.a);
  unfold4(MN, f.b);
}
DI void feat_exp(const f4 (&p)[4], Feat& f) {
#pragma unroll
  for (int t = 0; t < 4; ++t)
#pragma unroll
    for (int h = 0; h < 2; ++h) {
      const f2 v = {p[t][2 * h], p[t][2 * h + 1]};
      f.ep[t][h] = __builtin_elementwise_fma(v, (f2){LOG2E, LOG2E}, (f2){f.a[t], f.a[t]});
      f.en[t][h] = __builtin_elementwise_fma(v, (f2){-LOG2E, -LOG2E}, (f2){f.b[t], f.b[t]});
    }
  tie8(f.ep); tie8(f.en);
#pragma unroll
  for (int t = 0; t < 4; ++t)
#pragma unroll
    for (int h = 0; h < 2; ++h) {
      f.ep[t][h] = (f2){__builtin_amdgcn_exp2f(f.ep[t][h].x), __builtin_amdgcn_exp2f(f.ep[t][h].y)};
      f.en[t][h] = (f2){__builtin_amdgcn_exp2f(f.en[t][h].x), __builtin_amdgcn_exp2f(f.en[t][h].y)};
    }
  tie8(f.ep); tie8(f.en);
}
DI void feat_norm(const Feat& f, v2u (&pos)[4], v2u (&neg)[4]) {
  float sp[4], sn[4];
#pragma unroll
  for (int t = 0; t < 4; ++t) {
    const f2 a = f.ep[t][0] + f.ep[t][1], b = f.en[t][0] + f.en[t][1];
    sp[t] = a.x + a.y;
    sn[t] = b.x + b.y;
  }
  const float IP = __builtin_amdgcn_rcpf(fold4(sp[0], sp[1], sp[2], sp[3], [](float a_, float b_) { return a_ + b_; }));
  const float IN = __builtin_amdgcn_rcpf(fold4(sn[0], sn[1], sn[2], sn[3], [](float a_, float b_) { return a_ + b_; }));
  float ip[4], in[4];
  unfold4(IP, ip);
  unfold4(IN, in);
#pragma unroll
  for (int t = 0; t < 4; ++t) {
    const f2 a0 = f.ep[t][0] * ip[t], a1 = f.ep[t][1] * ip[t], b0 = f.en[t][0] * in[t], b1 = f.en[t][1] * in[t];
    pos[t] = (v2u){pk(a0.x, a0.y), pk(a1.x, a1.y)};
    neg[t] = (v2u){pk(b0.x, b0.y), pk(b1.x, b1.y)};
  }
}

template <unsigned OFF> DI unsigned win(unsigned base) {
  if constexpr (OFF >= 65536u - 16384u) { unsigned b = base + (OFF & ~16383u); asm volatile("" : "+v"(b)); return b; }
  else return base;
}
template <unsigned OFF> constexpr unsigned rem() { return OFF >= 65536u - 16384u ? (OFF & 16383u) : OFF; }
constexpr unsigned HIWIN = 49152;
#define PRIO(k) do { } while (0)
#define PRIO_OFF() do { } while (0)
#define AUX_SC1 16
constexpr unsigned SPIN_CAP = 1u << 16;

struct Ctx {
  LAS char* L;
  unsigned lbase;
  const char *x, *wo, *w2;
  char* ws;
  __amdgpu_buffer_rsrc_t rs;
  unsigned tag;
  int tid, lane, w, m, nh;
  int rg, q, jj, hq, isQ, rt, ct;
  const char *u0, *u1, *u2, *ush;
  unsigned ld1, ld2;
  unsigned vX0, vX1, vW0, vW1, vP0, vP1, vF0, vF1;
  const char *pX, *pW, *pP, *pF1, *pF5, *pWo;
  const char* pH;
  unsigned vH, vH1;
  unsigned ldsw;
};

#define TS(i) do { } while (0)
#define TSX(i) do { } while (0)
#define STAMP(c, i) do { } while (0)

template <int I, int U> DI void half_m0(const Ctx& c) { set_m0(c.ldsw + SC.slot[I] * 16384 + U * 1024); }
template <int I, int U> DI void half_go(const Ctx& c) {
  if constexpr (I < 16 && (I & 1) == 0) {
    glds16_m0<NT_X>(c.pX + (I >> 1) * 128, U ? c.vX1 : c.vX0);
  } else if constexpr (I < 16) {
    glds16_m0<NT_W>(c.pW + (size_t)(64 * (I >> 1)) * c.ld1, U ? c.vW1 : c.vW0);
  } else if constexpr (I == IT_S) {
    const unsigned rr = c.lane >> 1;
    glds16_m0<NT_W>(c.ush + (size_t)(64 * c.w + 32 * U) * 3584, (rr ^ ((rr & 8) >> 1)) * 3584 + (c.lane & 1) * 16);
  } else if constexpr (I < IT_F0) {
    glds16_m0<NT_W>(c.pP + (size_t)(128 * (I - IT_P0)) * c.ld2, U ? c.vP1 : c.vP0);
  } else {
    constexpr int C = I == IT_F0 ? 0 : I - IT_S;
    if constexpr (C < 14) {
      constexpr int d = pos_unit(2 * C + 1) - pos_unit(2 * C);
      static_assert(d == 1 || d == 5, "unit distance inside a chunk");
      glds16_m0<NT_F>((d == 1 ? c.pF1 : c.pF5) + pos_unit(2 * C) * 65536, U ? c.vF1 : c.vF0);
    } else {
      glds16_m0<NT_F>(c.pWo + (C - 14) * 131072, U ? c.vF1 : c.vF0);
    }
  }
}
template <int I, int U> DI void issue_half(const Ctx& c) { half_m0<I, U>(c); asm volatile("s_nop 0"); half_go<I, U>(c); }
template <int ST, int K> DI void op_m0(const Ctx& c) { constexpr int I = nth_item(ST, K >> 1); if constexpr (I >= 0) half_m0<I, (K & 1)>(c); }
template <int ST, int K> DI void op_go(const Ctx& c) { constexpr int I = nth_item(ST, K >> 1); if constexpr (I >= 0) half_go<I, (K & 1)>(c); }
template <int ST, int K> DI void issue_op(const Ctx& c) { constexpr int I = nth_item(ST, K >> 1); if constexpr (I >= 0) issue_half<I, (K & 1)>(c); }
template <int C> DI void issue_h0(const Ctx& c, v4u (&hf)[18][2]) { gld16_sc1(hf[C][0], c.pH + C * 4096, c.vH); }
template <int C> DI void issue_h1(const Ctx& c, v4u (&hf)[18][2]) { gld16_sc1(hf[C][1], c.pH + C * 4096, c.vH1); }
template <int C> DI void issue_h(const Ctx& c, v4u (&hf)[18][2]) { issue_h0<C>(c, hf); issue_h1<C>(c, hf); }

DI void spin_flags(const Ctx& c, const char* line, int n) {
#pragma unroll 1
  for (unsigned it = 0; it < SPIN_CAP; ++it) {
    unsigned v;
    gld4_sc1(v, line, (unsigned)(c.lane & 31) * 4);
    asm volatile("s_waitcnt vmcnt(0)" : "+v"(v)::"memory");
    if (__all(v == c.tag || (c.lane & 31) >= n)) return;
    __builtin_amdgcn_s_sleep(2);
  }
}
DI bool tags_ok(v4u a, unsigned tag) { return a.y == tag && a.w == tag; }

template <int N> DI void tie(f2 (&v)[N]) {
  if constexpr (N == 2) asm volatile("" : "+v"(v[0]), "+v"(v[1]));
  else if constexpr (N == 4) asm volatile("" : "+v"(v[0]), "+v"(v[1]), "+v"(v[2]), "+v"(v[3]));
  else static_assert(N == 2 || N == 4, "tie");
}
template <int N> DI void gelu_n(f2 (&u)[N]) {
  const float c0 = -2.0f * LOG2E * 0.7978845608028654f, c1 = c0 * 0.044715f;
  f2 e[N];
  tie(u);
#pragma unroll
  for (int r = 0; r < N; ++r) e[r] = u[r] * u[r];
#pragma unroll
  for (int r = 0; r < N; ++r) e[r] = __builtin_elementwise_fma(e[r], (f2){c1, c1}, (f2){c0, c0});
#pragma unroll
  for (int r = 0; r < N; ++r) e[r] = u[r] * e[r];
  tie(e);
#pragma unroll
  for (int r = 0; r < N; ++r) e[r] = (f2){__builtin_amdgcn_exp2f(e[r].x), __builtin_amdgcn_exp2f(e[r].y)};
  tie(e);
#pragma unroll
  for (int r = 0; r < N; ++r) e[r] = e[r] + (f2){1.0f, 1.0f};
  tie(e);
#pragma unroll
  for (int r = 0; r < N; ++r) e[r] = (f2){__builtin_amdgcn_rcpf(e[r].x), __builtin_amdgcn_rcpf(e[r].y)};
  tie(e);
#pragma unroll
  for (int r = 0; r < N; ++r) u[r] *= e[r];
}
DI void ln_bias(f2* u, f4 a, f4 cs, v2u bb, f2 st) {
  const f2 b0 = {bflo(bb.x), bfhi(bb.x)}, b1 = {bflo(bb.y), bfhi(bb.y)}, sx = {st.x, st.x}, sy = {-st.y, -st.y};
  u[0] = __builtin_elementwise_fma((f2){a[0], a[1]}, sx, __builtin_elementwise_fma(sy, (f2){cs[0], cs[1]}, b0));
  u[1] = __builtin_elementwise_fma((f2){a[2], a[3]}, sx, __builtin_elementwise_fma(sy, (f2){cs[2], cs[3]}, b1));
}
DI v2u ffn_tile(f4 a, f4 cs, v2u bb, f2 st) {
  f2 u[2];
  ln_bias(u, a, cs, bb, st);
  gelu_n<2>(u);
  return (v2u){pk(u[0].x, u[0].y), pk(u[1].x, u[1].y)};
}
DI v4u ffn_pair(f4 a0, f4 a1, f4 cs0, f4 cs1, v2u bb0, v2u bb1, f2 st) {
  f2 u[4];
  ln_bias(u, a0, cs0, bb0, st);
  ln_bias(u + 2, a1, cs1, bb1, st);
  gelu_n<4>(u);
  return (v4u){pk(u[0].x, u[0].y), pk(u[1].x, u[1].y), pk(u[2].x, u[2].y), pk(u[3].x, u[3].y)};
}
DI v2u ln_pack(f4 a, f4 cs, f2 st) {
  float v[4];
#pragma unroll
  for (int r = 0; r < 4; ++r) v[r] = __builtin_fmaf(a[r], st.x, -st.y * cs[r]);
  return (v2u){pk(v[0], v[1]), pk(v[2], v[3])};
}

__global__ __launch_bounds__(512, 2) void fused_kernel(const char* __restrict__ x, const char* __restrict__ wqkv, const char* __restrict__ wphi,
                                                       const char* __restrict__ wo, const char* __restrict__ w1, const char* __restrict__ b1,
                                                       const char* __restrict__ w2, const char* __restrict__ b2, unsigned short* __restrict__ out,
                                                       char* __restrict__ ws) {
  extern __shared__ __attribute__((aligned(16))) char smem[];
  Ctx c;
  c.L = (LAS char*)smem;
  c.lbase = (unsigned)(size_t)c.L;
  c.x = x; c.wo = wo; c.w2 = w2; c.ws = ws;
  c.tid = threadIdx.x; c.lane = c.tid & 63; c.w = __builtin_amdgcn_readfirstlane(c.tid >> 6); c.m = c.w & 3; c.nh = c.w >> 2;
  const int blk = blockIdx.x, xcd = blk & 7, idx = blk >> 3;
  c.rg = (xcd >> 2) * 8 + (idx >> 2); c.q = xcd & 3; c.jj = idx & 3; c.isQ = c.jj >> 1; c.hq = 2 * c.q + (c.jj & 1);
  c.rt = xcd * 4 + (idx >> 3); c.ct = idx & 7;
  c.rs = __builtin_amdgcn_make_buffer_rsrc((void*)ws, 0, WS_END, 0x00020000);
  int lane = c.lane, g = lane >> 4, l15 = lane & 15, q4 = l15 >> 2, p4 = lane & 3;
#define REFRESH() do { int t_ = threadIdx.x; asm volatile("" : "+v"(t_)); c.tid = t_; lane = t_ & 63; c.lane = lane; g = lane >> 4; l15 = lane & 15; q4 = l15 >> 2; p4 = lane & 3; } while (0)
  LAS char* L = c.L;
  STAMP(c, 0);
  asm volatile("" ::"s"(x), "s"(wqkv), "s"(wphi), "s"(wo), "s"(w1), "s"(b1), "s"(w2), "s"(b2), "s"(out), "s"(ws));
  c.ldsw = c.lbase + L_RING + c.w * 2048;
  c.vX0 = (lane >> 3) * 1024 + (((lane & 7) ^ (lane >> 4)) * 16);
  c.vX1 = (c.vX0 ^ 64u) + 8 * 1024;
  c.pX = x + (size_t)(128 * c.rg + 16 * c.w) * 1024;
  issue_half<0, 0>(c); issue_half<0, 1>(c);
  FENCE();
  if (c.w >= 8 - PT_WAVES) {
    unsigned p = (unsigned)((idx * PT_WAVES + (c.w - (8 - PT_WAVES))) * 64 + lane);
    constexpr unsigned G = PT_STRIDE;
    constexpr unsigned n0 = (2097152 + G - 1) / G, n1 = (1572864 + G - 1) / G, n2 = (1835008 + G - 1) / G, n3 = n2, n4 = (524288 + G - 1) / G, n5 = n0, n6 = (WS_DBG + G - 1) / G;
    const char* a = x;
    unsigned q = p;
    if (q >= n0) { q -= n0; a = wqkv;
      if (q >= n1) { q -= n1; a = w1;
        if (q >= n2) { q -= n2; a = w2;
          if (q >= n3) { q -= n3; a = wo;
            if (q >= n4) { q -= n4; a = (const char*)out;
              if (q >= n5) { q -= n5; a = ws;
                if (q >= n6) { q -= n6; a = q == 0 ? wphi : q == 1 ? b1 : q == 2 ? b2 : x; q = 0; } } } } } } }
    glds4v(a + (size_t)q * G, c.lbase + L_DUMMY);
  }
  FENCE();
  const int f0u = 7 * c.q;
  const int fa = f0u + (c.isQ ? 2 + 2 * (c.jj & 1) : c.jj);
  c.u0 = c.isQ ? w1 + fa * 128 : wqkv + (8 + c.hq) * 128;
  c.u1 = c.u0 + (c.isQ ? 128 : 1024);
  c.ld1 = c.isQ ? 3584 : 3072;
  c.vW0 = (lane >> 3) * c.ld1 + (((lane & 7) ^ ((lane >> 3) & 2)) * 16);
  c.vW1 = (c.vW0 ^ 64u) + 8 * c.ld1;
  c.pW = (c.nh ? c.u1 : c.u0) + (size_t)(16 * c.m) * c.ld1;
  issue_half<1, 0>(c); issue_half<1, 1>(c);
  FENCE();
  c.tag = *(const __attribute__((address_space(4))) unsigned*)(ws + WS_EPOCH) + 1u;
  v4u xr, cw0, cw1;
  const int j16 = 4 * c.q + c.jj;
  gld16(xr, x + (size_t)(128 * c.rg + 8 * j16 + c.w) * 1024, lane * 16);
  const int job = (xcd >> 2) * 32 + idx;
  const int jobc = job < 52 ? job : 51;
  const int li = jobc >> 2, kq = jobc & 3;
  const int gunit = li < 6 ? (li >> 1) * 8 + 2 * c.q + (li & 1) : 24 + f0u + (li - 6);
  {
    const char* wb = li < 6 ? wqkv + gunit * 128 : w1 + (gunit - 24) * 128;
    const unsigned ld = li < 6 ? 3072 : 3584;
    const char* bb = wb + (size_t)(128 * kq + 16 * c.w) * ld;
    const unsigned v = (lane >> 3) * ld + (lane & 7) * 16;
    gld16(cw0, bb, v);
    gld16(cw1, bb + 8 * ld, v);
  }
  {
    const char* p = c.w == 0 ? b1 + fa * 128 : c.w == 1 ? b1 + (f0u + 6) * 128 + c.jj * 32 : c.w < 4 ? wphi + c.hq * 2048 + (c.w - 2) * 1024 : b2 + c.ct * 128;
    const unsigned d = c.lbase + (c.w == 0 ? L_B1 : c.w == 1 ? L_B1 + 256 : c.w < 4 ? L_WPHI + (c.w - 2) * 1024 : L_B2);
    if (c.w == 2 || c.w == 3) glds16(p, lane * 16, d);
    else glds4(p, (lane & (c.w == 0 ? 63 : c.w == 1 ? 7 : 31)) * 4, d);
  }
  issue_half<2, 0>(c); issue_half<2, 1>(c);
  FENCE();
  c.u2 = c.isQ ? wqkv + c.hq * 128 : w1 + fa * 128;
  c.ld2 = c.isQ ? 3072 : 3584;
  c.ush = w1 + (f0u + 6) * 128 + c.jj * 32;
  {
    const unsigned id = c.tid, half = (id & 1) * 64, ln = id >> 1;
    const unsigned r = (ln * 205) >> 10, ox = r * 1024 + (3 + ln - 5 * r) * 128;
    const unsigned lj = ln < 240 ? ln - 160 : 79, un = lj >= 40, ow = (lj - 40 * un) * c.ld1 + un * (c.isQ ? 128 : 1024);
    const char* bx = x + (size_t)(128 * c.rg + 32 * c.jj) * 1024;
    const char* bw = c.u0 + (size_t)(192 + 40 * (c.rg & 7)) * c.ld1;
    glds4(c.w < 5 ? bx : bw, (c.w < 5 ? ox : ow) + half, c.lbase + L_DUMMY);
    if (c.w < 3) {
      const unsigned o2 = c.w < 2 ? (id < 112 ? id : 111) * 64 : (lane & 31) * 64;
      glds4(c.w < 2 ? w2 + (size_t)blk * (56 * 128) : wo + (size_t)blk * (16 * 128), o2, c.lbase + L_DUMMY);
    }
  }
  unsigned g1off = 0;
  STAMP(c, 1);
  FENCE();

  v4u xf[2][16];
  f4 acc[2][2][2];
  const f4 z4 = {0.f, 0.f, 0.f, 0.f};
#pragma unroll
  for (int a = 0; a < 2; ++a)
#pragma unroll
    for (int b = 0; b < 2; ++b)
#pragma unroll
      for (int t = 0; t < 2; ++t) acc[a][b][t] = z4;
  const unsigned xa0 = (unsigned)((32 * c.m + l15) * 128 + ((g ^ (l15 >> 1)) * 16));
  const unsigned xtt0 = c.nh * 2048, xtt1 = (1 - c.nh) * 2048;
  const unsigned wa0 = (unsigned)((8 * g + q4) * 128 + (((4 * c.nh + (p4 >> 1)) ^ ((((q4 >> 1) & 1) | ((g & 1) << 1)) << 1)) * 16) + (p4 & 1) * 8);
  unsigned xb[2][4] = {{xa0 + xtt0, xa0 + xtt1, (xa0 ^ 64) + xtt0, (xa0 ^ 64) + xtt1}, {}}, wa[2][2] = {{wa0, wa0 ^ 32}, {}};
#pragma unroll
  for (int i = 0; i < 4; ++i) { xb[1][i] = xb[0][i] + HIWIN; asm volatile("" : "+v"(xb[1][i])); }
#pragma unroll
  for (int i = 0; i < 2; ++i) { wa[1][i] = wa[0][i] + HIWIN; asm volatile("" : "+v"(wa[1][i])); }
  v4u g1a, g1b;
  v4u wfa[2][2], wfb[2][2];
  auto pass1_step = [&](auto KC) {
    constexpr int kc = decltype(KC)::value;
    constexpr unsigned sx = L_RING + SC.slot[2 * kc] * 16384, sw = L_RING + SC.slot[2 * kc + 1] * 16384;
    TS(3 * kc);
    if constexpr (kc > 0)
      asm volatile("" : "+v"(wfb[0][0]), "+v"(wfb[0][1]), "+v"(wfb[1][0]), "+v"(wfb[1][1]), "+v"(xf[0][kc > 0 ? 2 * kc - 1 : 0]), "+v"(xf[1][kc > 0 ? 2 * kc - 1 : 0]));
    if constexpr (kc == 0) {
      wait_top<top_wait(kc)>();
    } else {
      wait_vm<top_wait(kc)>();
    }
    TS(3 * kc + 1);
    bar();
    TS(3 * kc + 2);
    FENCE();
    constexpr int hx = sx >= HIWIN, hw = sw >= HIWIN;
    const unsigned x0 = xb[hx][0], x1 = xb[hx][1], x2 = xb[hx][2], x3 = xb[hx][3], w0 = wa[hw][0], w1_ = wa[hw][1];
    constexpr unsigned rx = sx - hx * HIWIN, rw = sw - hw * HIWIN;
#define P1_MMA(F, KS, UN, I, T) do { if constexpr (DOM_) acc[UN][I][T] = mfma32(F[UN][I], xf[T][KS], acc[UN][I][T]); FENCE(); } while (0)
#define P1_HALF(FR, FM, KSR, KSM, XA, XB, SOFF, G0, G1, DOM, TIE) {                                 \
    constexpr bool DOM_ = DOM;                                                                     \
    v2u t0, t1;                                                                                    \
    xf[0][KSR] = ld128(L, XA + rx); FENCE();                                                       \
    if constexpr (TIE) {                                                                           \
      xf[1][KSR] = ld128(L, XB + rx); FENCE();                                                     \
      t0 = trd(L, w0 + rw + SOFF); FENCE();                                                        \
      asm volatile("" : "+v"(FM[0][0]), "+v"(FM[0][1]), "+v"(FM[1][0]), "+v"(FM[1][1]), "+v"(xf[0][KSM]), "+v"(xf[1][KSM]));   \
      FENCE();                                                                                     \
      P1_MMA(FM, KSM, 0, 0, 0);                                                                    \
    } else {                                                                                       \
      P1_MMA(FM, KSM, 0, 0, 0);                                                                    \
      xf[1][KSR] = ld128(L, XB + rx); FENCE();                                                     \
      t0 = trd(L, w0 + rw + SOFF); FENCE();                                                        \
    }                                                                                              \
    P1_MMA(FM, KSM, 0, 0, 1);                                                                      \
    t1 = trd(L, w0 + rw + SOFF + 512); FR[0][0] = (v4u){t0.x, t0.y, t1.x, t1.y}; FENCE();          \
    op_m0<kc, G0>(c); FENCE();                                                                     \
    P1_MMA(FM, KSM, 0, 1, 0);                                                                      \
    FR[0][1] = trd2(L, w1_ + rw + SOFF, w1_ + rw + SOFF + 512); FENCE();                           \
    P1_MMA(FM, KSM, 0, 1, 1);                                                                      \
    t0 = trd(L, w0 + rw + SOFF + 8192); FENCE();                                                   \
    op_go<kc, G0>(c); FENCE();                                                                     \
    P1_MMA(FM, KSM, 1, 0, 0);                                                                      \
    t1 = trd(L, w0 + rw + SOFF + 8192 + 512); FR[1][0] = (v4u){t0.x, t0.y, t1.x, t1.y}; FENCE();   \
    op_m0<kc, G1>(c); FENCE();                                                                     \
    P1_MMA(FM, KSM, 1, 0, 1);                                                                      \
    FR[1][1] = trd2(L, w1_ + rw + SOFF + 8192, w1_ + rw + SOFF + 8192 + 512); FENCE();             \
    P1_MMA(FM, KSM, 1, 1, 0);                                                                      \
    op_go<kc, G1>(c); FENCE();                                                                     \
    P1_MMA(FM, KSM, 1, 1, 1);                                                                      \
  }
    P1_HALF(wfa, wfb, 2 * kc, (kc > 0 ? 2 * kc - 1 : 0), x0, x1, 0, 0, 1, (kc > 0), false)
    if constexpr (kc == 0) { issue_op<0, 2>(c); issue_op<0, 3>(c); issue_op<0, 4>(c); issue_op<0, 5>(c); FENCE(); }
    if constexpr (kc == 0) {
    {
      asm volatile("s_waitcnt vmcnt(%3)" : "+v"(xr), "+v"(cw0), "+v"(cw1) : "n"(SC.cum[0] - 1 - PRO_REG_LAST + imin(SC.cum[1] - SC.cum[0], 6)) : "memory");
      const float x0v = bitsf(__builtin_amdgcn_readfirstlane(xr.x << 16));
      float d[8] = {bflo(xr.x) - x0v, bfhi(xr.x) - x0v, bflo(xr.y) - x0v, bfhi(xr.y) - x0v, bflo(xr.z) - x0v, bfhi(xr.z) - x0v, bflo(xr.w) - x0v, bfhi(xr.w) - x0v};
      float s = ((d[0] + d[1]) + (d[2] + d[3])) + ((d[4] + d[5]) + (d[6] + d[7]));
      float qq = d[0] * d[0];
  #pragma unroll
      for (int e = 1; e < 8; ++e) qq = __builtin_fmaf(d[e], d[e], qq);
      float cs[8] = {bflo(cw0.x) + bflo(cw1.x), bfhi(cw0.x) + bfhi(cw1.x), bflo(cw0.y) + bflo(cw1.y), bfhi(cw0.y) + bfhi(cw1.y),
                     bflo(cw0.z) + bflo(cw1.z), bfhi(cw0.z) + bfhi(cw1.z), bflo(cw0.w) + bflo(cw1.w), bfhi(cw0.w) + bfhi(cw1.w)};
      s += dpp<0x128>(s); qq += dpp<0x128>(qq);
  #pragma unroll
      for (int e = 0; e < 4; ++e) { sw32(cs[e], cs[e + 4]); cs[e] += cs[e + 4]; }
      s += dpp<0x124>(s); qq += dpp<0x124>(qq);
  #pragma unroll
      for (int e = 0; e < 2; ++e) { sw16(cs[e], cs[e + 2]); cs[e] += cs[e + 2]; }
      s += dpp<0x122>(s); qq += dpp<0x122>(qq);
      cs[0] += dpp<0x128>(cs[0]); cs[1] += dpp<0x128>(cs[1]);
      s += dpp<0x121>(s); qq += dpp<0x121>(qq);
      if ((lane & 8) == 0) *(LAS f2*)(L + L_SCR + c.w * 256 + ((lane & 7) * 8 + (lane >> 5) * 4 + ((lane >> 4) & 1) * 2) * 4) = (f2){cs[0], cs[1]};
      sw32(s, qq);
      float sq = s + qq;
      { float a = sq, b = sq; sw16(a, b); sq = a + b; }
      const float sd = sq * (1.0f / 512.0f);
      const float m2 = bitsf(__builtin_amdgcn_readlane(fbits(sd), 32));
      if (lane == 0) {
        const float var = __builtin_fmaf(-sd, sd, m2);
        const float rstd = __builtin_amdgcn_rsqf(vmax(var, 0.f) + 1e-5f);
        __builtin_amdgcn_raw_buffer_store_b128((v4u){fbits(x0v + sd), c.tag, fbits(rstd), c.tag}, c.rs, WS_STATS + (128 * c.rg + 8 * j16 + c.w) * 16, 0, AUX_SC1);
      }
    }
      FENCE();
    }
    if constexpr (kc == 2) {
      REFRESH();
      const int ufa = 24 + fa;
      const int ua = c.isQ ? ufa : 8 + c.hq, ub = c.isQ ? ufa + 1 : 16 + c.hq, uc = c.isQ ? c.hq : ufa;
      const unsigned base = c.w < 2 ? WS_STATS + (128 * c.rg + 64 * c.w) * 16
                          : WS_CS + (c.w == 2 ? ua * 64 : c.w == 3 ? ub * 64 : c.w == 4 ? uc * 64 : (24 + f0u + 6) * 64 + 16 * c.jj) * 32;
      g1off = base + (c.w < 2 ? lane * 16 : (c.w < 5 ? lane : (lane & 15)) * 32);
      asm volatile("" : "+v"(g1off));
    }
    if constexpr (kc == 4) {
      REFRESH();
      c.vP0 = (lane >> 3) * c.ld2 + (((lane & 7) ^ ((lane >> 3) & 2)) * 16);
      c.vP1 = (c.vP0 ^ 64u) + 8 * c.ld2;
      c.pP = c.u2 + (size_t)(16 * c.w) * c.ld2;
      asm volatile("" : "+v"(c.vP0), "+v"(c.vP1));
    }
    if constexpr (kc == 6) {
      REFRESH();
      c.vF0 = (lane >> 3) * 1024 + (((lane & 7) ^ ((lane >> 3) & 6)) * 16);
      c.vF1 = c.vF0 + 8 * 1024;
      const unsigned o = (16 * c.m) * 1024 + c.ct * 128;
      c.pF1 = w2 + o + c.nh * 65536; c.pF5 = w2 + o + c.nh * 5 * 65536; c.pWo = wo + o + c.nh * 65536;
      asm volatile("" : "+v"(c.vF0), "+v"(c.vF1));
    }
    if constexpr (kc == 1) {
      if (c.w == 7 && job < 52) {
        float s = 0.f;
#pragma unroll
        for (int ww = 0; ww < 8; ++ww) s += *(LAS float*)(L + L_SCR + ww * 256 + lane * 4);
        __builtin_amdgcn_raw_buffer_store_b64((v2u){fbits(s), c.tag}, c.rs, WS_CS + (gunit * 64 + lane) * 32 + kq * 8, 0, AUX_SC1);
      }
      FENCE();
    }
    P1_HALF(wfb, wfa, 2 * kc + 1, 2 * kc, x2, x3, 4096, (kc == 0 ? 6 : 2), (kc == 0 ? 7 : 3), true, true)
    if constexpr (kc == ST_G1_ISSUE) {
      gld16_sc1(g1a, ws, g1off);
      gld16_sc1(g1b, ws + (c.w < 2 ? 0 : 16), g1off);
      FENCE();
    }
  };
  pass1_step(IC<0>{});
  STAMP(c, 2);
  pass1_step(IC<1>{});
  pass1_step(IC<2>{});
  pass1_step(IC<3>{});
  pass1_step(IC<4>{});
  pass1_step(IC<5>{});
  pass1_step(IC<6>{});
  pass1_step(IC<7>{});
  STAMP(c, 3);

  TS(3 * ST_E1);
  asm volatile("" : "+v"(wfb[0][0]), "+v"(wfb[0][1]), "+v"(wfb[1][0]), "+v"(wfb[1][1]), "+v"(xf[0][15]), "+v"(xf[1][15]));
  {
      asm volatile("s_waitcnt vmcnt(%2) lgkmcnt(0)" : "+v"(g1a), "+v"(g1b) : "n"(top_wait(ST_E1)) : "memory");
      REFRESH();
      const bool isst = c.w < 2;
      if (__builtin_expect(!__all(tags_ok(g1a, c.tag) && (isst || tags_ok(g1b, c.tag))), 0)) {
        const unsigned off = g1off;
#pragma unroll 1
        for (unsigned it = 0; it < SPIN_CAP; ++it) {
          __builtin_amdgcn_s_sleep(2);
          gld16_sc1(g1a, ws, off);
          gld16_sc1(g1b, ws + (isst ? 0 : 16), off);
          asm volatile("s_waitcnt vmcnt(0)" : "+v"(g1a), "+v"(g1b)::"memory");
          if (__all(tags_ok(g1a, c.tag) && (isst || tags_ok(g1b, c.tag)))) break;
        }
      }
      if (isst) *(LAS f2*)(L + L_ST + c.tid * 8) = (f2){bitsf(g1a.z), bitsf(g1a.x) * bitsf(g1a.z)};
      else if (c.tid < 128 + 208) *(LAS float*)(L + L_CS + (c.tid - 128) * 4) = (bitsf(g1a.x) + bitsf(g1a.z)) + (bitsf(g1b.x) + bitsf(g1b.z));
      wait_lgkm();
  }
  TS(3 * ST_E1 + 1);
  bar();
  TS(3 * ST_E1 + 2);
  FENCE();
  REFRESH();
  const unsigned gt0 = 8 * c.rg + 2 * c.m + c.nh, gt1 = 8 * c.rg + 2 * c.m + 1 - c.nh;
  {
    f4 cs4[2][2];
#pragma unroll
    for (int un = 0; un < 2; ++un)
#pragma unroll
      for (int i = 0; i < 2; ++i) cs4[un][i] = ldf4(L, L_CS + (64 * un + 32 * c.nh + 16 * i + 4 * g) * 4);
    const f2 st0 = ldf2(L, L_ST + (16 * (2 * c.m + c.nh) + l15) * 8), st1 = ldf2(L, L_ST + (16 * (2 * c.m + 1 - c.nh) + l15) * 8);
    FENCE();
#pragma unroll
    for (int un = 0; un < 2; ++un)
#pragma unroll
      for (int i = 0; i < 2; ++i) {
        acc[un][i][0] = mfma32(wfb[un][i], xf[0][15], acc[un][i][0]);
        acc[un][i][1] = mfma32(wfb[un][i], xf[1][15], acc[un][i][1]);
      }
    FENCE();
    TSX(0);
    if (c.isQ) {
      v2u bb[2][2];
#pragma unroll
      for (int un = 0; un < 2; ++un)
#pragma unroll
        for (int i = 0; i < 2; ++i) bb[un][i] = ld64(L, L_B1 + un * 128 + c.nh * 64 + i * 32 + 8 * g);
      const unsigned ks0 = 2 * unit_pos(c.q, 2 + 2 * (c.jj & 1)) + c.nh;
      const unsigned ho0 = WS_H + (gt0 * 72 + ks0) * 1024 + lane * 16, ho1 = WS_H + (gt1 * 72 + ks0) * 1024 + lane * 16;
      PRIO(1);
      {
        __builtin_amdgcn_raw_buffer_store_b128(ffn_pair(acc[0][0][0], acc[0][1][0], cs4[0][0], cs4[0][1], bb[0][0], bb[0][1], st0), c.rs, ho0, 0, AUX_SC1);
      }
      FENCE(); PRIO(0); issue_op<ST_E1, 0>(c); FENCE();
      {
        __builtin_amdgcn_raw_buffer_store_b128(ffn_pair(acc[0][0][1], acc[0][1][1], cs4[0][0], cs4[0][1], bb[0][0], bb[0][1], st1), c.rs, ho1, 0, AUX_SC1);
      }
      FENCE(); PRIO(1); issue_op<ST_E1, 1>(c); FENCE();
      {
        __builtin_amdgcn_raw_buffer_store_b128(ffn_pair(acc[1][0][0], acc[1][1][0], cs4[1][0], cs4[1][1], bb[1][0], bb[1][1], st0), c.rs, ho0 + 2048, 0, AUX_SC1);
      }
      FENCE(); PRIO(0);
      {
        __builtin_amdgcn_raw_buffer_store_b128(ffn_pair(acc[1][0][1], acc[1][1][1], cs4[1][0], cs4[1][1], bb[1][0], bb[1][1], st1), c.rs, ho1 + 2048, 0, AUX_SC1);
      }
    } else {
      const v4u ones = {0x3f803f80u, 0x3f803f80u, 0x3f803f80u, 0x3f803f80u};
      const v2u wt0 = trd(L, L_WPHI + (2 * c.nh) * 512 + (4 * g + q4) * 32 + p4 * 8), wt1 = trd(L, L_WPHI + (2 * c.nh + 1) * 512 + (4 * g + q4) * 32 + p4 * 8);
      f4 pk4[4];
      v2u vb[4], fp[4], fn[4];
      PRIO(1);
#pragma unroll
      for (int i = 0; i < 2; ++i)
#pragma unroll
        for (int tt = 0; tt < 2; ++tt) pk4[2 * i + tt] = mfma16(i ? wt1 : wt0, ln_pack(acc[0][i][tt], cs4[0][i], tt ? st1 : st0), z4);
#pragma unroll
      for (int i = 0; i < 2; ++i)
#pragma unroll
        for (int tt = 0; tt < 2; ++tt) vb[2 * i + tt] = ln_pack(acc[1][i][tt], cs4[1][i], tt ? st1 : st0);
      TSX(1);
      FENCE(); PRIO(0); issue_op<ST_E1, 0>(c); FENCE();
      Feat ft;
      feat_max(pk4, ft);
      TSX(2);
      FENCE(); PRIO(1); issue_op<ST_E1, 1>(c); FENCE();
      feat_exp(pk4, ft);
      TSX(3);
      FENCE(); PRIO(0); FENCE();
      feat_norm(ft, fp, fn);
      FENCE();
      TSX(4);
      constexpr unsigned IMG_P = L_SCR, IMG_N = L_SCR + 16384, IMG_V = L_RING + SC.slot[15] * 16384;
      const unsigned wo_ = (2 * c.nh) * 4096 + (32 * c.m + l15) * 32 + ((g ^ q4) * 8);
#pragma unroll
      for (int i = 0; i < 2; ++i)
#pragma unroll
        for (int tt = 0; tt < 2; ++tt) {
          *(LAS v2u*)(L + IMG_P + wo_ + i * 4096 + tt * 512) = fp[2 * i + tt];
          *(LAS v2u*)(L + IMG_N + wo_ + i * 4096 + tt * 512) = fn[2 * i + tt];
          *(LAS v2u*)(L + IMG_V + wo_ + i * 4096 + tt * 512) = vb[2 * i + tt];
        }
      wait_lgkm();
      TSX(5);
      bar();
      TSX(6);
      const unsigned ro = (c.w >> 1) * 4096 + (4 * g + q4) * 32 + ((p4 ^ g) * 8);
      const unsigned ra = IMG_P + (c.w & 1) * 16384 + ro, rb = IMG_V + ro;
      v4u A[4], B[4];
#pragma unroll
      for (int j = 0; j < 4; ++j) { A[j] = trd2(L, ra + j * 1024, ra + j * 1024 + 512); B[j] = trd2(L, rb + j * 1024, rb + j * 1024 + 512); }
      f4 kv0 = mfma32(A[0], B[0], z4), kv1 = mfma32(A[1], B[1], z4), z0 = mfma32(A[0], ones, z4), z1 = mfma32(A[1], ones, z4);
      kv0 = mfma32(A[2], B[2], kv0); kv1 = mfma32(A[3], B[3], kv1); z0 = mfma32(A[2], ones, z0); z1 = mfma32(A[3], ones, z1);
      kv0 += kv1; z0 += z1;
      TSX(7);
      const unsigned pbase = WS_P + (c.hq * 16 + c.rg) * PART_BYTES;
      __builtin_amdgcn_raw_buffer_store_b128((v4u){fbits(kv0[0]), c.tag, fbits(kv0[1]), c.tag}, c.rs, pbase + (c.w * 128 + lane) * 16, 0, AUX_SC1);
      __builtin_amdgcn_raw_buffer_store_b128((v4u){fbits(kv0[2]), c.tag, fbits(kv0[3]), c.tag}, c.rs, pbase + (c.w * 128 + 64 + lane) * 16, 0, AUX_SC1);
      if (l15 == 0) {
        __builtin_amdgcn_raw_buffer_store_b128((v4u){fbits(z0[0]), c.tag, fbits(z0[1]), c.tag}, c.rs, pbase + (1024 + c.w * 8 + g * 2) * 16, 0, AUX_SC1);
        __builtin_amdgcn_raw_buffer_store_b128((v4u){fbits(z0[2]), c.tag, fbits(z0[3]), c.tag}, c.rs, pbase + (1024 + c.w * 8 + g * 2 + 1) * 16, 0, AUX_SC1);
      }
    }
  }
  PRIO_OFF();
  TSX(8);
  STAMP(c, 4);
  FENCE();

  f4 acc2[2][2], accS = z4;
#pragma unroll
  for (int b = 0; b < 2; ++b)
#pragma unroll
    for (int t = 0; t < 2; ++t) acc2[b][t] = z4;
  REFRESH();
  const unsigned wb0 = (unsigned)((8 * g + q4) * 128 + (((4 * c.nh + (p4 >> 1)) ^ ((((q4 >> 1) & 1) | ((g & 1) << 1)) << 1)) * 16) + (p4 & 1) * 8);
  const unsigned sa0 = (unsigned)((8 * g + ((g & 1) << 2) + q4) * 32 + p4 * 8);
  constexpr unsigned SS = L_RING + SC.slot[IT_S] * 16384;
  unsigned wbb[2][2] = {{wb0, wb0 ^ 32}, {}};
#pragma unroll
  for (int i = 0; i < 2; ++i) { wbb[1][i] = wbb[0][i] + HIWIN; asm volatile("" : "+v"(wbb[1][i])); }
  unsigned s0 = sa0 + (SS >= HIWIN ? HIWIN : 0), s1 = (sa0 ^ 128) + (SS >= HIWIN ? HIWIN : 0);
  asm volatile("" : "+v"(s0), "+v"(s1));
  unsigned pollv;
  v4u rk0, rk1, rz0, rz1;
  const char* flagA = ws + WS_FLAG + (0 * 16 + (c.rt >> 1)) * 128;
  const char* flagB = ws + WS_FLAG + (1 * 16 + (c.rt >> 1)) * 128;
  const char* flagO = ws + WS_FLAG + (2 * 16 + (c.rt >> 1)) * 128;
  const char* rsrc = ws + WS_P + (c.hq * 16 + 2 * c.w) * PART_BYTES;
  v4u pwa[2], psa, pwb[2], psb;

  auto pass2_step = [&](auto KC) {
    constexpr int kc = decltype(KC)::value, st = 9 + kc;
    constexpr unsigned sp = L_RING + SC.slot[IT_P0 + kc] * 16384, ss = L_RING + SC.slot[IT_S] * 16384;
    TS(3 * st);
    if constexpr (kc > 0) { asm volatile("" : "+v"(pwb[0]), "+v"(pwb[1]), "+v"(psb)); wait_vm<top_wait(st)>(); }
    else wait_top<top_wait(st)>();
    TS(3 * st + 1);
    bar();
    TS(3 * st + 2);
    FENCE();
    constexpr int hp = sp >= HIWIN;
    const unsigned w0 = wbb[hp][0], w1_ = wbb[hp][1];
    constexpr unsigned rp = sp - hp * HIWIN, rs_ = ss - (ss >= HIWIN ? HIWIN : 0);
#define P2_MMA(A, F, T, KM) do { if constexpr (DOM_) A = mfma32(F, xf[T][KM], A); FENCE(); } while (0)
#define P2_Q(FR, SR, KR, FM, SM, KM, DOM, TIE) {                                                    \
    constexpr bool DOM_ = DOM;                                                                     \
    v2u t0, t1;                                                                                    \
    t0 = trd(L, w0 + rp + ((KR) & 3) * 4096); FENCE();                                             \
    if constexpr (TIE) { asm volatile("" : "+v"(FM[0]), "+v"(FM[1]), "+v"(SM)); FENCE(); }         \
    P2_MMA(acc2[0][0], FM[0], 0, KM);                                                              \
    t1 = trd(L, w0 + rp + ((KR) & 3) * 4096 + 512); FR[0] = (v4u){t0.x, t0.y, t1.x, t1.y}; FENCE(); \
    P2_MMA(acc2[0][1], FM[0], 1, KM);                                                              \
    FR[1] = trd2(L, w1_ + rp + ((KR) & 3) * 4096, w1_ + rp + ((KR) & 3) * 4096 + 512); FENCE();    \
    P2_MMA(acc2[1][0], FM[1], 0, KM);                                                              \
    t0 = trd(L, s0 + rs_ + (KR) * 1024); FENCE();                                                  \
    P2_MMA(acc2[1][1], FM[1], 1, KM);                                                              \
    t1 = trd(L, s1 + rs_ + (KR) * 1024); SR = (v4u){t0.x, t0.y, t1.x, t1.y}; FENCE();              \
    P2_MMA(accS, SM, 0, KM);                                                                       \
  }
    if constexpr (st == ST_POLLA_ISSUE) { REFRESH(); gld4_sc1(pollv, flagA, (lane & 31) * 4); }
    if constexpr (st == ST_R_ISSUE) {
      if (c.isQ) {
        REFRESH();
        const unsigned vk = (64 * c.rg + lane) * 16, vz = (1024 + 4 * c.rg + (lane & 3)) * 16;
        gld16_sc1(rk0, rsrc, vk);
        gld16_sc1(rk1, rsrc + PART_BYTES, vk);
        gld16_sc1(rz0, rsrc, vz);
        gld16_sc1(rz1, rsrc + PART_BYTES, vz);
      }
    }
    FENCE();
    op_m0<st, 0>(c);
    P2_Q(pwa, psa, 4 * kc, pwb, psb, (kc > 0 ? 4 * kc - 1 : 0), (kc > 0), false)
    op_go<st, 0>(c);
    if constexpr (st == ST_PUBA) {
      if (c.isQ && threadIdx.x == 0) __builtin_amdgcn_raw_buffer_store_b32(c.tag, c.rs, WS_FLAG + (0 * 16 + c.rg) * 128 + c.hq * 4, 0, AUX_SC1);
    }
    FENCE();
    op_m0<st, 1>(c);
    P2_Q(pwb, psb, 4 * kc + 1, pwa, psa, 4 * kc, true, true)
    op_go<st, 1>(c);
    FENCE();
    P2_Q(pwa, psa, 4 * kc + 2, pwb, psb, 4 * kc + 1, true, true)

    FENCE();
    P2_Q(pwb, psb, 4 * kc + 3, pwa, psa, 4 * kc + 2, true, true)
  };
  pass2_step(IC<0>{});
  pass2_step(IC<1>{});
  pass2_step(IC<2>{});
  pass2_step(IC<3>{});
  STAMP(c, 5);

  v4u hf[18][2];
  v4u rg0, rg1, rgz0, rgz1;
  TS(3 * ST_E2);
  wait_top<top_wait(ST_E2)>();
  asm volatile("s_waitcnt vmcnt(%1)" : "+v"(pollv) : "n"(extra_wait_first(ST_POLLA_ISSUE, ST_E2)) : "memory");
  REFRESH();
  if (c.w == 0) {
    if (__builtin_expect(!__all(pollv == c.tag || (lane & 31) >= 8), 0)) spin_flags(c, flagA, 8);
  }
  if (c.isQ) {
    asm volatile("s_waitcnt vmcnt(%4)" : "+v"(rk0), "+v"(rk1), "+v"(rz0), "+v"(rz1) : "n"(extra_wait_first(ST_R_ISSUE, ST_E2)) : "memory");
    if (__builtin_expect(!__all(tags_ok(rk0, c.tag) && tags_ok(rk1, c.tag) && tags_ok(rz0, c.tag) && tags_ok(rz1, c.tag)), 0)) {
      const unsigned vk = (64 * c.rg + lane) * 16, vz = (1024 + 4 * c.rg + (lane & 3)) * 16;
#pragma unroll 1
      for (unsigned it = 0; it < SPIN_CAP; ++it) {
        __builtin_amdgcn_s_sleep(2);
        gld16_sc1(rk0, rsrc, vk);
        gld16_sc1(rk1, rsrc + PART_BYTES, vk);
        gld16_sc1(rz0, rsrc, vz);
        gld16_sc1(rz1, rsrc + PART_BYTES, vz);
        asm volatile("s_waitcnt vmcnt(0)" : "+v"(rk0), "+v"(rk1), "+v"(rz0), "+v"(rz1)::"memory");
        if (__all(tags_ok(rk0, c.tag) && tags_ok(rk1, c.tag) && tags_ok(rz0, c.tag) && tags_ok(rz1, c.tag))) break;
      }
    }
    *(LAS f2*)(L + L_SCR + c.w * 1024 + lane * 8) = (f2){bitsf(rk0.x) + bitsf(rk1.x), bitsf(rk0.z) + bitsf(rk1.z)};
    if (lane < 4) *(LAS f2*)(L + L_SCR + c.w * 1024 + 512 + lane * 8) = (f2){bitsf(rz0.x) + bitsf(rz1.x), bitsf(rz0.z) + bitsf(rz1.z)};
    wait_lgkm();
  }
  TS(3 * ST_E2 + 1);
  bar();
  TS(3 * ST_E2 + 2);
  FENCE();
  c.pH = ws + WS_H + (size_t)((4 * c.rt + 2 * (c.w & 1)) * 72 + (c.w >> 1)) * 1024;
  c.vH = lane * 16;
  c.vH1 = lane * 16 + 72 * 1024;
  f2 st0, st1;
  f4 cs2[2];
  f4 pq4[4];
  Feat ftq;
  v2u fp[4], fn[4];
  {
#pragma unroll
    for (int i = 0; i < 2; ++i) {
      acc2[i][0] = mfma32(pwb[i], xf[0][15], acc2[i][0]);
      acc2[i][1] = mfma32(pwb[i], xf[1][15], acc2[i][1]);
    }
    accS = mfma32(psb, xf[0][15], accS);
    st0 = ldf2(L, L_ST + (16 * (2 * c.m + c.nh) + l15) * 8); st1 = ldf2(L, L_ST + (16 * (2 * c.m + 1 - c.nh) + l15) * 8);
    cs2[0] = ldf4(L, L_CS + (128 + 32 * c.nh + 4 * g) * 4); cs2[1] = ldf4(L, L_CS + (128 + 32 * c.nh + 16 + 4 * g) * 4);
    const f4 csS = ldf4(L, L_CS + (192 + 4 * g) * 4);
    const v2u bS = ld64(L, L_B1 + 256 + 8 * g);
    FENCE();
    issue_op<ST_E2, 0>(c);
    issue_h<0>(c, hf);
    FENCE();
    TSX(9);
    PRIO(1);
    const v2u bb0 = ld64(L, L_B1 + c.nh * 64 + 8 * g), bb1 = ld64(L, L_B1 + c.nh * 64 + 32 + 8 * g);
    const unsigned ksu = 2 * unit_pos(c.q, c.jj) + c.nh;
    if (c.isQ) {
      if (c.w == 0) {
        f2 s = {0.f, 0.f}, sz = {0.f, 0.f};
#pragma unroll
        for (int ww = 0; ww < 8; ++ww) { s += ldf2(L, L_SCR + ww * 1024 + lane * 8); sz += ldf2(L, L_SCR + ww * 1024 + 512 + (lane & 3) * 8); }
        const unsigned rb = WS_R + c.hq * PART_BYTES;
        __builtin_amdgcn_raw_buffer_store_b128((v4u){fbits(s.x), c.tag, fbits(s.y), c.tag}, c.rs, rb + (64 * c.rg + lane) * 16, 0, AUX_SC1);
        if (lane < 4) __builtin_amdgcn_raw_buffer_store_b128((v4u){fbits(sz.x), c.tag, fbits(sz.y), c.tag}, c.rs, rb + (1024 + 4 * c.rg + lane) * 16, 0, AUX_SC1);
      }
      const v2u wt0 = trd(L, L_WPHI + (2 * c.nh) * 512 + (4 * g + q4) * 32 + p4 * 8), wt1 = trd(L, L_WPHI + (2 * c.nh + 1) * 512 + (4 * g + q4) * 32 + p4 * 8);
#pragma unroll
      for (int i = 0; i < 2; ++i)
#pragma unroll
        for (int tt = 0; tt < 2; ++tt) pq4[2 * i + tt] = mfma16(i ? wt1 : wt0, ln_pack(acc2[i][tt], cs2[i], tt ? st1 : st0), z4);
      feat_max(pq4, ftq);
    } else {
      __builtin_amdgcn_raw_buffer_store_b128(ffn_pair(acc2[0][0], acc2[1][0], cs2[0], cs2[1], bb0, bb1, st0), c.rs, WS_H + (gt0 * 72 + ksu) * 1024 + lane * 16, 0, AUX_SC1);
    }
    FENCE();
    PRIO(0);
    issue_op<ST_E2, 1>(c);
    issue_h<1>(c, hf);
    FENCE();
    if (c.isQ) feat_exp(pq4, ftq);
    else __builtin_amdgcn_raw_buffer_store_b128(ffn_pair(acc2[0][1], acc2[1][1], cs2[0], cs2[1], bb0, bb1, st1), c.rs, WS_H + (gt1 * 72 + ksu) * 1024 + lane * 16, 0, AUX_SC1);
    FENCE();
    TSX(10);
    PRIO(1);
    issue_h<2>(c, hf);
    FENCE();
    {
      const v2u o = ffn_tile(accS, csS, bS, st0);
      const unsigned ks = 2 * unit_pos(c.q, 6) + (c.jj >> 1);
      __builtin_amdgcn_raw_buffer_store_b64(o, c.rs, WS_H + (gt0 * 72 + ks) * 1024 + lane * 16 + (c.jj & 1) * 8, 0, AUX_SC1);
    }
    FENCE();
    TSX(11);
    PRIO(0);
    issue_op<ST_E2, 2>(c);
    issue_h<3>(c, hf);
    issue_op<ST_E2, 3>(c);
    if constexpr (HPRE > 4) issue_h<4>(c, hf);
    FENCE();
    if (c.isQ) feat_norm(ftq, fp, fn);
    FENCE();
    TSX(12);
    if constexpr (HPRE > 5) issue_h<5>(c, hf);
    if constexpr (HPRE > 6) issue_h<6>(c, hf);
    if constexpr (HPRE > 7) issue_h<7>(c, hf);
    if (c.isQ) {
      const char* rres_ = ws + WS_R + c.hq * PART_BYTES;
      const unsigned vk = (c.tid >> 6) * 2048 + (c.tid & 63) * 16, vz = (c.tid & 31) * 32;
      gld16_sc1(rg0, rres_, vk);
      gld16_sc1(rg1, rres_ + 1024, vk);
      gld16_sc1(rgz0, rres_ + 16384, vz);
      gld16_sc1(rgz1, rres_ + 16384 + 16, vz);
    }
    PRIO_OFF();
    TSX(13);
    static_assert(HPRE >= 4 && HPRE <= 8 && ST_RG_ISSUE == ST_E2, "initial h loads");
  }
  STAMP(c, 6);
  FENCE();

  f4 accF[2][4];
#pragma unroll
  for (int j = 0; j < 2; ++j)
#pragma unroll
    for (int t = 0; t < 4; ++t) accF[j][t] = z4;
  REFRESH();
  const unsigned fa0 = (unsigned)((32 * (c.w >> 1) + 4 * g + q4) * 128 + (((p4 >> 1) ^ ((((g & 1) << 1) | (q4 >> 1)) << 1)) * 16) + (p4 & 1) * 8);
  unsigned fb[2][4] = {{fa0, fa0 ^ 32, fa0 ^ 64, fa0 ^ 96}, {}};
#pragma unroll
  for (int i = 0; i < 4; ++i) { fb[1][i] = fb[0][i] + HIWIN; asm volatile("" : "+v"(fb[1][i])); }
  v4u xres, fw[2][4];
  const char* rres = ws + WS_R + c.hq * PART_BYTES;

  auto phase2_step = [&](auto CC) {
    constexpr int ch = decltype(CC)::value, st = ST_F0 + ch;
    constexpr unsigned sf = L_RING + SC.slot[item_of_F(ch)] * 16384;
    TS(3 * st);
    constexpr int pc = ch > 0 ? ch - 1 : 0;
    if constexpr (ch > 0) {
      asm volatile("" : "+v"(fw[pc & 1][0]), "+v"(fw[pc & 1][1]), "+v"(fw[pc & 1][2]), "+v"(fw[pc & 1][3]));
      asm volatile("s_waitcnt vmcnt(%2)" : "+v"(hf[pc][0]), "+v"(hf[pc][1]) : "n"(top_wait(st)) : "memory");
    } else {
      wait_top<top_wait(st)>();
    }
    if constexpr (st == ST_POLLB_CHECK) {
      asm volatile("s_waitcnt vmcnt(%1)" : "+v"(pollv) : "n"(extra_wait_first(ST_POLLB_ISSUE, st)) : "memory");
      if (c.w == 0) { if (__builtin_expect(!__all(pollv == c.tag || (lane & 31) >= 16), 0)) spin_flags(c, flagB, 16); }
    }
    if constexpr (st == ST_POLLO_CHECK) {
      asm volatile("s_waitcnt vmcnt(%1)" : "+v"(pollv) : "n"(extra_wait_first(ST_POLLO_ISSUE, st)) : "memory");
      if (c.w == 0) { if (__builtin_expect(!__all(pollv == c.tag || (lane & 31) >= 8), 0)) spin_flags(c, flagO, 8); }
    }
    if constexpr (st == ST_RG_CHECK) {
      if (c.isQ) {
        asm volatile("s_waitcnt vmcnt(%4)" : "+v"(rg0), "+v"(rg1), "+v"(rgz0), "+v"(rgz1) : "n"(extra_wait(ST_RG_ISSUE, st)) : "memory");
        const unsigned t = c.tid;
        if (__builtin_expect(!__all(tags_ok(rg0, c.tag) && tags_ok(rg1, c.tag) && tags_ok(rgz0, c.tag) && tags_ok(rgz1, c.tag)), 0)) {
#pragma unroll 1
          for (unsigned it = 0; it < SPIN_CAP; ++it) {
            __builtin_amdgcn_s_sleep(2);
            gld16_sc1(rg0, rres, (t >> 6) * 2048 + (t & 63) * 16);
            gld16_sc1(rg1, rres + 1024, (t >> 6) * 2048 + (t & 63) * 16);
            gld16_sc1(rgz0, rres + 16384, (t & 31) * 32);
            gld16_sc1(rgz1, rres + 16384 + 16, (t & 31) * 32);
            asm volatile("s_waitcnt vmcnt(0)" : "+v"(rg0), "+v"(rg1), "+v"(rgz0), "+v"(rgz1)::"memory");
            if (__all(tags_ok(rg0, c.tag) && tags_ok(rg1, c.tag) && tags_ok(rgz0, c.tag) && tags_ok(rgz1, c.tag))) break;
          }
        }
        *(LAS v2u*)(L + L_SCR + (t >> 7) * 1024 + (t & 63) * 16 + ((t >> 6) & 1) * 8) =
            (v2u){pk(bitsf(rg0.x), bitsf(rg0.z)), pk(bitsf(rg1.x), bitsf(rg1.z))};
        if (t < 32)
          *(LAS v2u*)(L + L_SCR + 4096 + (t >> 3) * 64 + (t & 3) * 16 + ((t >> 2) & 1) * 8) =
              (v2u){pk(bitsf(rgz0.x), bitsf(rgz0.z)), pk(bitsf(rgz1.x), bitsf(rgz1.z))};
        wait_lgkm();
      }
    }
    TS(3 * st + 1);
    bar();
    TS(3 * st + 2);
    FENCE();
    constexpr int hi = sf >= HIWIN;
    const unsigned f0 = fb[hi][0], f1 = fb[hi][1], f2_ = fb[hi][2], f3 = fb[hi][3];
    constexpr unsigned rf = sf - hi * HIWIN;
    {
      constexpr int hc = h_at(st, 0), hd = h_at(st, 1);
      static_assert(h_at(st, 2) < 0, "at most two h chunks per step");
#define F_MMA(J, T) do { if constexpr (ch > 0) accF[J][T] = mfma32(fw[pc & 1][T], hf[pc][J], accF[J][T]); FENCE(); } while (0)
      v2u t0, t1;
      t0 = trd(L, f0 + rf); FENCE();
      if constexpr (st == ST_POLLB_ISSUE) gld4_sc1(pollv, flagB, (lane & 31) * 4);
      if constexpr (st == ST_POLLO_ISSUE) gld4_sc1(pollv, flagO, (lane & 31) * 4);
      op_m0<st, 0>(c); FENCE();
      F_MMA(0, 0);
      t1 = trd(L, f0 + rf + 2048); fw[ch & 1][0] = (v4u){t0.x, t0.y, t1.x, t1.y}; FENCE();
      if constexpr (st == ST_PUBB) {
        if (threadIdx.x == 0) __builtin_amdgcn_raw_buffer_store_b32(c.tag, c.rs, WS_FLAG + (1 * 16 + c.rg) * 128 + (4 * c.q + c.jj) * 4, 0, AUX_SC1);
      }
      if constexpr (st == ST_PUBO) {
        if (c.isQ && threadIdx.x == 0) __builtin_amdgcn_raw_buffer_store_b32(c.tag, c.rs, WS_FLAG + (2 * 16 + c.rg) * 128 + c.hq * 4, 0, AUX_SC1);
      }
      op_go<st, 0>(c); FENCE();
      F_MMA(1, 0);
      t0 = trd(L, f1 + rf); FENCE();
      op_m0<st, 1>(c); FENCE();
      F_MMA(0, 1);
      t1 = trd(L, f1 + rf + 2048); fw[ch & 1][1] = (v4u){t0.x, t0.y, t1.x, t1.y}; FENCE();
      op_go<st, 1>(c); FENCE();
      F_MMA(1, 1);
      t0 = trd(L, f2_ + rf); FENCE();
      F_MMA(0, 2);
      t1 = trd(L, f2_ + rf + 2048); fw[ch & 1][2] = (v4u){t0.x, t0.y, t1.x, t1.y}; FENCE();
      if constexpr (hc >= 0) { issue_h0<(hc >= 0 ? hc : 0)>(c, hf); FENCE(); }
      F_MMA(1, 2);
      t0 = trd(L, f3 + rf); FENCE();
      F_MMA(0, 3);
      t1 = trd(L, f3 + rf + 2048); fw[ch & 1][3] = (v4u){t0.x, t0.y, t1.x, t1.y}; FENCE();
      if constexpr (hc >= 0) { issue_h1<(hc >= 0 ? hc : 0)>(c, hf); FENCE(); }
      if constexpr (hd >= 0) { issue_h<(hd >= 0 ? hd : 0)>(c, hf); FENCE(); }
      if constexpr (ch == 14) gld16(xres, x + (size_t)(64 * c.rt) * 1024 + c.ct * 128, (c.tid >> 3) * 1024 + (c.tid & 7) * 16);
      FENCE();
      F_MMA(1, 3);
    }
    if constexpr (st == ST_O) {
      if (c.isQ) {
        v2u ob[2][2];
#pragma unroll
        for (int i = 0; i < 2; ++i) {
          const int hl = 2 * c.nh + i;
          const v4u Akv = ld128(L, L_SCR + hl * 1024 + lane * 16), Az = ld128(L, L_SCR + 4096 + hl * 64 + g * 16);
#pragma unroll
          for (int tt = 0; tt < 2; ++tt) {
            const v4u ph = {fp[2 * i + tt].x, fp[2 * i + tt].y, fn[2 * i + tt].x, fn[2 * i + tt].y};
            const f4 num = mfma32(Akv, ph, z4), den = mfma32(Az, ph, z4);
            const float inv = __builtin_amdgcn_rcpf(den[0] + 1e-6f);
            ob[i][tt] = (v2u){pk(num[0] * inv, num[1] * inv), pk(num[2] * inv, num[3] * inv)};
          }
        }
        const unsigned ks = 56 + 2 * c.hq + c.nh;
        __builtin_amdgcn_raw_buffer_store_b128((v4u){ob[0][0].x, ob[0][0].y, ob[1][0].x, ob[1][0].y}, c.rs, WS_H + (gt0 * 72 + ks) * 1024 + lane * 16, 0, AUX_SC1);
        __builtin_amdgcn_raw_buffer_store_b128((v4u){ob[0][1].x, ob[0][1].y, ob[1][1].x, ob[1][1].y}, c.rs, WS_H + (gt1 * 72 + ks) * 1024 + lane * 16, 0, AUX_SC1);
      }
    }
    FENCE();
  };
  phase2_step(IC<0>{});
  phase2_step(IC<1>{});
  phase2_step(IC<2>{});
  phase2_step(IC<3>{});
  phase2_step(IC<4>{});
  phase2_step(IC<5>{});
  STAMP(c, 7);
  phase2_step(IC<6>{});
  phase2_step(IC<7>{});
  phase2_step(IC<8>{});
  phase2_step(IC<9>{});
  phase2_step(IC<10>{});
  phase2_step(IC<11>{});
  STAMP(c, 8);
  phase2_step(IC<12>{});
  phase2_step(IC<13>{});
  phase2_step(IC<14>{});
  phase2_step(IC<15>{});
  phase2_step(IC<16>{});
  phase2_step(IC<17>{});
  STAMP(c, 9);

  TS(3 * NSTEP);
  asm volatile("" : "+v"(fw[1][0]), "+v"(fw[1][1]), "+v"(fw[1][2]), "+v"(fw[1][3]));
  asm volatile("s_waitcnt vmcnt(0) lgkmcnt(0)" : "+v"(xres), "+v"(hf[17][0]), "+v"(hf[17][1])::"memory");
  TS(3 * NSTEP + 1);
#pragma unroll
  for (int t = 0; t < 4; ++t) { accF[0][t] = mfma32(fw[1][t], hf[17][0], accF[0][t]); accF[1][t] = mfma32(fw[1][t], hf[17][1], accF[1][t]); }
  REFRESH();
  constexpr unsigned FIN = L_RING + 4 * 16384;
  static_assert(SC.slot[NITEM - 1] < 4 && FIN + 4 * 17408 <= L_CS, "final partial sums");
#pragma unroll
  for (int j = 0; j < 2; ++j)
#pragma unroll
    for (int t = 0; t < 4; ++t) *(LAS f4*)(L + FIN + (c.w >> 1) * 17408 + (16 * (2 * (c.w & 1) + j) + l15) * 272 + (16 * t + 4 * g) * 4) = accF[j][t];
  wait_lgkm();
  bar();
  {
    const int tok = c.tid >> 3, cc = c.tid & 7;
    const unsigned a = FIN + tok * 272 + cc * 32;
    f4 s0 = (ldf4(L, a) + ldf4(L, a + 17408)) + (ldf4(L, a + 2 * 17408) + ldf4(L, a + 3 * 17408));
    f4 s1 = (ldf4(L, a + 16) + ldf4(L, a + 16 + 17408)) + (ldf4(L, a + 16 + 2 * 17408) + ldf4(L, a + 16 + 3 * 17408));
    const v4u bb = ld128(L, L_B2 + cc * 16);
    v4u o;
    o.x = pk(s0[0] + bflo(xres.x) + bflo(bb.x), s0[1] + bfhi(xres.x) + bfhi(bb.x));
    o.y = pk(s0[2] + bflo(xres.y) + bflo(bb.y), s0[3] + bfhi(xres.y) + bfhi(bb.y));
    o.z = pk(s1[0] + bflo(xres.z) + bflo(bb.z), s1[1] + bfhi(xres.z) + bfhi(bb.z));
    o.w = pk(s1[2] + bflo(xres.w) + bflo(bb.w), s1[3] + bfhi(xres.w) + bfhi(bb.w));
    { const __amdgpu_buffer_rsrc_t ro = __builtin_amdgcn_make_buffer_rsrc((void*)out, 0, 2048 * 1024, 0x00020000);
      __builtin_amdgcn_raw_buffer_store_b128(o, ro, (64 * c.rt + tok) * 1024 + c.ct * 128 + cc * 16, 0, 2);
    }
  }
  asm volatile(".Lhh_code_end:");
  if (blk == 0 && c.tid == 0) *(unsigned*)(ws + WS_EPOCH) = c.tag;
  STAMP(c, 10);
}

}

extern "C" void kernel_launch(void* const* d_in, const int* in_sizes, int n_in, void* d_out, int out_size, void* d_ws, size_t ws_size,
                              hipStream_t stream) {
  (void)in_sizes; (void)n_in; (void)out_size;
  const size_t WS_OFFSET = 0; (void)ws_size;
  (void)hipFuncSetAttribute((const void*)hh::fused_kernel, hipFuncAttributeMaxDynamicSharedMemorySize, hh::L_END);
  hh::fused_kernel<<<256, 512, hh::L_END, stream>>>((const char*)d_in[0], (const char*)d_in[1], (const char*)d_in[2], (const char*)d_in[3],
                                                    (const char*)d_in[5], (const char*)d_in[6], (const char*)d_in[7], (const char*)d_in[8],
                                                    (unsigned short*)d_out, (char*)d_ws + WS_OFFSET);
}
```

```cpp
#include <hip/hip_runtime.h>
#include <stdint.h>
#include <type_traits>

namespace hh {

#define LAS __attribute__((address_space(3)))
typedef unsigned v4u __attribute__((ext_vector_type(4)));
typedef unsigned v2u __attribute__((ext_vector_type(2)));
typedef float f4 __attribute__((ext_vector_type(4)));
typedef float f2 __attribute__((ext_vector_type(2)));
typedef short s4 __attribute__((ext_vector_type(4)));
typedef __bf16 b8 __attribute__((ext_vector_type(8)));
typedef __bf16 b2 __attribute__((ext_vector_type(2)));
template <int V> using IC = std::integral_constant<int, V>;

constexpr unsigned WS_EPOCH = 0;
constexpr unsigned WS_FLAG = 256;
constexpr unsigned WS_STATS = 8192;
constexpr unsigned WS_CS = WS_STATS + 2048 * 16;
constexpr unsigned PART_BYTES = 17408;
constexpr unsigned WS_P = WS_CS + 3328 * 32;
constexpr unsigned WS_R = WS_P + 8 * 16 * PART_BYTES;
constexpr unsigned WS_H = (WS_R + 8 * PART_BYTES + 4095u) & ~4095u;
constexpr unsigned WS_DBG = WS_H + 128 * 72 * 1024;
constexpr unsigned WS_END = WS_DBG;

constexpr int NSLOT = 7;
constexpr unsigned L_RING = 0;
constexpr unsigned L_SCR = NSLOT * 16384;
constexpr unsigned SCRW = 5248;
constexpr unsigned L_CS = L_SCR + 8 * SCRW;
constexpr unsigned L_ST = L_CS + 1024;
constexpr unsigned L_B1 = L_ST + 1024;
constexpr unsigned L_WPHI = L_B1 + 512;
constexpr unsigned L_B2 = L_WPHI + 2048;
constexpr unsigned L_DUMMY = L_B2 + 256;
constexpr unsigned L_END = L_DUMMY + 256;
static_assert(L_END <= 163840, "LDS");

constexpr int NITEM = 39, NSTEP = 32;
constexpr int ST_E1 = 8, ST_E2 = 13, ST_F0 = 14;
constexpr int IT_P0 = 16, IT_F0 = 20, IT_S = 21;
constexpr int item_of_F(int c) { return c == 0 ? IT_F0 : IT_S + c; }
#define HPRE_N 4
constexpr int HPRE = HPRE_N;
constexpr int DH = 2;
constexpr int ST_G1_ISSUE = 4, ST_G1_CHECK = 8;
#define PUBA_STEP 10
constexpr int ST_PUBA = PUBA_STEP;
constexpr int ST_POLLA_ISSUE = 12;
constexpr int ST_R_ISSUE = 11;
constexpr int ST_PUBB = 15;
constexpr int E2_OPS_AFTER_STORES = 4 + 2 * (HPRE - 4);
constexpr int ST_POLLB_ISSUE = 18, ST_POLLB_CHECK = 20;
constexpr int ST_RG_ISSUE = 13, ST_RG_CHECK = 16;
constexpr int ST_O = 17;
constexpr int ST_PUBO = 20;
constexpr int ST_POLLO_ISSUE = 24, ST_POLLO_CHECK = 26;
static_assert(ST_F0 + 8 - DH == ST_POLLB_CHECK && ST_F0 + 14 - DH == ST_POLLO_CHECK, "flag checks sit in front of the first dependent loads");

struct Sched {
  int slot[NITEM], issue[NITEM], first[NITEM], last[NITEM];
  int cum[NSTEP + 1];
  int pos_item[2][NITEM];
  int pos_h[2][18];
  int h_issue[18];
  int pos_g1[2];
};
constexpr int NPRO = 3;
constexpr int PRO_REG_LAST = 6;
constexpr Sched make_sched() {
  Sched s{};
  for (int i = 0; i < NITEM; ++i) {
    if (i < 16) { s.first[i] = i >> 1; s.last[i] = i >> 1; }
    else if (i < IT_F0) { s.first[i] = 9 + (i - IT_P0); s.last[i] = s.first[i]; }
    else if (i == IT_F0) { s.first[i] = ST_F0; s.last[i] = ST_F0; }
    else if (i == IT_S) { s.first[i] = 9; s.last[i] = 12; }
    else { s.first[i] = ST_F0 + (i - IT_S); s.last[i] = s.first[i]; }
  }
  s.last[15] = ST_E1;
  for (int c = 0; c < 18; ++c)
    s.h_issue[c] = c < HPRE ? ST_E2 : c < 8 ? ST_F0 + c - HPRE : c < 14 ? ST_POLLB_CHECK + (c - 8) / 2 : ST_POLLO_CHECK + (c - 14) / 2;
  int n = 0;
  int next = 0;
  int owner[NSLOT] = {};
  for (int k = 0; k < NSLOT; ++k) owner[k] = -2;
  for (int k = 0; k < NPRO; ++k) {
    s.slot[next] = k; s.issue[next] = -1; owner[k] = next; n += 2; s.pos_item[0][next] = s.pos_item[1][next] = n - 1; ++next;
    if (k == 1) n += 3;
  }
  for (int st = 0; st < NSTEP; ++st) {
    s.cum[st] = n;
    const int first_new = next;
    for (int rep = 0; rep < NSLOT; ++rep) {
      int best = -1;
      for (int k = 0; k < NSLOT; ++k)
        if ((owner[k] == -2 || (owner[k] >= 0 && s.last[owner[k]] < st)) && (best < 0 || owner[k] < owner[best])) best = k;
      if (best < 0) break;
      if (next < NITEM) { s.slot[next] = best; s.issue[next] = st; owner[best] = next; ++next; }
      else owner[best] = -1;
    }
    int nreg = 0;
    for (int c = 0; c < 18; ++c) if (s.h_issue[c] == st) nreg += 2;
    if (st == ST_G1_ISSUE) nreg += 2;
    const int nring = 2 * (next - first_new);
    for (int o = 0; o < 2; ++o) {
      int p = n + (o == 0 ? 0 : nreg);
      for (int i = first_new; i < next; ++i) { p += 2; s.pos_item[o][i] = p - 1; }
      p = n + (o == 0 ? nring : 0);
      for (int c = 0; c < 18; ++c) if (s.h_issue[c] == st) { p += 2; s.pos_h[o][c] = p - 1; }
      if (st == ST_G1_ISSUE) { p += 2; s.pos_g1[o] = p - 1; }
    }
    n += nring + nreg;
  }
  s.cum[NSTEP] = n;
  return s;
}
constexpr Sched SC = make_sched();
constexpr int imin(int a, int b) { return a < b ? a : b; }
constexpr int imax(int a, int b) { return a > b ? a : b; }
constexpr int top_wait(int st) {
  int need = -1;
  for (int o = 0; o < 2; ++o) {
    for (int i = 0; i < NITEM; ++i) if (SC.first[i] == st) need = imax(need, SC.pos_item[o][i]);
    if (st > ST_F0) need = imax(need, SC.pos_h[o][st - ST_F0 - 1]);
    if (st == ST_G1_CHECK) need = imax(need, SC.pos_g1[o]);
  }
  int n = need < 0 ? 63 : SC.cum[st] - 1 - need;
  if (st == 9) n += 2;
  if (st == ST_PUBA) n = imin(n, SC.cum[st] - SC.cum[ST_E1 + 1]);
  if (st == ST_PUBB) n = imin(n, SC.cum[st] - SC.cum[ST_E2 + 1] + E2_OPS_AFTER_STORES);
  if (st == ST_PUBO) n = imin(n, SC.cum[st] - SC.cum[ST_O + 1]);
  return n > 63 ? 63 : n;
}
constexpr int extra_wait(int a, int b) { int n = SC.cum[b] - SC.cum[a + 1]; return n > 63 ? 63 : n; }
constexpr int extra_wait_first(int a, int b) { int n = SC.cum[b] - SC.cum[a]; return n > 63 ? 63 : n; }
constexpr int h_at(int st, int k) {
  for (int c = 0; c < 18; ++c) if (SC.h_issue[c] == st) { if (k == 0) return c; --k; }
  return -1;
}
constexpr int nth_item(int st, int k) {
  for (int i = 0; i < NITEM; ++i) if (SC.issue[i] == st) { if (k == 0) return i; --k; }
  return -1;
}
static_assert(nth_item(0, 4) < 0 && nth_item(ST_E1, 0) == IT_S && nth_item(ST_E1, 1) < 0 && nth_item(ST_E2, 2) < 0 && nth_item(9, 1) < 0 && nth_item(10, 1) < 0 && nth_item(11, 1) < 0 && nth_item(12, 1) < 0, "schedule shape");

__host__ __device__ constexpr int unit_pos(int q, int off) { return off >= 2 && off <= 5 ? 4 * q + (off - 2) : 16 + 3 * q + (off == 6 ? 2 : off); }
__host__ __device__ constexpr int pos_unit(int pos) { return pos < 16 ? 7 * (pos >> 2) + 2 + (pos & 3) : 7 * ((pos - 16) / 3) + (((pos - 16) % 3) == 2 ? 6 : (pos - 16) % 3); }

#define DI __device__ __forceinline__
DI void glds16(const void* sbase, unsigned voff, unsigned lds) {
  asm volatile("s_mov_b32 m0, %2\n\ts_nop 0\n\tglobal_load_lds_dwordx4 %0, %1" ::"v"(voff), "s"(sbase), "s"(lds) : "memory");
}
DI void set_m0(unsigned lds) { asm volatile("s_mov_b32 m0, %0" ::"s"(lds) : "memory"); }
template <bool NT = false> DI void glds16_m0(const void* sbase, unsigned voff) {
  if constexpr (NT) asm volatile("global_load_lds_dwordx4 %0, %1 nt" ::"v"(voff), "s"(sbase) : "memory");
  else asm volatile("global_load_lds_dwordx4 %0, %1" ::"v"(voff), "s"(sbase) : "memory");
}
#define NT_X 0
#define NT_W 0
#define NT_F 0
DI void glds4(const void* sbase, unsigned voff, unsigned lds) {
  asm volatile("s_mov_b32 m0, %2\n\ts_nop 0\n\tglobal_load_lds_dword %0, %1" ::"v"(voff), "s"(sbase), "s"(lds) : "memory");
}
DI void glds4v(const void* vaddr, unsigned lds) {
  asm volatile("s_mov_b32 m0, %1\n\ts_nop 0\n\tglobal_load_lds_dword %0, off" ::"v"(vaddr), "s"(lds) : "memory");
}
DI void gld16(v4u& d, const void* sbase, unsigned voff) {
  asm volatile("global_load_dwordx4 %0, %1, %2" : "=v"(d) : "v"(voff), "s"(sbase) : "memory");
}
DI void gld16_sc1(v4u& d, const void* sbase, unsigned voff) {
  asm volatile("global_load_dwordx4 %0, %1, %2 sc1" : "=v"(d) : "v"(voff), "s"(sbase) : "memory");
}
template <int OFF> DI void gld16_sc1_off(v4u& d, const void* sbase, unsigned voff) {
  asm volatile("global_load_dwordx4 %0, %1, %2 offset:%3 sc1" : "=v"(d) : "v"(voff), "s"(sbase), "n"(OFF) : "memory");
}
DI void gld4_sc1(unsigned& d, const void* sbase, unsigned voff) {
  asm volatile("global_load_dword %0, %1, %2 sc1" : "=v"(d) : "v"(voff), "s"(sbase) : "memory");
}
template <int N> DI void wait_top() { asm volatile("s_waitcnt vmcnt(%0) lgkmcnt(0)" ::"n"(N) : "memory"); }
template <int N> DI void wait_vm() { asm volatile("s_waitcnt vmcnt(%0)" ::"n"(N) : "memory"); }
DI void wait_lgkm() { asm volatile("s_waitcnt lgkmcnt(0)" ::: "memory"); }
DI void bar() { asm volatile("" ::: "memory"); __builtin_amdgcn_s_barrier(); asm volatile("" ::: "memory"); }
#define FENCE() __builtin_amdgcn_sched_barrier(0)

DI unsigned pk(float a, float b) { b2 r = {(__bf16)a, (__bf16)b}; return __builtin_bit_cast(unsigned, r); }
DI float bflo(unsigned u) { return __builtin_bit_cast(float, u << 16); }
DI float bfhi(unsigned u) { return __builtin_bit_cast(float, u & 0xffff0000u); }
DI unsigned fbits(float f) { return __builtin_bit_cast(unsigned, f); }
DI float bitsf(unsigned u) { return __builtin_bit_cast(float, u); }
DI f4 mfma32(v4u a, v4u b, f4 c) { return __builtin_amdgcn_mfma_f32_16x16x32_bf16(__builtin_bit_cast(b8, a), __builtin_bit_cast(b8, b), c, 0, 0, 0); }
DI f4 mfma16(v2u a, v2u b, f4 c) { return __builtin_amdgcn_mfma_f32_16x16x16bf16_1k(__builtin_bit_cast(s4, a), __builtin_bit_cast(s4, b), c, 0, 0, 0); }
DI v2u trd(LAS char* L, unsigned a) { return __builtin_bit_cast(v2u, __builtin_amdgcn_ds_read_tr16_b64_v4i16((LAS s4*)(L + a))); }
DI v4u trd2(LAS char* L, unsigned a0, unsigned a1) { v2u x = trd(L, a0), y = trd(L, a1); return (v4u){x.x, x.y, y.x, y.y}; }
DI v4u ld128(LAS char* L, unsigned a) { return *(LAS v4u*)(L + a); }
DI f4 ldf4(LAS char* L, unsigned a) { return *(LAS f4*)(L + a); }
DI f2 ldf2(LAS char* L, unsigned a) { return *(LAS f2*)(L + a); }
DI v2u ld64(LAS char* L, unsigned a) { return *(LAS v2u*)(L + a); }

template <int CTRL> DI float dpp(float v) { return bitsf((unsigned)__builtin_amdgcn_update_dpp(0, (int)fbits(v), CTRL, 0xf, 0xf, true)); }
DI void sw32(float& a, float& b) { auto r = __builtin_amdgcn_permlane32_swap(fbits(a), fbits(b), false, false); a = bitsf((unsigned)r[0]); b = bitsf((unsigned)r[1]); }
DI void sw16(float& a, float& b) { auto r = __builtin_amdgcn_permlane16_swap(fbits(a), fbits(b), false, false); a = bitsf((unsigned)r[0]); b = bitsf((unsigned)r[1]); }
DI float xsum(float v) { float a = v, b = v; sw16(a, b); v = a + b; a = v; b = v; sw32(a, b); return a + b; }
DI float rowsum16(float v) { v += dpp<0x128>(v); v += dpp<0x124>(v); v += dpp<0x122>(v); v += dpp<0x121>(v); return v; }
DI float wavesum(float v) { return xsum(rowsum16(v)); }

constexpr float LOG2E = 1.4426950408889634f;
DI float vmax(float a, float b) { return __builtin_elementwise_maximum(a, b); }
DI float vmin(float a, float b) { return __builtin_elementwise_minimum(a, b); }
template <class OP> DI float fold4(float a0, float a1, float a2, float a3, OP op) {
  sw32(a0, a1); sw32(a2, a3);
  float m01 = op(a0, a1), m23 = op(a2, a3);
  sw16(m01, m23);
  return op(m01, m23);
}
DI void unfold4(float m, float (&o)[4]) {
  float a = m, b = m;
  sw16(a, b);
  float a2 = a, b2 = b;
  sw32(a, a2); sw32(b, b2);
  o[0] = a; o[1] = a2; o[2] = b; o[3] = b2;
}
struct Feat { float a[4], b[4]; f2 ep[4][2], en[4][2]; };
DI void tie8(f2 (&a)[4][2]) { asm volatile("" : "+v"(a[0][0]), "+v"(a[0][1]), "+v"(a[1][0]), "+v"(a[1][1]), "+v"(a[2][0]), "+v"(a[2][1]), "+v"(a[3][0]), "+v"(a[3][1])); }
DI void feat_max(const f4 (&p)[4], Feat& f) {
  float mx[4], mn[4];
#pragma unroll
  for (int t = 0; t < 4; ++t) {
    mx[t] = vmax(vmax(vmax(p[t][0], p[t][1]), p[t][2]), p[t][3]);
    mn[t] = vmin(vmin(vmin(p[t][0], p[t][1]), p[t][2]), p[t][3]);
  }
  const float MX = fold4(mx[0], mx[1], mx[2], mx[3], [](float a, float b) { return vmax(a, b); }) * -LOG2E;
  const float MN = fold4(mn[0], mn[1], mn[2], mn[3], [](float a, float b) { return vmin(a, b); }) * LOG2E;
  unfold4(MX, f.a);
  unfold4(MN, f.b);
}
DI void feat_exp(const f4 (&p)[4], Feat& f) {
#pragma unroll
  for (int t = 0; t < 4; ++t)
#pragma unroll
    for (int h = 0; h < 2; ++h) {
      const f2 v = {p[t][2 * h], p[t][2 * h + 1]};
      f.ep[t][h] = __builtin_elementwise_fma(v, (f2){LOG2E, LOG2E}, (f2){f.a[t], f.a[t]});
      f.en[t][h] = __builtin_elementwise_fma(v, (f2){-LOG2E, -LOG2E}, (f2){f.b[t], f.b[t]});
    }
  FENCE();
#pragma unroll
  for (int t = 0; t < 4; ++t)
#pragma unroll
    for (int h = 0; h < 2; ++h) {
      f.ep[t][h] = (f2){__builtin_amdgcn_exp2f(f.ep[t][h].x), __builtin_amdgcn_exp2f(f.ep[t][h].y)};
      f.en[t][h] = (f2){__builtin_amdgcn_exp2f(f.en[t][h].x), __builtin_amdgcn_exp2f(f.en[t][h].y)};
    }
  FENCE();
}
DI void feat_norm(const Feat& f, v2u (&pos)[4], v2u (&neg)[4]) {
  float sp[4], sn[4];
#pragma unroll
  for (int t = 0; t < 4; ++t) {
    const f2 a = f.ep[t][0] + f.ep[t][1], b = f.en[t][0] + f.en[t][1];
    sp[t] = a.x + a.y;
    sn[t] = b.x + b.y;
  }
  const float IP = __builtin_amdgcn_rcpf(fold4(sp[0], sp[1], sp[2], sp[3], [](float a_, float b_) { return a_ + b_; }));
  const float IN = __builtin_amdgcn_rcpf(fold4(sn[0], sn[1], sn[2], sn[3], [](float a_, float b_) { return a_ + b_; }));
  float ip[4], in[4];
  unfold4(IP, ip);
  unfold4(IN, in);
#pragma unroll
  for (int t = 0; t < 4; ++t) {
    const f2 a0 = f.ep[t][0] * ip[t], a1 = f.ep[t][1] * ip[t], b0 = f.en[t][0] * in[t], b1 = f.en[t][1] * in[t];
    pos[t] = (v2u){pk(a0.x, a0.y), pk(a1.x, a1.y)};
    neg[t] = (v2u){pk(b0.x, b0.y), pk(b1.x, b1.y)};
  }
}

template <unsigned OFF> DI unsigned win(unsigned base) {
  if constexpr (OFF >= 65536u - 16384u) { unsigned b = base + (OFF & ~16383u); asm volatile("" : "+v"(b)); return b; }
  else return base;
}
template <unsigned OFF> constexpr unsigned rem() { return OFF >= 65536u - 16384u ? (OFF & 16383u) : OFF; }
constexpr unsigned HIWIN = 49152;
#define PRIO(k) do { } while (0)
#define PRIO_OFF() do { } while (0)
#define AUX_SC1 16
constexpr unsigned SPIN_CAP = 1u << 16;

struct Ctx {
  LAS char* L;
  unsigned lbase;
  const char *x, *wo, *w2;
  char* ws;
  __amdgpu_buffer_rsrc_t rs;
  unsigned tag;
  int tid, lane, w, m, nh;
  int rg, q, jj, hq, isQ, rt, ct;
  const char *u0, *u1, *u2, *ush;
  unsigned ld1, ld2;
  unsigned vX0, vX1, vW0, vW1, vP0, vP1, vF0, vF1;
  const char *pX, *pW, *pP, *pF1, *pF5, *pWo;
  const char* pH;
  unsigned vH, vH1;
  unsigned ldsw;
};

#define TS(i) do { } while (0)
#define TSX(i) do { } while (0)
#define STAMP(c, i) do { } while (0)

template <int I, int U> DI void half_m0(const Ctx& c) { set_m0(c.ldsw + SC.slot[I] * 16384 + U * 1024); }
template <int I, int U> DI void half_go(const Ctx& c) {
  if constexpr (I < 16 && (I & 1) == 0) {
    glds16_m0<NT_X>(c.pX + (I >> 1) * 128, U ? c.vX1 : c.vX0);
  } else if constexpr (I < 16) {
    glds16_m0<NT_W>(c.pW + (size_t)(64 * (I >> 1)) * c.ld1, U ? c.vW1 : c.vW0);
  } else if constexpr (I == IT_S) {
    const unsigned rr = c.lane >> 1;
    glds16_m0<NT_W>(c.ush + (size_t)(64 * c.w + 32 * U) * 3584, (rr ^ ((rr & 8) >> 1)) * 3584 + (c.lane & 1) * 16);
  } else if constexpr (I < IT_F0) {
    glds16_m0<NT_W>(c.pP + (size_t)(128 * (I - IT_P0)) * c.ld2, U ? c.vP1 : c.vP0);
  } else {
    constexpr int C = I == IT_F0 ? 0 : I - IT_S;
    if constexpr (C < 14) {
      constexpr int d = pos_unit(2 * C + 1) - pos_unit(2 * C);
      static_assert(d == 1 || d == 5, "unit distance inside a chunk");
      glds16_m0<NT_F>((d == 1 ? c.pF1 : c.pF5) + pos_unit(2 * C) * 65536, U ? c.vF1 : c.vF0);
    } else {
      glds16_m0<NT_F>(c.pWo + (C - 14) * 131072, U ? c.vF1 : c.vF0);
    }
  }
}
template <int I, int U> DI void issue_half(const Ctx& c) { half_m0<I, U>(c); asm volatile("s_nop 0"); half_go<I, U>(c); }
template <int ST, int K> DI void op_m0(const Ctx& c) { constexpr int I = nth_item(ST, K >> 1); if constexpr (I >= 0) half_m0<I, (K & 1)>(c); }
template <int ST, int K> DI void op_go(const Ctx& c) { constexpr int I = nth_item(ST, K >> 1); if constexpr (I >= 0) half_go<I, (K & 1)>(c); }
template <int ST, int K> DI void issue_op(const Ctx& c) { constexpr int I = nth_item(ST, K >> 1); if constexpr (I >= 0) issue_half<I, (K & 1)>(c); }
template <int C> DI void issue_h0(const Ctx& c, v4u (&hf)[18][2]) { gld16_sc1(hf[C][0], c.pH + C * 4096, c.vH); }
template <int C> DI void issue_h1(const Ctx& c, v4u (&hf)[18][2]) { gld16_sc1(hf[C][1], c.pH + C * 4096, c.vH1); }
template <int C> DI void issue_h(const Ctx& c, v4u (&hf)[18][2]) { issue_h0<C>(c, hf); issue_h1<C>(c, hf); }

DI void spin_flags(const Ctx& c, const char* line, int n) {
#pragma unroll 1
  for (unsigned it = 0; it < SPIN_CAP; ++it) {
    unsigned v;
    gld4_sc1(v, line, (unsigned)(c.lane & 31) * 4);
    asm volatile("s_waitcnt vmcnt(0)" : "+v"(v)::"memory");
    if (__all(v == c.tag || (c.lane & 31) >= n)) return;
    __builtin_amdgcn_s_sleep(2);
  }
}
DI bool tags_ok(v4u a, unsigned tag) { return a.y == tag && a.w == tag; }

template <int N> DI void tie(f2 (&v)[N]) {
  if constexpr (N == 2) asm volatile("" : "+v"(v[0]), "+v"(v[1]));
  else if constexpr (N == 4) asm volatile("" : "+v"(v[0]), "+v"(v[1]), "+v"(v[2]), "+v"(v[3]));
  else static_assert(N == 2 || N == 4, "tie");
}
template <int N> DI void gelu_n(f2 (&u)[N]) {
  const float c0 = -2.0f * LOG2E * 0.7978845608028654f, c1 = c0 * 0.044715f;
  f2 e[N];
  FENCE();
#pragma unroll
  for (int r = 0; r < N; ++r) e[r] = u[r] * u[r];
#pragma unroll
  for (int r = 0; r < N; ++r) e[r] = __builtin_elementwise_fma(e[r], (f2){c1, c1}, (f2){c0, c0});
#pragma unroll
  for (int r = 0; r < N; ++r) e[r] = u[r] * e[r];
  FENCE();
#pragma unroll
  for (int r = 0; r < N; ++r) e[r] = (f2){__builtin_amdgcn_exp2f(e[r].x), __builtin_amdgcn_exp2f(e[r].y)};
  FENCE();
#pragma unroll
  for (int r = 0; r < N; ++r) e[r] = e[r] + (f2){1.0f, 1.0f};
  FENCE();
#pragma unroll
  for (int r = 0; r < N; ++r) e[r] = (f2){__builtin_amdgcn_rcpf(e[r].x), __builtin_amdgcn_rcpf(e[r].y)};
  FENCE();
#pragma unroll
  for (int r = 0; r < N; ++r) u[r] *= e[r];
}
DI void ln_bias(f2* u, f4 a, f4 cs, v2u bb, f2 st) {
  const f2 b0 = {bflo(bb.x), bfhi(bb.x)}, b1 = {bflo(bb.y), bfhi(bb.y)}, sx = {st.x, st.x}, sy = {-st.y, -st.y};
  u[0] = __builtin_elementwise_fma((f2){a[0], a[1]}, sx, __builtin_elementwise_fma(sy, (f2){cs[0], cs[1]}, b0));
  u[1] = __builtin_elementwise_fma((f2){a[2], a[3]}, sx, __builtin_elementwise_fma(sy, (f2){cs[2], cs[3]}, b1));
}
DI v2u ffn_tile(f4 a, f4 cs, v2u bb, f2 st) {
  f2 u[2];
  ln_bias(u, a, cs, bb, st);
  gelu_n<2>(u);
  return (v2u){pk(u[0].x, u[0].y), pk(u[1].x, u[1].y)};
}
DI v4u ffn_pair(f4 a0, f4 a1, f4 cs0, f4 cs1, v2u bb0, v2u bb1, f2 st) {
  f2 u[4];
  ln_bias(u, a0, cs0, bb0, st);
  ln_bias(u + 2, a1, cs1, bb1, st);
  gelu_n<4>(u);
  return (v4u){pk(u[0].x, u[0].y), pk(u[1].x, u[1].y), pk(u[2].x, u[2].y), pk(u[3].x, u[3].y)};
}
DI v2u ln_pack(f4 a, f4 cs, f2 st) {
  float v[4];
#pragma unroll
  for (int r = 0; r < 4; ++r) v[r] = __builtin_fmaf(a[r], st.x, -st.y * cs[r]);
  return (v2u){pk(v[0], v[1]), pk(v[2], v[3])};
}

__global__ __launch_bounds__(512, 2) void fused_kernel(const char* __restrict__ x, const char* __restrict__ wqkv, const char* __restrict__ wphi,
                                                       const char* __restrict__ wo, const char* __restrict__ w1, const char* __restrict__ b1,
                                                       const char* __restrict__ w2, const char* __restrict__ b2, unsigned short* __restrict__ out,
                                                       char* __restrict__ ws) {
  extern __shared__ __attribute__((aligned(16))) char smem[];
  Ctx c;
  c.L = (LAS char*)smem;
  c.lbase = (unsigned)(size_t)c.L;
  c.x = x; c.wo = wo; c.w2 = w2; c.ws = ws;
  c.tid = threadIdx.x; c.lane = c.tid & 63; c.w = __builtin_amdgcn_readfirstlane(c.tid >> 6); c.m = c.w & 3; c.nh = c.w >> 2;
  const int blk = blockIdx.x, xcd = blk & 7, idx = blk >> 3;
  c.rg = (xcd >> 2) * 8 + (idx >> 2); c.q = xcd & 3; c.jj = idx & 3; c.isQ = c.jj >> 1; c.hq = 2 * c.q + (c.jj & 1);
  c.rt = xcd * 4 + (idx >> 3); c.ct = idx & 7;
  c.rs = __builtin_amdgcn_make_buffer_rsrc((void*)ws, 0, WS_END, 0x00020000);
  int lane = c.lane, g = lane >> 4, l15 = lane & 15, q4 = l15 >> 2, p4 = lane & 3;
#define REFRESH() do { int t_ = threadIdx.x; asm volatile("" : "+v"(t_)); c.tid = t_; lane = t_ & 63; c.lane = lane; g = lane >> 4; l15 = lane & 15; q4 = l15 >> 2; p4 = lane & 3; } while (0)
  LAS char* L = c.L;
  STAMP(c, 0);
  asm volatile("" ::"s"(x), "s"(wqkv), "s"(wphi), "s"(wo), "s"(w1), "s"(b1), "s"(w2), "s"(b2), "s"(out), "s"(ws));
  c.ldsw = c.lbase + L_RING + c.w * 2048;
  c.vX0 = (lane >> 3) * 1024 + (((lane & 7) ^ (lane >> 4)) * 16);
  c.vX1 = (c.vX0 ^ 64u) + 8 * 1024;
  c.pX = x + (size_t)(128 * c.rg + 16 * c.w) * 1024;
  issue_half<0, 0>(c); issue_half<0, 1>(c);
  FENCE();
  const int f0u = 7 * c.q;
  const int fa = f0u + (c.isQ ? 2 + 2 * (c.jj & 1) : c.jj);
  c.u0 = c.isQ ? w1 + fa * 128 : wqkv + (8 + c.hq) * 128;
  c.u1 = c.u0 + (c.isQ ? 128 : 1024);
  c.ld1 = c.isQ ? 3584 : 3072;
  c.vW0 = (lane >> 3) * c.ld1 + (((lane & 7) ^ ((lane >> 3) & 2)) * 16);
  c.vW1 = (c.vW0 ^ 64u) + 8 * c.ld1;
  c.pW = (c.nh ? c.u1 : c.u0) + (size_t)(16 * c.m) * c.ld1;
  issue_half<1, 0>(c); issue_half<1, 1>(c);
  FENCE();
  c.tag = *(const __attribute__((address_space(4))) unsigned*)(ws + WS_EPOCH) + 1u;
  v4u xr, cw0, cw1;
  const int j16 = 4 * c.q + c.jj;
  gld16(xr, x + (size_t)(128 * c.rg + 8 * j16 + c.w) * 1024, lane * 16);
  const int job = (xcd >> 2) * 32 + idx;
  const int jobc = job < 52 ? job : 51;
  const int li = jobc >> 2, kq = jobc & 3;
  const int gunit = li < 6 ? (li >> 1) * 8 + 2 * c.q + (li & 1) : 24 + f0u + (li - 6);
  {
    const char* wb = li < 6 ? wqkv + gunit * 128 : w1 + (gunit - 24) * 128;
    const unsigned ld = li < 6 ? 3072 : 3584;
    const char* bb = wb + (size_t)(128 * kq + 16 * c.w) * ld;
    const unsigned v = (lane >> 3) * ld + (lane & 7) * 16;
    gld16(cw0, bb, v);
    gld16(cw1, bb + 8 * ld, v);
  }
  issue_half<2, 0>(c); issue_half<2, 1>(c);
  FENCE();
  c.u2 = c.isQ ? wqkv + c.hq * 128 : w1 + fa * 128;
  c.ld2 = c.isQ ? 3072 : 3584;
  c.ush = w1 + (f0u + 6) * 128 + c.jj * 32;
  unsigned g1off = 0;
  STAMP(c, 1);
  FENCE();

  v4u xf[2][16];
  f4 acc[2][2][2];
  const f4 z4 = {0.f, 0.f, 0.f, 0.f};
#pragma unroll
  for (int a = 0; a < 2; ++a)
#pragma unroll
    for (int b = 0; b < 2; ++b)
#pragma unroll
      for (int t = 0; t < 2; ++t) acc[a][b][t] = z4;
  const unsigned xa0 = (unsigned)((32 * c.m + l15) * 128 + ((g ^ (l15 >> 1)) * 16));
  const unsigned xtt0 = c.nh * 2048, xtt1 = (1 - c.nh) * 2048;
  const unsigned wa0 = (unsigned)((8 * g + q4) * 128 + (((4 * c.nh + (p4 >> 1)) ^ ((((q4 >> 1) & 1) | ((g & 1) << 1)) << 1)) * 16) + (p4 & 1) * 8);
  unsigned xb[2][4] = {{xa0 + xtt0, xa0 + xtt1, (xa0 ^ 64) + xtt0, (xa0 ^ 64) + xtt1}, {}}, wa[2][2] = {{wa0, wa0 ^ 32}, {}};
#pragma unroll
  for (int i = 0; i < 4; ++i) { xb[1][i] = xb[0][i] + HIWIN; asm volatile("" : "+v"(xb[1][i])); }
#pragma unroll
  for (int i = 0; i < 2; ++i) { wa[1][i] = wa[0][i] + HIWIN; asm volatile("" : "+v"(wa[1][i])); }
  v4u g1a, g1b;
  v4u wfa[2][2], wfb[2][2];
  auto pass1_step = [&](auto KC) {
    constexpr int kc = decltype(KC)::value;
    constexpr unsigned sx = L_RING + SC.slot[2 * kc] * 16384, sw = L_RING + SC.slot[2 * kc + 1] * 16384;
    TS(3 * kc);
    if constexpr (kc > 0)
      asm volatile("" : "+v"(wfb[0][0]), "+v"(wfb[0][1]), "+v"(wfb[1][0]), "+v"(wfb[1][1]), "+v"(xf[0][kc > 0 ? 2 * kc - 1 : 0]), "+v"(xf[1][kc > 0 ? 2 * kc - 1 : 0]));
    if constexpr (kc == 0) {
      wait_top<top_wait(kc)>();
    } else {
      wait_vm<top_wait(kc)>();
    }
    TS(3 * kc + 1);
    bar();
    TS(3 * kc + 2);
    FENCE();
    constexpr int hx = sx >= HIWIN, hw = sw >= HIWIN;
    const unsigned x0 = xb[hx][0], x1 = xb[hx][1], x2 = xb[hx][2], x3 = xb[hx][3], w0 = wa[hw][0], w1_ = wa[hw][1];
    constexpr unsigned rx = sx - hx * HIWIN, rw = sw - hw * HIWIN;
#define P1_MMA(F, KS, UN, I, T) do { if constexpr (DOM_) acc[UN][I][T] = mfma32(F[UN][I], xf[T][KS], acc[UN][I][T]); FENCE(); } while (0)
#define P1_HALF(FR, FM, KSR, KSM, XA, XB, SOFF, G0, G1, DOM, TIE) {                                 \
    constexpr bool DOM_ = DOM;                                                                     \
    v2u t0, t1;                                                                                    \
    xf[0][KSR] = ld128(L, XA + rx); FENCE();                                                       \
    if constexpr (TIE) {                                                                           \
      xf[1][KSR] = ld128(L, XB + rx); FENCE();                                                     \
      t0 = trd(L, w0 + rw + SOFF); FENCE();                                                        \
      P1_MMA(FM, KSM, 0, 0, 0);                                                                    \
    } else {                                                                                       \
      P1_MMA(FM, KSM, 0, 0, 0);                                                                    \
      xf[1][KSR] = ld128(L, XB + rx); FENCE();                                                     \
      t0 = trd(L, w0 + rw + SOFF); FENCE();                                                        \
    }                                                                                              \
    P1_MMA(FM, KSM, 0, 0, 1);                                                                      \
    t1 = trd(L, w0 + rw + SOFF + 512); FR[0][0] = (v4u){t0.x, t0.y, t1.x, t1.y}; FENCE();          \
    op_m0<kc, G0>(c); FENCE();                                                                     \
    P1_MMA(FM, KSM, 0, 1, 0);                                                                      \
    FR[0][1] = trd2(L, w1_ + rw + SOFF, w1_ + rw + SOFF + 512); FENCE();                           \
    P1_MMA(FM, KSM, 0, 1, 1);                                                                      \
    t0 = trd(L, w0 + rw + SOFF + 8192); FENCE();                                                   \
    op_go<kc, G0>(c); FENCE();                                                                     \
    P1_MMA(FM, KSM, 1, 0, 0);                                                                      \
    t1 = trd(L, w0 + rw + SOFF + 8192 + 512); FR[1][0] = (v4u){t0.x, t0.y, t1.x, t1.y}; FENCE();   \
    op_m0<kc, G1>(c); FENCE();                                                                     \
    P1_MMA(FM, KSM, 1, 0, 1);                                                                      \
    FR[1][1] = trd2(L, w1_ + rw + SOFF + 8192, w1_ + rw + SOFF + 8192 + 512); FENCE();             \
    P1_MMA(FM, KSM, 1, 1, 0);                                                                      \
    op_go<kc, G1>(c); FENCE();                                                                     \
    P1_MMA(FM, KSM, 1, 1, 1);                                                                      \
  }
    P1_HALF(wfa, wfb, 2 * kc, (kc > 0 ? 2 * kc - 1 : 0), x0, x1, 0, 0, 1, (kc > 0), false)
    if constexpr (kc == 0) { issue_op<0, 2>(c); issue_op<0, 3>(c); issue_op<0, 4>(c); issue_op<0, 5>(c); FENCE(); }
    if constexpr (kc == 0) {
    {
      asm volatile("s_waitcnt vmcnt(%3)" : "+v"(xr), "+v"(cw0), "+v"(cw1) : "n"(SC.cum[0] - 1 - PRO_REG_LAST + imin(SC.cum[1] - SC.cum[0], 6)) : "memory");
      const float x0v = bitsf(__builtin_amdgcn_readfirstlane(xr.x << 16));
      float d[8] = {bflo(xr.x) - x0v, bfhi(xr.x) - x0v, bflo(xr.y) - x0v, bfhi(xr.y) - x0v, bflo(xr.z) - x0v, bfhi(xr.z) - x0v, bflo(xr.w) - x0v, bfhi(xr.w) - x0v};
      float s = ((d[0] + d[1]) + (d[2] + d[3])) + ((d[4] + d[5]) + (d[6] + d[7]));
      float qq = d[0] * d[0];
  #pragma unroll
      for (int e = 1; e < 8; ++e) qq = __builtin_fmaf(d[e], d[e], qq);
      float cs[8] = {bflo(cw0.x) + bflo(cw1.x), bfhi(cw0.x) + bfhi(cw1.x), bflo(cw0.y) + bflo(cw1.y), bfhi(cw0.y) + bfhi(cw1.y),
                     bflo(cw0.z) + bflo(cw1.z), bfhi(cw0.z) + bfhi(cw1.z), bflo(cw0.w) + bflo(cw1.w), bfhi(cw0.w) + bfhi(cw1.w)};
      s += dpp<0x128>(s); qq += dpp<0x128>(qq);
  #pragma unroll
      for (int e = 0; e < 4; ++e) { sw32(cs[e], cs[e + 4]); cs[e] += cs[e + 4]; }
      s += dpp<0x124>(s); qq += dpp<0x124>(qq);
  #pragma unroll
      for (int e = 0; e < 2; ++e) { sw16(cs[e], cs[e + 2]); cs[e] += cs[e + 2]; }
      s += dpp<0x122>(s); qq += dpp<0x122>(qq);
      cs[0] += dpp<0x128>(cs[0]); cs[1] += dpp<0x128>(cs[1]);
      s += dpp<0x121>(s); qq += dpp<0x121>(qq);
      if ((lane & 8) == 0) *(LAS f2*)(L + L_SCR + c.w * 256 + ((lane & 7) * 8 + (lane >> 5) * 4 + ((lane >> 4) & 1) * 2) * 4) = (f2){cs[0], cs[1]};
      sw32(s, qq);
      float sq = s + qq;
      { float a = sq, b = sq; sw16(a, b); sq = a + b; }
      const float sd = sq * (1.0f / 512.0f);
      const float m2 = bitsf(__builtin_amdgcn_readlane(fbits(sd), 32));
      if (lane == 0) {
        const float var = __builtin_fmaf(-sd, sd, m2);
        const float rstd = __builtin_amdgcn_rsqf(vmax(var, 0.f) + 1e-5f);
        __builtin_amdgcn_raw_buffer_store_b128((v4u){fbits(x0v + sd), c.tag, fbits(rstd), c.tag}, c.rs, WS_STATS + (128 * c.rg + 8 * j16 + c.w) * 16, 0, AUX_SC1);
      }
    }
      FENCE();
    }
    if constexpr (kc == 2) {
      REFRESH();
      const int ww = c.w;
      const char* p = ww < 2 ? b1 + (ww == 0 ? fa * 128 : (f0u + 6) * 128 + c.jj * 32) : ww < 4 ? wphi + c.hq * 2048 + (ww - 2) * 1024 : b2 + c.ct * 128;
      const unsigned d = c.lbase + (ww < 2 ? L_B1 + ww * 256 : ww < 4 ? L_WPHI + (ww - 2) * 1024 : L_B2);
      if ((ww >> 1) == 1) glds16(p, lane * 16, d);
      else glds4(p, (lane & (ww == 0 ? 63 : ww == 1 ? 7 : 31)) * 4, d);
      FENCE();
    }
    if constexpr (kc == 3) {
      if (c.w < 3) {
        REFRESH();
        const unsigned o2 = c.w < 2 ? (c.tid < 112 ? c.tid : 111) * 64 : (lane & 31) * 64;
        glds4(c.w < 2 ? w2 + (size_t)blk * (56 * 128) : wo + (size_t)blk * (16 * 128), o2, c.lbase + L_DUMMY);
      }
      FENCE();
    }
    if constexpr (kc == 2) {
      REFRESH();
      const int ufa = 24 + fa;
      const int ua = c.isQ ? ufa : 8 + c.hq, ub = c.isQ ? ufa + 1 : 16 + c.hq, uc = c.isQ ? c.hq : ufa;
      const unsigned base = c.w < 2 ? WS_STATS + (128 * c.rg + 64 * c.w) * 16
                          : WS_CS + (c.w == 2 ? ua * 64 : c.w == 3 ? ub * 64 : c.w == 4 ? uc * 64 : (24 + f0u + 6) * 64 + 16 * c.jj) * 32;
      g1off = base + (c.w < 2 ? lane * 16 : (c.w < 5 ? lane : (lane & 15)) * 32);
      asm volatile("" : "+v"(g1off));
    }
    if constexpr (kc == 4) {
      REFRESH();
      c.vP0 = (lane >> 3) * c.ld2 + (((lane & 7) ^ ((lane >> 3) & 2)) * 16);
      c.vP1 = (c.vP0 ^ 64u) + 8 * c.ld2;
      c.pP = c.u2 + (size_t)(16 * c.w) * c.ld2;
      asm volatile("" : "+v"(c.vP0), "+v"(c.vP1));
    }
    if constexpr (kc == 6) {
      REFRESH();
      c.vF0 = (lane >> 3) * 1024 + (((lane & 7) ^ ((lane >> 3) & 6)) * 16);
      c.vF1 = c.vF0 + 8 * 1024;
      const unsigned o = (16 * c.m) * 1024 + c.ct * 128;
      c.pF1 = w2 + o + c.nh * 65536; c.pF5 = w2 + o + c.nh * 5 * 65536; c.pWo = wo + o + c.nh * 65536;
      asm volatile("" : "+v"(c.vF0), "+v"(c.vF1));
    }
    if constexpr (kc == 1) {
      if (c.w == 7 && job < 52) {
        float s = 0.f;
#pragma unroll
        for (int ww = 0; ww < 8; ++ww) s += *(LAS float*)(L + L_SCR + ww * 256 + lane * 4);
        __builtin_amdgcn_raw_buffer_store_b64((v2u){fbits(s), c.tag}, c.rs, WS_CS + (gunit * 64 + lane) * 32 + kq * 8, 0, AUX_SC1);
      }
      FENCE();
    }
    P1_HALF(wfb, wfa, 2 * kc + 1, 2 * kc, x2, x3, 4096, (kc == 0 ? 6 : 2), (kc == 0 ? 7 : 3), true, true)
    if constexpr (kc == ST_G1_ISSUE) {
      gld16_sc1(g1a, ws, g1off);
      gld16_sc1(g1b, ws + (c.w < 2 ? 0 : 16), g1off);
      FENCE();
    }
  };
  pass1_step(IC<0>{});
  STAMP(c, 2);
  pass1_step(IC<1>{});
  pass1_step(IC<2>{});
  pass1_step(IC<3>{});
  pass1_step(IC<4>{});
  pass1_step(IC<5>{});
  pass1_step(IC<6>{});
  pass1_step(IC<7>{});
  STAMP(c, 3);

  TS(3 * ST_E1);
  asm volatile("" : "+v"(wfb[0][0]), "+v"(wfb[0][1]), "+v"(wfb[1][0]), "+v"(wfb[1][1]), "+v"(xf[0][15]), "+v"(xf[1][15]));
  {
      asm volatile("s_waitcnt vmcnt(%2) lgkmcnt(0)" : "+v"(g1a), "+v"(g1b) : "n"(top_wait(ST_E1)) : "memory");
      REFRESH();
      const bool isst = c.w < 2;
      if (__builtin_expect(!__all(tags_ok(g1a, c.tag) && (isst || tags_ok(g1b, c.tag))), 0)) {
        const unsigned off = g1off;
#pragma unroll 1
        for (unsigned it = 0; it < SPIN_CAP; ++it) {
          __builtin_amdgcn_s_sleep(2);
          gld16_sc1(g1a, ws, off);
          gld16_sc1(g1b, ws + (isst ? 0 : 16), off);
          asm volatile("s_waitcnt vmcnt(0)" : "+v"(g1a), "+v"(g1b)::"memory");
          if (__all(tags_ok(g1a, c.tag) && (isst || tags_ok(g1b, c.tag)))) break;
        }
      }
      if (isst) *(LAS f2*)(L + L_ST + c.tid * 8) = (f2){bitsf(g1a.z), bitsf(g1a.x) * bitsf(g1a.z)};
      else if (c.tid < 128 + 208) *(LAS float*)(L + L_CS + (c.tid - 128) * 4) = (bitsf(g1a.x) + bitsf(g1a.z)) + (bitsf(g1b.x) + bitsf(g1b.z));
      wait_lgkm();
  }
  TS(3 * ST_E1 + 1);
  bar();
  TS(3 * ST_E1 + 2);
  FENCE();
  REFRESH();
  const unsigned gt0 = 8 * c.rg + 2 * c.m + c.nh, gt1 = 8 * c.rg + 2 * c.m + 1 - c.nh;
  {
    f4 cs4[2][2];
#pragma unroll
    for (int un = 0; un < 2; ++un)
#pragma unroll
      for (int i = 0; i < 2; ++i) cs4[un][i] = ldf4(L, L_CS + (64 * un + 32 * c.nh + 16 * i + 4 * g) * 4);
    const f2 st0 = ldf2(L, L_ST + (16 * (2 * c.m + c.nh) + l15) * 8), st1 = ldf2(L, L_ST + (16 * (2 * c.m + 1 - c.nh) + l15) * 8);
    FENCE();
#pragma unroll
    for (int un = 0; un < 2; ++un)
#pragma unroll
      for (int i = 0; i < 2; ++i) {
        acc[un][i][0] = mfma32(wfb[un][i], xf[0][15], acc[un][i][0]);
        acc[un][i][1] = mfma32(wfb[un][i], xf[1][15], acc[un][i][1]);
      }
    FENCE();
    TSX(0);
    if (c.isQ) {
      v2u bb[2][2];
#pragma unroll
      for (int un = 0; un < 2; ++un)
#pragma unroll
        for (int i = 0; i < 2; ++i) bb[un][i] = ld64(L, L_B1 + un * 128 + c.nh * 64 + i * 32 + 8 * g);
      const unsigned ks0 = 2 * unit_pos(c.q, 2 + 2 * (c.jj & 1)) + c.nh;
      const unsigned ho0 = WS_H + (gt0 * 72 + ks0) * 1024 + lane * 16, ho1 = WS_H + (gt1 * 72 + ks0) * 1024 + lane * 16;
      PRIO(1);
      {
        __builtin_amdgcn_raw_buffer_store_b128(ffn_pair(acc[0][0][0], acc[0][1][0], cs4[0][0], cs4[0][1], bb[0][0], bb[0][1], st0), c.rs, ho0, 0, AUX_SC1);
      }
      FENCE(); PRIO(0); issue_op<ST_E1, 0>(c); FENCE();
      {
        __builtin_amdgcn_raw_buffer_store_b128(ffn_pair(acc[0][0][1], acc[0][1][1], cs4[0][0], cs4[0][1], bb[0][0], bb[0][1], st1), c.rs, ho1, 0, AUX_SC1);
      }
      FENCE(); PRIO(1); issue_op<ST_E1, 1>(c); FENCE();
      {
        __builtin_amdgcn_raw_buffer_store_b128(ffn_pair(acc[1][0][0], acc[1][1][0], cs4[1][0], cs4[1][1], bb[1][0], bb[1][1], st0), c.rs, ho0 + 2048, 0, AUX_SC1);
      }
      FENCE(); PRIO(0);
      {
        __builtin_amdgcn_raw_buffer_store_b128(ffn_pair(acc[1][0][1], acc[1][1][1], cs4[1][0], cs4[1][1], bb[1][0], bb[1][1], st1), c.rs, ho1 + 2048, 0, AUX_SC1);
      }
    } else {
      const v4u ones = {0x3f803f80u, 0x3f803f80u, 0x3f803f80u, 0x3f803f80u};
      const v2u wt0 = trd(L, L_WPHI + (2 * c.nh) * 512 + (4 * g + q4) * 32 + p4 * 8), wt1 = trd(L, L_WPHI + (2 * c.nh + 1) * 512 + (4 * g + q4) * 32 + p4 * 8);
      f4 pk4[4];
      v2u vb[4], fp[4], fn[4];
      PRIO(1);
#pragma unroll
      for (int i = 0; i < 2; ++i)
#pragma unroll
        for (int tt = 0; tt < 2; ++tt) pk4[2 * i + tt] = mfma16(i ? wt1 : wt0, ln_pack(acc[0][i][tt], cs4[0][i], tt ? st1 : st0), z4);
#pragma unroll
      for (int i = 0; i < 2; ++i)
#pragma unroll
        for (int tt = 0; tt < 2; ++tt) vb[2 * i + tt] = ln_pack(acc[1][i][tt], cs4[1][i], tt ? st1 : st0);
      TSX(1);
      FENCE(); PRIO(0); issue_op<ST_E1, 0>(c); FENCE();
      Feat ft;
      feat_max(pk4, ft);
      TSX(2);
      FENCE(); PRIO(1); issue_op<ST_E1, 1>(c); FENCE();
      feat_exp(pk4, ft);
      TSX(3);
      FENCE(); PRIO(0); FENCE();
      feat_norm(ft, fp, fn);
      FENCE();
      TSX(4);
      constexpr unsigned IMG_P = L_SCR, IMG_N = L_SCR + 16384, IMG_V = L_RING + SC.slot[15] * 16384;
      const unsigned wo_ = (2 * c.nh) * 4096 + (32 * c.m + l15) * 32 + ((g ^ q4) * 8);
#pragma unroll
      for (int i = 0; i < 2; ++i)
#pragma unroll
        for (int tt = 0; tt < 2; ++tt) {
          *(LAS v2u*)(L + IMG_P + wo_ + i * 4096 + tt * 512) = fp[2 * i + tt];
          *(LAS v2u*)(L + IMG_N + wo_ + i * 4096 + tt * 512) = fn[2 * i + tt];
          *(LAS v2u*)(L + IMG_V + wo_ + i * 4096 + tt * 512) = vb[2 * i + tt];
        }
      wait_lgkm();
      TSX(5);
      bar();
      TSX(6);
      const unsigned ro = (c.w >> 1) * 4096 + (4 * g + q4) * 32 + ((p4 ^ g) * 8);
      const unsigned ra = IMG_P + (c.w & 1) * 16384 + ro, rb = IMG_V + ro;
      v4u A[4], B[4];
#pragma unroll
      for (int j = 0; j < 4; ++j) { A[j] = trd2(L, ra + j * 1024, ra + j * 1024 + 512); B[j] = trd2(L, rb + j * 1024, rb + j * 1024 + 512); }
      f4 kv0 = mfma32(A[0], B[0], z4), kv1 = mfma32(A[1], B[1], z4), z0 = mfma32(A[0], ones, z4), z1 = mfma32(A[1], ones, z4);
      kv0 = mfma32(A[2], B[2], kv0); kv1 = mfma32(A[3], B[3], kv1); z0 = mfma32(A[2], ones, z0); z1 = mfma32(A[3], ones, z1);
      kv0 += kv1; z0 += z1;
      TSX(7);
      const unsigned pbase = WS_P + (c.hq * 16 + c.rg) * PART_BYTES;
      __builtin_amdgcn_raw_buffer_store_b128((v4u){fbits(kv0[0]), c.tag, fbits(kv0[1]), c.tag}, c.rs, pbase + (c.w * 128 + lane) * 16, 0, AUX_SC1);
      __builtin_amdgcn_raw_buffer_store_b128((v4u){fbits(kv0[2]), c.tag, fbits(kv0[3]), c.tag}, c.rs, pbase + (c.w * 128 + 64 + lane) * 16, 0, AUX_SC1);
      if (l15 == 0) {
        __builtin_amdgcn_raw_buffer_store_b128((v4u){fbits(z0[0]), c.tag, fbits(z0[1]), c.tag}, c.rs, pbase + (1024 + c.w * 8 + g * 2) * 16, 0, AUX_SC1);
        __builtin_amdgcn_raw_buffer_store_b128((v4u){fbits(z0[2]), c.tag, fbits(z0[3]), c.tag}, c.rs, pbase + (1024 + c.w * 8 + g * 2 + 1) * 16, 0, AUX_SC1);
      }
    }
  }
  PRIO_OFF();
  TSX(8);
  STAMP(c, 4);
  FENCE();

  f4 acc2[2][2], accS = z4;
#pragma unroll
  for (int b = 0; b < 2; ++b)
#pragma unroll
    for (int t = 0; t < 2; ++t) acc2[b][t] = z4;
  REFRESH();
  const unsigned wb0 = (unsigned)((8 * g + q4) * 128 + (((4 * c.nh + (p4 >> 1)) ^ ((((q4 >> 1) & 1) | ((g & 1) << 1)) << 1)) * 16) + (p4 & 1) * 8);
  const unsigned sa0 = (unsigned)((8 * g + ((g & 1) << 2) + q4) * 32 + p4 * 8);
  constexpr unsigned SS = L_RING + SC.slot[IT_S] * 16384;
  unsigned wbb[2][2] = {{wb0, wb0 ^ 32}, {}};
#pragma unroll
  for (int i = 0; i < 2; ++i) { wbb[1][i] = wbb[0][i] + HIWIN; asm volatile("" : "+v"(wbb[1][i])); }
  unsigned s0 = sa0 + (SS >= HIWIN ? HIWIN : 0), s1 = (sa0 ^ 128) + (SS >= HIWIN ? HIWIN : 0);
  asm volatile("" : "+v"(s0), "+v"(s1));
  unsigned pollv;
  v4u rk0, rk1, rz0, rz1;
  const char* flagA = ws + WS_FLAG + (0 * 16 + (c.rt >> 1)) * 128;
  const char* flagB = ws + WS_FLAG + (1 * 16 + (c.rt >> 1)) * 128;
  const char* flagO = ws + WS_FLAG + (2 * 16 + (c.rt >> 1)) * 128;
  const char* rsrc = ws + WS_P + (c.hq * 16 + 2 * c.w) * PART_BYTES;
  v4u pwa[2], psa, pwb[2], psb;

  auto pass2_step = [&](auto KC) {
    constexpr int kc = decltype(KC)::value, st = 9 + kc;
    constexpr unsigned sp = L_RING + SC.slot[IT_P0 + kc] * 16384, ss = L_RING + SC.slot[IT_S] * 16384;
    TS(3 * st);
    if constexpr (kc > 0) { asm volatile("" : "+v"(pwb[0]), "+v"(pwb[1]), "+v"(psb)); wait_vm<top_wait(st)>(); }
    else wait_top<top_wait(st)>();
    TS(3 * st + 1);
    bar();
    TS(3 * st + 2);
    FENCE();
    constexpr int hp = sp >= HIWIN;
    const unsigned w0 = wbb[hp][0], w1_ = wbb[hp][1];
    constexpr unsigned rp = sp - hp * HIWIN, rs_ = ss - (ss >= HIWIN ? HIWIN : 0);
#define P2_MMA(A, F, T, KM) do { if constexpr (DOM_) A = mfma32(F, xf[T][KM], A); FENCE(); } while (0)
#define P2_Q(FR, SR, KR, FM, SM, KM, DOM, TIE) {                                                    \
    constexpr bool DOM_ = DOM;                                                                     \
    v2u t0, t1;                                                                                    \
    t0 = trd(L, w0 + rp + ((KR) & 3) * 4096); FENCE();                                             \
    P2_MMA(acc2[0][0], FM[0], 0, KM);                                                              \
    t1 = trd(L, w0 + rp + ((KR) & 3) * 4096 + 512); FR[0] = (v4u){t0.x, t0.y, t1.x, t1.y}; FENCE(); \
    P2_MMA(acc2[0][1], FM[0], 1, KM);                                                              \
    FR[1] = trd2(L, w1_ + rp + ((KR) & 3) * 4096, w1_ + rp + ((KR) & 3) * 4096 + 512); FENCE();    \
    P2_MMA(acc2[1][0], FM[1], 0, KM);                                                              \
    t0 = trd(L, s0 + rs_ + (KR) * 1024); FENCE();                                                  \
    P2_MMA(acc2[1][1], FM[1], 1, KM);                                                              \
    t1 = trd(L, s1 + rs_ + (KR) * 1024); SR = (v4u){t0.x, t0.y, t1.x, t1.y}; FENCE();              \
    P2_MMA(accS, SM, 0, KM);                                                                       \
  }
    if constexpr (st == ST_POLLA_ISSUE) { REFRESH(); gld4_sc1(pollv, flagA, (lane & 31) * 4); }
    if constexpr (st == ST_R_ISSUE) {
      if (c.isQ) {
        REFRESH();
        const unsigned vk = (64 * c.rg + lane) * 16, vz = (1024 + 4 * c.rg + (lane & 3)) * 16;
        gld16_sc1(rk0, rsrc, vk);
        gld16_sc1(rk1, rsrc + PART_BYTES, vk);
        gld16_sc1(rz0, rsrc, vz);
        gld16_sc1(rz1, rsrc + PART_BYTES, vz);
      }
    }
    FENCE();
    op_m0<st, 0>(c);
    P2_Q(pwa, psa, 4 * kc, pwb, psb, (kc > 0 ? 4 * kc - 1 : 0), (kc > 0), false)
    op_go<st, 0>(c);
    if constexpr (st == ST_PUBA) {
      if (c.isQ && threadIdx.x == 0) __builtin_amdgcn_raw_buffer_store_b32(c.tag, c.rs, WS_FLAG + (0 * 16 + c.rg) * 128 + c.hq * 4, 0, AUX_SC1);
    }
    FENCE();
    op_m0<st, 1>(c);
    P2_Q(pwb, psb, 4 * kc + 1, pwa, psa, 4 * kc, true, true)
    op_go<st, 1>(c);
    FENCE();
    P2_Q(pwa, psa, 4 * kc + 2, pwb, psb, 4 * kc + 1, true, true)

    FENCE();
    P2_Q(pwb, psb, 4 * kc + 3, pwa, psa, 4 * kc + 2, true, true)
  };
  pass2_step(IC<0>{});
  pass2_step(IC<1>{});
  pass2_step(IC<2>{});
  pass2_step(IC<3>{});
  STAMP(c, 5);

  v4u hf[18][2];
  v4u rg0, rg1, rgz0, rgz1;
  TS(3 * ST_E2);
  wait_top<top_wait(ST_E2)>();
  asm volatile("s_waitcnt vmcnt(%1)" : "+v"(pollv) : "n"(extra_wait_first(ST_POLLA_ISSUE, ST_E2)) : "memory");
  REFRESH();
  if (c.w == 0) {
    if (__builtin_expect(!__all(pollv == c.tag || (lane & 31) >= 8), 0)) spin_flags(c, flagA, 8);
  }
  if (c.isQ) {
    asm volatile("s_waitcnt vmcnt(%4)" : "+v"(rk0), "+v"(rk1), "+v"(rz0), "+v"(rz1) : "n"(extra_wait_first(ST_R_ISSUE, ST_E2)) : "memory");
    if (__builtin_expect(!__all(tags_ok(rk0, c.tag) && tags_ok(rk1, c.tag) && tags_ok(rz0, c.tag) && tags_ok(rz1, c.tag)), 0)) {
      const unsigned vk = (64 * c.rg + lane) * 16, vz = (1024 + 4 * c.rg + (lane & 3)) * 16;
#pragma unroll 1
      for (unsigned it = 0; it < SPIN_CAP; ++it) {
        __builtin_amdgcn_s_sleep(2);
        gld16_sc1(rk0, rsrc, vk);
        gld16_sc1(rk1, rsrc + PART_BYTES, vk);
        gld16_sc1(rz0, rsrc, vz);
        gld16_sc1(rz1, rsrc + PART_BYTES, vz);
        asm volatile("s_waitcnt vmcnt(0)" : "+v"(rk0), "+v"(rk1), "+v"(rz0), "+v"(rz1)::"memory");
        if (__all(tags_ok(rk0, c.tag) && tags_ok(rk1, c.tag) && tags_ok(rz0, c.tag) && tags_ok(rz1, c.tag))) break;
      }
    }
    *(LAS f2*)(L + L_SCR + c.w * 1024 + lane * 8) = (f2){bitsf(rk0.x) + bitsf(rk1.x), bitsf(rk0.z) + bitsf(rk1.z)};
    if (lane < 4) *(LAS f2*)(L + L_SCR + c.w * 1024 + 512 + lane * 8) = (f2){bitsf(rz0.x) + bitsf(rz1.x), bitsf(rz0.z) + bitsf(rz1.z)};
    wait_lgkm();
  }
  TS(3 * ST_E2 + 1);
  bar();
  TS(3 * ST_E2 + 2);
  FENCE();
  c.pH = ws + WS_H + (size_t)((4 * c.rt + 2 * (c.w & 1)) * 72 + (c.w >> 1)) * 1024;
  c.vH = lane * 16;
  c.vH1 = lane * 16 + 72 * 1024;
  f2 st0, st1;
  f4 cs2[2];
  f4 pq4[4];
  Feat ftq;
  v2u fp[4], fn[4];
  {
#pragma unroll
    for (int i = 0; i < 2; ++i) {
      acc2[i][0] = mfma32(pwb[i], xf[0][15], acc2[i][0]);
      acc2[i][1] = mfma32(pwb[i], xf[1][15], acc2[i][1]);
    }
    accS = mfma32(psb, xf[0][15], accS);
    st0 = ldf2(L, L_ST + (16 * (2 * c.m + c.nh) + l15) * 8); st1 = ldf2(L, L_ST + (16 * (2 * c.m + 1 - c.nh) + l15) * 8);
    cs2[0] = ldf4(L, L_CS + (128 + 32 * c.nh + 4 * g) * 4); cs2[1] = ldf4(L, L_CS + (128 + 32 * c.nh + 16 + 4 * g) * 4);
    const f4 csS = ldf4(L, L_CS + (192 + 4 * g) * 4);
    const v2u bS = ld64(L, L_B1 + 256 + 8 * g);
    FENCE();
    issue_op<ST_E2, 0>(c);
    issue_h<0>(c, hf);
    FENCE();
    TSX(9);
    PRIO(1);
    const v2u bb0 = ld64(L, L_B1 + c.nh * 64 + 8 * g), bb1 = ld64(L, L_B1 + c.nh * 64 + 32 + 8 * g);
    const unsigned ksu = 2 * unit_pos(c.q, c.jj) + c.nh;
    if (c.isQ) {
      if (c.w == 0) {
        f2 s = {0.f, 0.f}, sz = {0.f, 0.f};
#pragma unroll
        for (int ww = 0; ww < 8; ++ww) { s += ldf2(L, L_SCR + ww * 1024 + lane * 8); sz += ldf2(L, L_SCR + ww * 1024 + 512 + (lane & 3) * 8); }
        const unsigned rb = WS_R + c.hq * PART_BYTES;
        __builtin_amdgcn_raw_buffer_store_b128((v4u){fbits(s.x), c.tag, fbits(s.y), c.tag}, c.rs, rb + (64 * c.rg + lane) * 16, 0, AUX_SC1);
        if (lane < 4) __builtin_amdgcn_raw_buffer_store_b128((v4u){fbits(sz.x), c.tag, fbits(sz.y), c.tag}, c.rs, rb + (1024 + 4 * c.rg + lane) * 16, 0, AUX_SC1);
      }
      const v2u wt0 = trd(L, L_WPHI + (2 * c.nh) * 512 + (4 * g + q4) * 32 + p4 * 8), wt1 = trd(L, L_WPHI + (2 * c.nh + 1) * 512 + (4 * g + q4) * 32 + p4 * 8);
#pragma unroll
      for (int i = 0; i < 2; ++i)
#pragma unroll
        for (int tt = 0; tt < 2; ++tt) pq4[2 * i + tt] = mfma16(i ? wt1 : wt0, ln_pack(acc2[i][tt], cs2[i], tt ? st1 : st0), z4);
      feat_max(pq4, ftq);
    } else {
      __builtin_amdgcn_raw_buffer_store_b128(ffn_pair(acc2[0][0], acc2[1][0], cs2[0], cs2[1], bb0, bb1, st0), c.rs, WS_H + (gt0 * 72 + ksu) * 1024 + lane * 16, 0, AUX_SC1);
    }
    FENCE();
    PRIO(0);
    issue_op<ST_E2, 1>(c);
    issue_h<1>(c, hf);
    FENCE();
    if (c.isQ) feat_exp(pq4, ftq);
    else __builtin_amdgcn_raw_buffer_store_b128(ffn_pair(acc2[0][1], acc2[1][1], cs2[0], cs2[1], bb0, bb1, st1), c.rs, WS_H + (gt1 * 72 + ksu) * 1024 + lane * 16, 0, AUX_SC1);
    FENCE();
    TSX(10);
    PRIO(1);
    issue_h<2>(c, hf);
    FENCE();
    {
      const v2u o = ffn_tile(accS, csS, bS, st0);
      const unsigned ks = 2 * unit_pos(c.q, 6) + (c.jj >> 1);
      __builtin_amdgcn_raw_buffer_store_b64(o, c.rs, WS_H + (gt0 * 72 + ks) * 1024 + lane * 16 + (c.jj & 1) * 8, 0, AUX_SC1);
    }
    FENCE();
    TSX(11);
    PRIO(0);
    issue_op<ST_E2, 2>(c);
    issue_h<3>(c, hf);
    issue_op<ST_E2, 3>(c);
    if constexpr (HPRE > 4) issue_h<4>(c, hf);
    FENCE();
    if (c.isQ) feat_norm(ftq, fp, fn);
    FENCE();
    TSX(12);
    if constexpr (HPRE > 5) issue_h<5>(c, hf);
    if constexpr (HPRE > 6) issue_h<6>(c, hf);
    if constexpr (HPRE > 7) issue_h<7>(c, hf);
    if (c.isQ) {
      const char* rres_ = ws + WS_R + c.hq * PART_BYTES;
      const unsigned vk = (c.tid >> 6) * 2048 + (c.tid & 63) * 16, vz = (c.tid & 31) * 32;
      gld16_sc1(rg0, rres_, vk);
      gld16_sc1(rg1, rres_ + 1024, vk);
      gld16_sc1(rgz0, rres_ + 16384, vz);
      gld16_sc1(rgz1, rres_ + 16384 + 16, vz);
    }
    PRIO_OFF();
    TSX(13);
    static_assert(HPRE >= 4 && HPRE <= 8 && ST_RG_ISSUE == ST_E2, "initial h loads");
  }
  STAMP(c, 6);
  FENCE();

  f4 accF[2][4];
#pragma unroll
  for (int j = 0; j < 2; ++j)
#pragma unroll
    for (int t = 0; t < 4; ++t) accF[j][t] = z4;
  REFRESH();
  const unsigned fa0 = (unsigned)((32 * (c.w >> 1) + 4 * g + q4) * 128 + (((p4 >> 1) ^ ((((g & 1) << 1) | (q4 >> 1)) << 1)) * 16) + (p4 & 1) * 8);
  unsigned fb[2][4] = {{fa0, fa0 ^ 32, fa0 ^ 64, fa0 ^ 96}, {}};
#pragma unroll
  for (int i = 0; i < 4; ++i) { fb[1][i] = fb[0][i] + HIWIN; asm volatile("" : "+v"(fb[1][i])); }
  v4u xres, fw[2][4];
  const char* rres = ws + WS_R + c.hq * PART_BYTES;

  auto phase2_step = [&](auto CC) {
    constexpr int ch = decltype(CC)::value, st = ST_F0 + ch;
    constexpr unsigned sf = L_RING + SC.slot[item_of_F(ch)] * 16384;
    TS(3 * st);
    constexpr int pc = ch > 0 ? ch - 1 : 0;
    if constexpr (ch > 0) {
      asm volatile("" : "+v"(fw[pc & 1][0]), "+v"(fw[pc & 1][1]), "+v"(fw[pc & 1][2]), "+v"(fw[pc & 1][3]));
      asm volatile("s_waitcnt vmcnt(%2)" : "+v"(hf[pc][0]), "+v"(hf[pc][1]) : "n"(top_wait(st)) : "memory");
    } else {
      wait_top<top_wait(st)>();
    }
    if constexpr (st == ST_POLLB_CHECK) {
      asm volatile("s_waitcnt vmcnt(%1)" : "+v"(pollv) : "n"(extra_wait_first(ST_POLLB_ISSUE, st)) : "memory");
      if (c.w == 0) { if (__builtin_expect(!__all(pollv == c.tag || (lane & 31) >= 16), 0)) spin_flags(c, flagB, 16); }
    }
    if constexpr (st == ST_POLLO_CHECK) {
      asm volatile("s_waitcnt vmcnt(%1)" : "+v"(pollv) : "n"(extra_wait_first(ST_POLLO_ISSUE, st)) : "memory");
      if (c.w == 0) { if (__builtin_expect(!__all(pollv == c.tag || (lane & 31) >= 8), 0)) spin_flags(c, flagO, 8); }
    }
    if constexpr (st == ST_RG_CHECK) {
      if (c.isQ) {
        asm volatile("s_waitcnt vmcnt(%4)" : "+v"(rg0), "+v"(rg1), "+v"(rgz0), "+v"(rgz1) : "n"(extra_wait(ST_RG_ISSUE, st)) : "memory");
        const unsigned t = c.tid;
        if (__builtin_expect(!__all(tags_ok(rg0, c.tag) && tags_ok(rg1, c.tag) && tags_ok(rgz0, c.tag) && tags_ok(rgz1, c.tag)), 0)) {
#pragma unroll 1
          for (unsigned it = 0; it < SPIN_CAP; ++it) {
            __builtin_amdgcn_s_sleep(2);
            gld16_sc1(rg0, rres, (t >> 6) * 2048 + (t & 63) * 16);
            gld16_sc1(rg1, rres + 1024, (t >> 6) * 2048 + (t & 63) * 16);
            gld16_sc1(rgz0, rres + 16384, (t & 31) * 32);
            gld16_sc1(rgz1, rres + 16384 + 16, (t & 31) * 32);
            asm volatile("s_waitcnt vmcnt(0)" : "+v"(rg0), "+v"(rg1), "+v"(rgz0), "+v"(rgz1)::"memory");
            if (__all(tags_ok(rg0, c.tag) && tags_ok(rg1, c.tag) && tags_ok(rgz0, c.tag) && tags_ok(rgz1, c.tag))) break;
          }
        }
        *(LAS v2u*)(L + L_SCR + (t >> 7) * 1024 + (t & 63) * 16 + ((t >> 6) & 1) * 8) =
            (v2u){pk(bitsf(rg0.x), bitsf(rg0.z)), pk(bitsf(rg1.x), bitsf(rg1.z))};
        if (t < 32)
          *(LAS v2u*)(L + L_SCR + 4096 + (t >> 3) * 64 + (t & 3) * 16 + ((t >> 2) & 1) * 8) =
              (v2u){pk(bitsf(rgz0.x), bitsf(rgz0.z)), pk(bitsf(rgz1.x), bitsf(rgz1.z))};
        wait_lgkm();
      }
    }
    TS(3 * st + 1);
    bar();
    TS(3 * st + 2);
    FENCE();
    constexpr int hi = sf >= HIWIN;
    const unsigned f0 = fb[hi][0], f1 = fb[hi][1], f2_ = fb[hi][2], f3 = fb[hi][3];
    constexpr unsigned rf = sf - hi * HIWIN;
    {
      constexpr int hc = h_at(st, 0), hd = h_at(st, 1);
      static_assert(h_at(st, 2) < 0, "at most two h chunks per step");
#define F_MMA(J, T) do { if constexpr (ch > 0) accF[J][T] = mfma32(fw[pc & 1][T], hf[pc][J], accF[J][T]); FENCE(); } while (0)
      v2u t0, t1;
      t0 = trd(L, f0 + rf); FENCE();
      if constexpr (st == ST_POLLB_ISSUE) gld4_sc1(pollv, flagB, (lane & 31) * 4);
      if constexpr (st == ST_POLLO_ISSUE) gld4_sc1(pollv, flagO, (lane & 31) * 4);
      op_m0<st, 0>(c); FENCE();
      F_MMA(0, 0);
      t1 = trd(L, f0 + rf + 2048); fw[ch & 1][0] = (v4u){t0.x, t0.y, t1.x, t1.y}; FENCE();
      if constexpr (st == ST_PUBB) {
        if (threadIdx.x == 0) __builtin_amdgcn_raw_buffer_store_b32(c.tag, c.rs, WS_FLAG + (1 * 16 + c.rg) * 128 + (4 * c.q + c.jj) * 4, 0, AUX_SC1);
      }
      if constexpr (st == ST_PUBO) {
        if (c.isQ && threadIdx.x == 0) __builtin_amdgcn_raw_buffer_store_b32(c.tag, c.rs, WS_FLAG + (2 * 16 + c.rg) * 128 + c.hq * 4, 0, AUX_SC1);
      }
      op_go<st, 0>(c); FENCE();
      F_MMA(1, 0);
      t0 = trd(L, f1 + rf); FENCE();
      op_m0<st, 1>(c); FENCE();
      F_MMA(0, 1);
      t1 = trd(L, f1 + rf + 2048); fw[ch & 1][1] = (v4u){t0.x, t0.y, t1.x, t1.y}; FENCE();
      op_go<st, 1>(c); FENCE();
      F_MMA(1, 1);
      t0 = trd(L, f2_ + rf); FENCE();
      F_MMA(0, 2);
      t1 = trd(L, f2_ + rf + 2048); fw[ch & 1][2] = (v4u){t0.x, t0.y, t1.x, t1.y}; FENCE();
      if constexpr (hc >= 0) { issue_h0<(hc >= 0 ? hc : 0)>(c, hf); FENCE(); }
      F_MMA(1, 2);
      t0 = trd(L, f3 + rf); FENCE();
      F_MMA(0, 3);
      t1 = trd(L, f3 + rf + 2048); fw[ch & 1][3] = (v4u){t0.x, t0.y, t1.x, t1.y}; FENCE();
      if constexpr (hc >= 0) { issue_h1<(hc >= 0 ? hc : 0)>(c, hf); FENCE(); }
      if constexpr (hd >= 0) { issue_h<(hd >= 0 ? hd : 0)>(c, hf); FENCE(); }
      if constexpr (ch == 14) gld16(xres, x + (size_t)(64 * c.rt) * 1024 + c.ct * 128, (c.tid >> 3) * 1024 + (c.tid & 7) * 16);
      FENCE();
      F_MMA(1, 3);
    }
    if constexpr (st == ST_O) {
      if (c.isQ) {
        v2u ob[2][2];
#pragma unroll
        for (int i = 0; i < 2; ++i) {
          const int hl = 2 * c.nh + i;
          const v4u Akv = ld128(L, L_SCR + hl * 1024 + lane * 16), Az = ld128(L, L_SCR + 4096 + hl * 64 + g * 16);
#pragma unroll
          for (int tt = 0; tt < 2; ++tt) {
            const v4u ph = {fp[2 * i + tt].x, fp[2 * i + tt].y, fn[2 * i + tt].x, fn[2 * i + tt].y};
            const f4 num = mfma32(Akv, ph, z4), den = mfma32(Az, ph, z4);
            const float inv = __builtin_amdgcn_rcpf(den[0] + 1e-6f);
            ob[i][tt] = (v2u){pk(num[0] * inv, num[1] * inv), pk(num[2] * inv, num[3] * inv)};
          }
        }
        const unsigned ks = 56 + 2 * c.hq + c.nh;
        __builtin_amdgcn_raw_buffer_store_b128((v4u){ob[0][0].x, ob[0][0].y, ob[1][0].x, ob[1][0].y}, c.rs, WS_H + (gt0 * 72 + ks) * 1024 + lane * 16, 0, AUX_SC1);
        __builtin_amdgcn_raw_buffer_store_b128((v4u){ob[0][1].x, ob[0][1].y, ob[1][1].x, ob[1][1].y}, c.rs, WS_H + (gt1 * 72 + ks) * 1024 + lane * 16, 0, AUX_SC1);
      }
    }
    FENCE();
  };
  phase2_step(IC<0>{});
  phase2_step(IC<1>{});
  phase2_step(IC<2>{});
  phase2_step(IC<3>{});
  phase2_step(IC<4>{});
  phase2_step(IC<5>{});
  STAMP(c, 7);
  phase2_step(IC<6>{});
  phase2_step(IC<7>{});
  phase2_step(IC<8>{});
  phase2_step(IC<9>{});
  phase2_step(IC<10>{});
  phase2_step(IC<11>{});
  STAMP(c, 8);
  phase2_step(IC<12>{});
  phase2_step(IC<13>{});
  phase2_step(IC<14>{});
  phase2_step(IC<15>{});
  phase2_step(IC<16>{});
  phase2_step(IC<17>{});
  STAMP(c, 9);

  TS(3 * NSTEP);
  asm volatile("" : "+v"(fw[1][0]), "+v"(fw[1][1]), "+v"(fw[1][2]), "+v"(fw[1][3]));
  asm volatile("s_waitcnt vmcnt(0) lgkmcnt(0)" : "+v"(xres), "+v"(hf[17][0]), "+v"(hf[17][1])::"memory");
  TS(3 * NSTEP + 1);
#pragma unroll
  for (int t = 0; t < 4; ++t) { accF[0][t] = mfma32(fw[1][t], hf[17][0], accF[0][t]); accF[1][t] = mfma32(fw[1][t], hf[17][1], accF[1][t]); }
  REFRESH();
  constexpr unsigned FIN = L_RING + 4 * 16384;
  static_assert(SC.slot[NITEM - 1] < 4 && FIN + 4 * 17408 <= L_CS, "final partial sums");
#pragma unroll
  for (int j = 0; j < 2; ++j)
#pragma unroll
    for (int t = 0; t < 4; ++t) *(LAS f4*)(L + FIN + (c.w >> 1) * 17408 + (16 * (2 * (c.w & 1) + j) + l15) * 272 + (16 * t + 4 * g) * 4) = accF[j][t];
  wait_lgkm();
  bar();
  {
    const int tok = c.tid >> 3, cc = c.tid & 7;
    const unsigned a = FIN + tok * 272 + cc * 32;
    f4 s0 = (ldf4(L, a) + ldf4(L, a + 17408)) + (ldf4(L, a + 2 * 17408) + ldf4(L, a + 3 * 17408));
    f4 s1 = (ldf4(L, a + 16) + ldf4(L, a + 16 + 17408)) + (ldf4(L, a + 16 + 2 * 17408) + ldf4(L, a + 16 + 3 * 17408));
    const v4u bb = ld128(L, L_B2 + cc * 16);
    v4u o;
    o.x = pk(s0[0] + bflo(xres.x) + bflo(bb.x), s0[1] + bfhi(xres.x) + bfhi(bb.x));
    o.y = pk(s0[2] + bflo(xres.y) + bflo(bb.y), s0[3] + bfhi(xres.y) + bfhi(bb.y));
    o.z = pk(s1[0] + bflo(xres.z) + bflo(bb.z), s1[1] + bfhi(xres.z) + bfhi(bb.z));
    o.w = pk(s1[2] + bflo(xres.w) + bflo(bb.w), s1[3] + bfhi(xres.w) + bfhi(bb.w));
    { const __amdgpu_buffer_rsrc_t ro = __builtin_amdgcn_make_buffer_rsrc((void*)out, 0, 2048 * 1024, 0x00020000);
      __builtin_amdgcn_raw_buffer_store_b128(o, ro, (64 * c.rt + tok) * 1024 + c.ct * 128 + cc * 16, 0, 2);
    }
  }
  asm volatile(".Lhh_code_end:");
  if (blk == 0 && c.tid == 0) *(unsigned*)(ws + WS_EPOCH) = c.tag;
  STAMP(c, 10);
}

}

extern "C" void kernel_launch(void* const* d_in, const int* in_sizes, int n_in, void* d_out, int out_size, void* d_ws, size_t ws_size,
                              hipStream_t stream) {
  (void)in_sizes; (void)n_in; (void)out_size;
  const size_t WS_OFFSET = 0; (void)ws_size;
  (void)hipFuncSetAttribute((const void*)hh::fused_kernel, hipFuncAttributeMaxDynamicSharedMemorySize, hh::L_END);
  hh::fused_kernel<<<256, 512, hh::L_END, stream>>>((const char*)d_in[0], (const char*)d_in[1], (const char*)d_in[2], (const char*)d_in[3],
                                                    (const char*)d_in[5], (const char*)d_in[6], (const char*)d_in[7], (const char*)d_in[8],
                                                    (unsigned short*)d_out, (char*)d_ws + WS_OFFSET);
}
```
